```python
import torch

def _softmax(x, axis=-1):
    return torch.softmax(x, dim=axis)
def _gelu(x):
    return torch.nn.functional.gelu(x, approximate="tanh")
def _silu(x):
    return torch.nn.functional.silu(x)
def _sigmoid(x):
    return torch.sigmoid(x)
def _layernorm(x, axis=-1, eps=1e-5):
    m = x.mean(dim=axis, keepdim=True)
    v = x.var(dim=axis, keepdim=True, unbiased=False)
    return (x - m) / torch.sqrt(v + eps)
def _scatter_add(tgt, idx, src, axis=0):
    return tgt.clone().index_add_(axis, idx, src)
def _segment_sum(src, seg, n):
    out = torch.zeros((n,) + src.shape[1:], dtype=src.dtype)
    return out.index_add_(0, seg, src)
def _pad(x, pw):
    flat = tuple(v for pair in reversed(pw) for v in pair)
    return torch.nn.functional.pad(x, flat)

kernel_name = 'recurrent_scan__ssm_conv_h3__glu_gated_geglu__prompt'

def setup_inputs():

    _w = (lambda a: (lambda t: t.to(torch.bfloat16) if t.is_floating_point() else t)(torch.as_tensor(a)))
    def _A_setup():
        B, S, D = 1, 2048, 512
        ks = 4
        return {
            "q": _w((lambda *s: torch.randn(*s, dtype=torch.bfloat16) * 0.05)(B, S, D)),
            "k": _w((lambda *s: torch.randn(*s, dtype=torch.bfloat16) * 0.8)(B, S, D)),
            "v": _w((lambda *s: torch.randn(*s, dtype=torch.bfloat16) * 0.8)(B, S, D)),
            "shift_k": _w((lambda *s: torch.randn(*s, dtype=torch.bfloat16) * 0.8)(D, ks) * 0.5),
            "ssm_k": _w((lambda *s: torch.randn(*s, dtype=torch.bfloat16) * 1.6)(D, S) * (1.0 / S ** 0.5)),
            "ssm_D": _w((lambda *s: torch.randn(*s, dtype=torch.bfloat16) * 0.8)(D)),
        }
    _A_in = _A_setup()

    def _B_setup():
        B, S, Dm, Df = 1, 2048, 512, 1376
        return {
            "x": _w((lambda *s: torch.randn(*s, dtype=torch.bfloat16) * 0.05)(B, S, Dm)),
            "w_gate": _w((lambda *s: torch.randn(*s, dtype=torch.bfloat16) * 0.2)(Dm, Df) * (Dm ** -0.5)),
            "w_up": _w((lambda *s: torch.randn(*s, dtype=torch.bfloat16) * 0.2)(Dm, Df) * (Dm ** -0.5)),
            "w_down": _w((lambda *s: torch.randn(*s, dtype=torch.bfloat16) * 0.2)(Df, Dm) * (Df ** -0.5)),
        }
    _B_in = _B_setup()

    return {
        "A_q": _A_in["q"],
        "A_k": _A_in["k"],
        "A_v": _A_in["v"],
        "A_shift_k": _A_in["shift_k"],
        "A_ssm_k": _A_in["ssm_k"],
        "A_ssm_D": _A_in["ssm_D"],
        "B_x": _B_in["x"],
        "B_w_gate": _B_in["w_gate"],
        "B_w_up": _B_in["w_up"],
        "B_w_down": _B_in["w_down"],
    }

def reference(A_q, A_k, A_v, A_shift_k, A_ssm_k, A_ssm_D, B_x, B_w_gate, B_w_up, B_w_down):
    def _A_ref(q, k, v, shift_k, ssm_k, ssm_D):
        B, S, D = q.shape
        ks = shift_k.shape[-1]
        k_pad = _pad(k, ((0, 0), (ks - 1, 0), (0, 0)))
        k_shift = torch.zeros_like(k)
        for t in range(ks):
            k_shift = k_shift + k_pad[:, t:t + S, :] * shift_k[:, ks - 1 - t][None, None, :]
        kv = k_shift * v
        L = 2 * S
        kv_pad = _pad(kv, ((0, 0), (0, S), (0, 0)))
        kf_pad = _pad(ssm_k, ((0, 0), (0, S)))
        kv_f = (lambda x, axes=(-2,-1): torch.fft.rfft2(x.float(), dim=axes))(kv_pad[:, None, :, :], axes=(1, 2))
        kf_f = (lambda x, axes=(-2,-1): torch.fft.rfft2(x.float(), dim=axes))(kf_pad[:, None, :], axes=(1, 2))
        kf_f = (lambda x, *d: x.permute(*d))(kf_f, 1, 2, 0)
        y_f = kv_f * kf_f[None, :, :, :]
        y = (lambda x, s, axes=(-2,-1): torch.fft.irfft2(x, s=s, dim=axes).to(torch.bfloat16))(y_f, (1, L), axes=(1, 2))[:, 0, :S, :]
        y = y + kv * ssm_D[None, None, :]
        return q * y

    def _B_ref(x, w_gate, w_up, w_down):
        g = torch.einsum("bsd,df->bsf", x, w_gate)
        u = torch.einsum("bsd,df->bsf", x, w_up)
        h = _gelu(g) * u
        return torch.einsum("bsf,fd->bsd", h, w_down)

    x = A_q
    xn = _layernorm(x, axis=-1)
    s = _A_ref(xn, A_k, A_v, A_shift_k, A_ssm_k, A_ssm_D)
    g = _silu(_B_ref(xn, B_w_gate, B_w_up, B_w_down))
    return x + s * g
```

```cpp
#include <hip/hip_runtime.h>
#include <hip/hip_bf16.h>
typedef unsigned short u16;
__device__ __forceinline__ float bf2f(u16 h){ return __uint_as_float(((unsigned)h)<<16); }
__device__ __forceinline__ u16 f2bf(float f){ unsigned u=__float_as_uint(f); u += 0x7FFF + ((u>>16)&1); return (u16)(u>>16); }
__device__ __forceinline__ float rbf(float f){ return bf2f(f2bf(f)); }
constexpr int S=2048, D=512, DF=1376;

__global__ void n_ln(const u16* x, u16* xn){
  int row=blockIdx.x; int lane=threadIdx.x;
  float v[8]; float s=0;
  for(int j=0;j<8;j++){ v[j]=bf2f(x[row*D+lane*8+j]); s+=v[j]; }
  for(int m=32;m>=1;m>>=1) s+=__shfl_xor(s,m);
  float mean=s/D; float q=0;
  for(int j=0;j<8;j++){ float d=v[j]-mean; q+=d*d; }
  for(int m=32;m>=1;m>>=1) q+=__shfl_xor(q,m);
  float var=rbf(q/D); float mb=rbf(mean);
  float sd=rbf(sqrtf(rbf(var+1e-5f)));
  for(int j=0;j<8;j++){ xn[row*D+lane*8+j]=f2bf(rbf(v[j]-mb)/sd); }
}
__global__ void n_kv(const u16* k,const u16* v,const u16* sk,u16* kv){
  int i=blockIdx.x*blockDim.x+threadIdx.x; int t=i/D, d=i%D;
  float acc=0;
  for(int tt=0;tt<4;tt++){ int src=t+tt-3; float kk= src>=0? bf2f(k[src*D+d]):0.f; acc=rbf(acc+rbf(kk*bf2f(sk[d*4+3-tt]))); }
  kv[i]=f2bf(acc*bf2f(v[i]));
}
__global__ void n_conv(const u16* kv,const u16* h,const u16* Dv,u16* z){
  int i=blockIdx.x*blockDim.x+threadIdx.x; int t=i/D, d=i%D;
  float acc=0;
  for(int j=0;j<=t;j++) acc+=bf2f(h[d*S+j])*bf2f(kv[(t-j)*D+d]);
  z[i]=f2bf(rbf(acc)+rbf(bf2f(kv[i])*bf2f(Dv[d])));
}
__device__ __forceinline__ float gelu_t(float x){ float u=0.7978845608028654f*(x+0.044715f*x*x*x); return 0.5f*x*(1.f+tanhf(u)); }
__global__ void n_g1(const u16* xn,const u16* wg,const u16* wu,u16* H){
  int i=blockIdx.x*blockDim.x+threadIdx.x; int t=i/DF, f=i%DF;
  float g=0,u=0;
  for(int k=0;k<D;k++){ float a=bf2f(xn[t*D+k]); g+=a*bf2f(wg[k*DF+f]); u+=a*bf2f(wu[k*DF+f]); }
  H[i]=f2bf(rbf(gelu_t(rbf(g)))*rbf(u));
}
__global__ void n_g2(const u16* H,const u16* wd,const u16* x,const u16* xn,const u16* z,u16* out){
  int i=blockIdx.x*blockDim.x+threadIdx.x; int t=i/D, d=i%D;
  float a=0;
  for(int k=0;k<DF;k++) a+=bf2f(H[t*DF+k])*bf2f(wd[k*D+d]);
  a=rbf(a);
  float g=rbf(a/(1.f+__expf(-a)));
  float s=rbf(bf2f(xn[i])*bf2f(z[i]));
  out[i]=f2bf(bf2f(x[i])+rbf(s*g));
}
extern "C" void kernel_launch(void* const* d_in, const int* in_sizes, int n_in,
                              void* d_out, int out_size, void* d_ws, size_t ws_size,
                              hipStream_t stream) {
  const u16* q=(const u16*)d_in[0]; const u16* k=(const u16*)d_in[1]; const u16* v=(const u16*)d_in[2];
  const u16* sk=(const u16*)d_in[3]; const u16* h=(const u16*)d_in[4]; const u16* Dv=(const u16*)d_in[5];
  const u16* wg=(const u16*)d_in[7]; const u16* wu=(const u16*)d_in[8]; const u16* wd=(const u16*)d_in[9];
  u16* ws=(u16*)d_ws; u16* xn=ws; u16* kv=ws+S*D; u16* z=ws+2*S*D; u16* H=ws+3*S*D;
  n_ln<<<S,64,0,stream>>>(q,xn);
  n_kv<<<S*D/256,256,0,stream>>>(k,v,sk,kv);
  n_conv<<<S*D/256,256,0,stream>>>(kv,h,Dv,z);
  n_g1<<<S*DF/256,256,0,stream>>>(xn,wg,wu,H);
  n_g2<<<S*D/256,256,0,stream>>>(H,wd,q,xn,z,(u16*)d_out);
}
```

```cpp
#include <hip/hip_runtime.h>
#include <hip/hip_bf16.h>

typedef unsigned short u16;
typedef unsigned long long u64;
typedef __attribute__((ext_vector_type(8))) short bf16x8;
typedef __attribute__((ext_vector_type(4))) short s16x4;
typedef __attribute__((ext_vector_type(2))) __bf16 bf16x2_t;
typedef __attribute__((ext_vector_type(4))) float f32x4;
typedef __attribute__((ext_vector_type(16))) float f32x16;
typedef __attribute__((ext_vector_type(4))) unsigned u32x4;
typedef __attribute__((ext_vector_type(2))) unsigned u32x2;
#define LAS __attribute__((address_space(3)))
#define GAS __attribute__((address_space(1)))
typedef __amdgpu_buffer_rsrc_t rsrc_t;

constexpr int S_ = 2048, D_ = 512, DF = 1376;
constexpr unsigned WS_GEN = 0, WS_FA = 4096, WS_FH = WS_FA + 256 * 128, WS_FZ = WS_FH + 256 * 128;
constexpr unsigned WS_ST = WS_FZ + 256 * 128;
constexpr unsigned WS_KVT = 1u << 20;
constexpr unsigned WS_ZT = WS_KVT + (2u << 20);
constexpr unsigned WS_H = WS_ZT + (2u << 20);
constexpr unsigned WS_BYTES = WS_H + 64 * 86 * 1024;
constexpr int SLOT = 40960, L_KA = 3 * SLOT, L_VA = L_KA + 9216;
constexpr int L_SYNC = 163840 - 32;
constexpr int L_CV = 114688, L_UR = L_CV + 33280, L_WD4 = 131072, L_ZT = 65536;
constexpr int LDS_BYTES = 163840;
constexpr unsigned OOB = 0x80000000u;
constexpr unsigned SPIN_MAX = 1u << 18;

#define MFMA32(a, b, c) __builtin_amdgcn_mfma_f32_32x32x16_bf16(a, b, c, 0, 0, 0)
#define MFMA16(a, b, c) __builtin_amdgcn_mfma_f32_16x16x32_bf16(a, b, c, 0, 0, 0)
#define WAIT_BAR(N) asm volatile("s_waitcnt vmcnt(" #N ") lgkmcnt(0)\n\ts_barrier" ::: "memory")
#define LBAR() asm volatile("s_waitcnt lgkmcnt(0)\n\ts_barrier" ::: "memory")
#define WAITV(N) asm volatile("s_waitcnt vmcnt(" #N ")" ::: "memory")
#define SB() __builtin_amdgcn_sched_barrier(0)

__device__ __forceinline__ rsrc_t mk_rsrc(const void* p, unsigned bytes) {
  return __builtin_amdgcn_make_buffer_rsrc((void*)p, (short)0, (int)bytes, 0x00020000);
}
__device__ __forceinline__ void dma16(rsrc_t rs, unsigned voff, unsigned soff, unsigned lds) {
  asm volatile("s_mov_b32 m0, %0\n\ts_nop 0\n\tbuffer_load_dwordx4 %1, %2, %3 offen lds" ::"s"(lds), "v"(voff), "s"(rs), "s"(soff) : "memory");
}
__device__ __forceinline__ u32x4 ld16(rsrc_t rs, unsigned voff) {
  u32x4 r; asm volatile("buffer_load_dwordx4 %0, %1, %2, 0 offen" : "=v"(r) : "v"(voff), "s"(rs) : "memory"); return r;
}
__device__ __forceinline__ u32x4 ld16_dev(rsrc_t rs, unsigned voff, unsigned soff) {
  u32x4 r; asm volatile("buffer_load_dwordx4 %0, %1, %2, %3 offen sc1" : "=v"(r) : "v"(voff), "s"(rs), "s"(soff) : "memory"); return r;
}
__device__ __forceinline__ u32x2 ld8(rsrc_t rs, unsigned voff) {
  u32x2 r; asm volatile("buffer_load_dwordx2 %0, %1, %2, 0 offen" : "=v"(r) : "v"(voff), "s"(rs) : "memory"); return r;
}
__device__ __forceinline__ u32x2 ld8_dev(rsrc_t rs, unsigned voff) {
  u32x2 r; asm volatile("buffer_load_dwordx2 %0, %1, %2, 0 offen sc1" : "=v"(r) : "v"(voff), "s"(rs) : "memory"); return r;
}
#define PIN(x) asm volatile("" : "+v"(x))
__device__ __forceinline__ void st16_dev(rsrc_t rs, unsigned voff, u32x4 v) { __builtin_amdgcn_raw_buffer_store_b128(v, rs, voff, 0, 16); }
__device__ __forceinline__ void st8_dev(rsrc_t rs, unsigned voff, u32x2 v) { __builtin_amdgcn_raw_buffer_store_b64(v, rs, voff, 0, 16); }

__device__ __forceinline__ unsigned cvtpk(float lo, float hi) {
  bf16x2_t r = {(__bf16)lo, (__bf16)hi};
  return __builtin_bit_cast(unsigned, r);
}
__device__ __forceinline__ float blo(unsigned w) { return __uint_as_float(w << 16); }
__device__ __forceinline__ float bhi(unsigned w) { return __uint_as_float(w & 0xffff0000u); }
__device__ __forceinline__ float rbf(float f) { return blo(cvtpk(f, f)); }
__device__ __forceinline__ s16x4 trd(unsigned a) { return __builtin_amdgcn_ds_read_tr16_b64_v4i16((LAS s16x4*)(size_t)a); }
__device__ __forceinline__ bf16x8 cat(s16x4 a, s16x4 b) { return __builtin_shufflevector(a, b, 0, 1, 2, 3, 4, 5, 6, 7); }
template <class T> __device__ __forceinline__ T lds_ld(unsigned a) { return *(const LAS T*)(size_t)a; }
template <class T> __device__ __forceinline__ void lds_st(unsigned a, T v) { *(LAS T*)(size_t)a = v; }
template <int CTRL> __device__ __forceinline__ float dpp_add(float x) {
  return x + __uint_as_float(__builtin_amdgcn_update_dpp(0u, __float_as_uint(x), CTRL, 0xf, 0xf, true));
}
__device__ __forceinline__ float wave_sum(float x) {
  x = dpp_add<0x128>(x); x = dpp_add<0x124>(x); x = dpp_add<0x122>(x); x = dpp_add<0x121>(x);
  auto a = __builtin_amdgcn_permlane16_swap(__float_as_uint(x), __float_as_uint(x), false, false);
  x = __uint_as_float(a[0]) + __uint_as_float(a[1]);
  auto b = __builtin_amdgcn_permlane32_swap(__float_as_uint(x), __float_as_uint(x), false, false);
  return __uint_as_float(b[0]) + __uint_as_float(b[1]);
}
__device__ __forceinline__ f32x4 sel4(bool c, f32x4 a, f32x4 b) {
  asm volatile("" : "+v"(a), "+v"(b));
  return f32x4{c ? a[0] : b[0], c ? a[1] : b[1], c ? a[2] : b[2], c ? a[3] : b[3]};
}
__device__ __forceinline__ void spin_flags(rsrc_t rs, unsigned voff, u32x2 f, unsigned lo, unsigned hi) {
  for (unsigned n = 0; !__all(f.x == lo && f.y == hi) && n < SPIN_MAX; ++n) {
    __builtin_amdgcn_s_sleep(1);
    f = ld8_dev(rs, voff); WAITV(0); PIN(f);
  }
}
__device__ __forceinline__ u32x2 spin_tag(rsrc_t rs, unsigned voff, u32x2 f, unsigned tag) {
  for (unsigned n = 0; !__all(f.y == tag) && n < SPIN_MAX; ++n) {
    __builtin_amdgcn_s_sleep(1);
    f = ld8_dev(rs, voff); WAITV(0); PIN(f);
  }
  return f;
}
#define STAMP(i)

__global__ __launch_bounds__(512, 2) void h3_geglu_fused(const u16* __restrict__ x, const u16* __restrict__ kin, const u16* __restrict__ vin,
                                                         const u16* __restrict__ shk, const u16* __restrict__ ssk, const u16* __restrict__ ssd,
                                                         const u16* __restrict__ wg, const u16* __restrict__ wu, const u16* __restrict__ wd,
                                                         u16* __restrict__ out, char* __restrict__ ws) {
  extern __shared__ __attribute__((aligned(16))) char smem[];
  const unsigned L0 = (unsigned)(size_t)(LAS char*)smem;
  const int tid = threadIdx.x, lane = tid & 63;
  const int w = __builtin_amdgcn_readfirstlane(tid >> 6);
  STAMP(0);
  const int b = blockIdx.x, xcd = b & 7, idx = b >> 3, rh = xcd & 1, q4 = xcd >> 1;
  const int pan = 4 * rh + q4, aa = 32 * pan + idx;
  const int l3 = lane >> 3, l7 = lane & 7, G = lane >> 4, qq = (lane >> 2) & 3, pp = lane & 3, hh = lane >> 5;
  const rsrc_t rX = mk_rsrc(x, S_ * D_ * 2), rK = mk_rsrc(kin, S_ * D_ * 2), rV = mk_rsrc(vin, S_ * D_ * 2);
  const rsrc_t rW = mk_rsrc(ws, WS_BYTES);
  {
    const unsigned kb = (unsigned)((64 * idx + l3) * 1024 + pan * 128), fl = (unsigned)(l7 ^ ((l3 >> 1) & 1));
    dma16(rK, kb + ((fl ^ (((w + 1) & 3) << 1)) << 4), w * 8192, L0 + L_KA + (w + 1) * 1024);
    dma16(rV, kb + ((fl ^ ((w & 3) << 1)) << 4), w * 8192, L0 + L_VA + w * 1024);
    if (w == 0) dma16(rK, (idx == 0) ? OOB : kb - 8192 + (fl << 4), 0, L0 + L_KA);
  }
  u32x4 xrow = ld16(rX, (unsigned)((8 * aa + w) * 1024 + lane * 16));
  u32x2 shv = ld8(mk_rsrc(shk, D_ * 4 * 2), (unsigned)((64 * pan + 8 * w + 4 * (G & 1) + pp) * 8));
  u32x2 gen = ld8_dev(rW, WS_GEN);
  const int cg = idx < 24 ? ((q4 * 7 + 1) >> 1) + (idx >> 3) : idx < 28 ? (q4 < 2 ? 3 : 10) : 14 + ((idx >> 1) & 1);
  const int rt = 8 * rh + (idx < 24 ? (idx & 7) : idx < 28 ? (q4 & 1) * 4 + (idx & 3) : 2 * q4 + (idx & 1));
  const unsigned vx = (cg < 15) ? (unsigned)((128 * rt + 8 * w + l3) * 1024 + ((l7 ^ ((4 * w + (l3 >> 1)) & 7)) << 4)) : OOB;
#define DX(c, j) dma16(rX, vx, (c) * 128 + (j) * 65536, L0 + ((c) & 3) * SLOT + (w + 8 * (j)) * 1024)
  DX(0, 0); DX(0, 1);
  unsigned vw[3];
#pragma unroll
  for (int j = 0; j < 3; ++j) {
    const int n2 = w + 8 * j, m = n2 >= 12;
    const unsigned P = 64 * (n2 - 12 * m) + lane, k = (P * 43691u) >> 19, col = 96 * cg + 8 * ((P - 12 * k) ^ (2 * m));
    vw[j] = (col < DF) ? k * (DF * 2) + col * 2 : OOB;
  }
  const rsrc_t rG = mk_rsrc(wg, D_ * DF * 2), rU = mk_rsrc(wu, D_ * DF * 2), rM = mk_rsrc(w < 4 ? wg : wu, D_ * DF * 2);
#define DW(c, j) dma16((j) == 0 ? rG : (j) == 1 ? rM : rU, vw[j], (c) * 64 * DF * 2, L0 + ((c) & 3) * SLOT + 16384 + (w + 8 * (j)) * 1024)
#define CHUNK(c) do { DX(c, 0); DW(c, 0); DX(c, 1); DW(c, 1); DW(c, 2); } while (0)
  STAMP(14);
  DW(0, 0); DW(0, 1); DW(0, 2);
  CHUNK(1); CHUNK(2);
  STAMP(15);
  unsigned dpair = ((const unsigned*)ssd)[aa];
  asm volatile("" : "+s"(dpair));
  const rsrc_t rHk = mk_rsrc(ssk, D_ * S_ * 2), rD = mk_rsrc(wd, DF * D_ * 2);
  asm volatile("s_waitcnt vmcnt(15)\n\ts_barrier" : "+v"(gen), "+v"(xrow), "+v"(shv)::"memory");
  STAMP(1);
  s16x4 kq_[3], vq_[2];
  {
    const int o = 4 * (G >> 1) + pp;
#pragma unroll
    for (int j = 0; j < 3; ++j) { const int r = 8 * o + 4 + 4 * j + qq, f = ((r >> 1) & 1) | (((r >> 3) & 3) << 1);
      kq_[j] = trd(L0 + L_KA + r * 128 + ((w ^ f) << 4) + 8 * (G & 1)); }
#pragma unroll
    for (int j = 0; j < 2; ++j) { const int r = 8 * o + 4 * j + qq, f = ((r >> 1) & 1) | (((r >> 3) & 3) << 1);
      vq_[j] = trd(L0 + L_VA + r * 128 + ((w ^ f) << 4) + 8 * (G & 1)); }
  }
  LBAR();
  CHUNK(3);
  const unsigned elo = __builtin_amdgcn_readfirstlane(gen.x) + 1u;
  const unsigned ehi = __builtin_amdgcn_readfirstlane(gen.y) + (elo == 0u ? 1u : 0u);
  const int cch = w >> 2, ce = tid & 255;
  {
    float kk[12], vv[8];
#pragma unroll
    for (int j = 0; j < 3; ++j) { const u32x2 t = __builtin_bit_cast(u32x2, kq_[j]); kk[4 * j] = blo(t.x); kk[4 * j + 1] = bhi(t.x); kk[4 * j + 2] = blo(t.y); kk[4 * j + 3] = bhi(t.y); }
#pragma unroll
    for (int j = 0; j < 2; ++j) { const u32x2 t = __builtin_bit_cast(u32x2, vq_[j]); vv[4 * j] = blo(t.x); vv[4 * j + 1] = bhi(t.x); vv[4 * j + 2] = blo(t.y); vv[4 * j + 3] = bhi(t.y); }
    const float s0 = blo(shv.x), s1 = bhi(shv.x), s2 = blo(shv.y), s3 = bhi(shv.y);
    float r[8];
#pragma unroll
    for (int i = 0; i < 8; ++i) r[i] = (((kk[i + 1] * s3 + kk[i + 2] * s2) + kk[i + 3] * s1) + kk[i + 4] * s0) * vv[i];
    u32x4 pk = {cvtpk(r[7], r[6]), cvtpk(r[5], r[4]), cvtpk(r[3], r[2]), cvtpk(r[1], r[0])};
    const int o = 7 - l7, src = 16 * (((o >> 2) << 1) | (l3 >> 2)) + 4 * (o & 3) + (l3 & 3);
    u32x4 po;
#pragma unroll
    for (int i = 0; i < 4; ++i) po[i] = (unsigned)__builtin_amdgcn_ds_bpermute(src * 4, (int)pk[i]);
    st16_dev(rW, WS_KVT + (unsigned)((64 * pan + 8 * w + l3) * 4096 + (1984 - 64 * idx + 8 * l7) * 2), po);
    float xv[8];
#pragma unroll
    for (int i = 0; i < 4; ++i) { xv[2 * i] = blo(xrow[i]); xv[2 * i + 1] = bhi(xrow[i]); }
    float sm = ((xv[0] + xv[1]) + (xv[2] + xv[3])) + ((xv[4] + xv[5]) + (xv[6] + xv[7]));
    const float mean = wave_sum(sm) * (1.0f / D_);
    float sq = 0.f;
#pragma unroll
    for (int i = 0; i < 8; ++i) { const float d = xv[i] - mean; sq += d * d; }
    const float var = rbf(wave_sum(sq) * (1.0f / D_));
    const float sd = __builtin_sqrtf(rbf(var + 1e-5f));
    u32x2 g = {cvtpk(mean, sd), elo};
    st8_dev(rW, lane == 0 ? WS_ST + (unsigned)((8 * aa + w) * 8) : OOB, g);
  }
  STAMP(2);
  const int tg = w & 3, ch = w >> 2;
  unsigned ax[4], aw[3][4];
  {
    const int tr = 32 * tg + (lane & 31), m = G & 1;
#pragma unroll
    for (int j = 0; j < 4; ++j) {
      const int s = (j + tg) & 3;
      ax[j] = L0 + tr * 128 + (((2 * s + hh) ^ ((tr >> 1) & 7)) << 4);
#pragma unroll
      for (int i = 0; i < 3; ++i)
        aw[i][j] = L0 + 16384 + m * 12288 + (16 * s + 8 * hh + qq) * 192 + ((2 * ((3 * ch + i) ^ m) + (pp & 1)) << 4) + 8 * (pp >> 1);
    }
  }
  f32x16 acc[3];
  float cs[3] = {0.f, 0.f, 0.f};
#pragma unroll
  for (int i = 0; i < 3; ++i)
#pragma unroll
    for (int r = 0; r < 16; ++r) acc[i][r] = 0.f;
  const bf16x2_t ones = {(__bf16)1.0f, (__bf16)1.0f};
  bf16x8 xf[2], wf[2][3];
#define RD(c, j) do { const unsigned so_ = ((c) & 3) * SLOT;                                                      \
    _Pragma("unroll") for (int i = 0; i < 3; ++i) {                                                               \
      wf[(j) & 1][i] = cat(trd(aw[i][j] + so_), trd(aw[i][j] + so_ + 768));                                        \
      if (i == 0) xf[(j) & 1] = lds_ld<bf16x8>(ax[j] + so_); } SB(); } while (0)
#define PINACC() do { PIN(acc[0]); PIN(acc[1]); PIN(acc[2]); PIN(cs[0]); PIN(cs[1]); PIN(cs[2]); } while (0)
#define MM(j) do { _Pragma("unroll") for (int i = 0; i < 3; ++i) {                                                \
      acc[i] = MFMA32(wf[(j) & 1][i], xf[(j) & 1], acc[i]);                                                        \
      if ((j) == 0) { const u32x4 u_ = __builtin_bit_cast(u32x4, wf[0][i]);                                        \
        _Pragma("unroll") for (int e = 0; e < 4; ++e) { const unsigned t_ = u_[e];                                 \
          cs[i] = __builtin_amdgcn_fdot2_f32_bf16(__builtin_bit_cast(bf16x2_t, t_), ones, cs[i], false); } } }     \
    PINACC(); SB(); } while (0)
#define COMPUTE(c, D0, D1, D2, D3) do { RD(c, 0); if ((c) > 0) MM(3); D0; RD(c, 1); MM(0); D1; RD(c, 2); MM(1); D2; RD(c, 3); MM(2); D3; } while (0)
#define COMPUTE_N(c) COMPUTE(c, { DX(c + 3, 0); DW(c + 3, 0); }, DX(c + 3, 1), DW(c + 3, 1), DW(c + 3, 2))
  const int kq = w & 3, tt2 = w >> 2, r64 = 4 * pan + (idx >> 3), ct = idx & 7;
  const int ks0 = 22 * kq - (kq == 3 ? 1 : 0);
  const unsigned vd = (unsigned)((16 * ks0 + 8 * tt2 + l3) * 1024 + ct * 128 + ((l7 ^ (((l3 >> 1) & 1) << 2)) << 4));
#define WDU(j, lds) dma16(rD, vd, (j) * 16384, L0 + (lds) + tt2 * 1024)
#define WDA(j) WDU(j, ((j) * 4 + kq) * 2048)

  WAIT_BAR(17); STAMP(16); COMPUTE(0, {}, {}, {}, {}); STAMP(17);
  WAIT_BAR(12); STAMP(18); COMPUTE_N(1); STAMP(20);
  WAIT_BAR(12); STAMP(21); COMPUTE_N(2); STAMP(22);
  WAIT_BAR(10); STAMP(23);
  if (tid == 0) { u32x2 e = {elo, ehi}; st8_dev(rW, WS_FA + (unsigned)(aa * 128), e); }
  COMPUTE_N(3);
  STAMP(3);
  WAIT_BAR(10); COMPUTE_N(4);
  WAIT_BAR(10);
  const unsigned vst = WS_ST + (unsigned)((128 * rt + 32 * tg + (lane & 31)) * 8);
  u32x2 stg, fa;
  COMPUTE(5, { stg = ld8_dev(rW, vst); WDA(0); }, WDA(1), WDA(2), WDA(3));
  WAIT_BAR(10);
  const unsigned vfa = WS_FA + (unsigned)((32 * pan + (lane & 31)) * 128);
  COMPUTE(6, { fa = ld8_dev(rW, vfa); WDA(4); }, { WDA(5); WDA(6); }, { WDA(7); WDA(8); }, WDA(9));
  asm volatile("s_waitcnt vmcnt(11) lgkmcnt(0)\n\ts_barrier" : "+v"(stg)::"memory");
  STAMP(4);
  RD(7, 0); MM(3); WDA(10); RD(7, 1); MM(0); WDA(11);
#pragma unroll
  for (int i = 0; i < 3; ++i) lds_st<float>(L0 + L_CV + ((ch * 3 + i) * 4 + tg) * 256 + lane * 4, cs[i]);
  RD(7, 2); MM(1); WDA(12);
  LBAR();
  u32x4 hcur, hprv;
  {
    float c4[3][4];
    RD(7, 3);
#pragma unroll
    for (int i = 0; i < 3; ++i)
#pragma unroll
      for (int t = 0; t < 4; ++t) c4[i][t] = lds_ld<float>(L0 + L_CV + ((ch * 3 + i) * 4 + t) * 256 + lane * 4);
    SB();
    MM(2); WDA(13);
    LBAR();
    MM(3);
    hcur = ld16(rHk, (unsigned)((2 * aa + cch) * 4096 + ce * 16));
    hprv = ld16(rHk, ce ? (unsigned)((2 * aa + cch) * 4096 + ce * 16 - 16) : OOB);
    stg = spin_tag(rW, vst, stg, elo);
    const unsigned mn = (stg.x & 0xffffu) ^ 0x8000u;
    const u32x4 bm = {mn | (mn << 16), mn, 0u, 0u};
#pragma unroll
    for (int i = 0; i < 3; ++i) {
      const float c = (c4[i][0] + c4[i][1]) + (c4[i][2] + c4[i][3]);
      const float c1 = c - rbf(c), c2 = c1 - rbf(c1);
      const u32x4 am = {cvtpk(c, c1), cvtpk(c2, 0.f), 0u, 0u};
      acc[i] = MFMA32(__builtin_bit_cast(bf16x8, am), __builtin_bit_cast(bf16x8, bm), acc[i]);
    }
    PINACC();
  }
  STAMP(5);
  u32x4 urow;
  {
    const float sd = bhi(stg.x), rs = __builtin_amdgcn_rcpf(sd);
    const float k0 = rs * -2.3022081983f, k1 = k0 * rs * rs * 0.044715f, s2 = sd * sd;
#pragma unroll
    for (int i = 0; i < 3; ++i) {
      float hv[8];
#pragma unroll
      for (int r = 0; r < 8; ++r) {
        const float g = acc[i][r], u = acc[i][r + 8];
        const float e = __builtin_amdgcn_exp2f(g * __builtin_fmaf(g * g, k1, k0));
        hv[r] = (g * u) * __builtin_amdgcn_rcpf(__builtin_fmaf(e, s2, s2));
      }
      const u32x4 hp = {cvtpk(hv[0], hv[1]), cvtpk(hv[2], hv[3]), cvtpk(hv[4], hv[5]), cvtpk(hv[6], hv[7])};
      const int ks = 6 * cg + 3 * ch + i;
      st16_dev(rW, ks < 86 ? WS_H + (unsigned)(((4 * rt + tg) * 86 + ks) * 1024 + lane * 16) : OOB, hp);
      if (i == 0) {
        asm volatile("s_waitcnt vmcnt(13)" : "+v"(fa)::"memory");
        spin_flags(rW, vfa, fa, elo, ehi);
        urow = ld16_dev(rW, WS_KVT + (unsigned)((2 * aa + cch) * 4096 + ce * 16), 0);
      }
    }
  }
  STAMP(6);
  asm volatile("s_waitcnt vmcnt(2)" : "+v"(urow), "+v"(hcur), "+v"(hprv)::"memory");
  {
    const unsigned hb = L0 + L_CV + cch * 16640;
    auto put = [&](u32x4 pv, u32x4 cu, int blk) {
      const unsigned a0 = __builtin_amdgcn_alignbit(pv[1], pv[0], 16), a1 = __builtin_amdgcn_alignbit(pv[2], pv[1], 16),
                     a2 = __builtin_amdgcn_alignbit(pv[3], pv[2], 16), a3 = __builtin_amdgcn_alignbit(cu[0], pv[3], 16),
                     a4 = __builtin_amdgcn_alignbit(cu[1], cu[0], 16);
      lds_st<u32x4>(hb + blk * 16, u32x4{a0, a1, a2, a3});
      lds_st<u32x4>(hb + 4160 + blk * 16, u32x4{pv[1], pv[2], pv[3], cu[0]});
      lds_st<u32x4>(hb + 8320 + blk * 16, u32x4{a1, a2, a3, a4});
      lds_st<u32x4>(hb + 12480 + blk * 16, u32x4{pv[2], pv[3], cu[0], cu[1]});
    };
    put(hprv, hcur, ce + 1);
    const u32x4 zero4 = {0u, 0u, 0u, 0u};
    if (ce == 0) put(zero4, zero4, 0);
    if (ce == 255) put(hcur, zero4, 257);
    lds_st<u32x4>(L0 + L_UR + cch * 4608 + ce * 16, urow);
    if (ce < 30) lds_st<u32x4>(L0 + L_UR + cch * 4608 + 4096 + ce * 16, zero4);
    if (tid < 2) lds_st<u32x4>(L0 + L_SYNC + tid * 16, zero4);
  }
  LBAR();
  STAMP(7);
  const unsigned ad = L0 + (8 * hh + qq) * 128 + ((((4 * ((qq >> 1) & 1)) + 2 * (G & 1) + (pp >> 1))) << 4) + 8 * (pp & 1);
#define WDF(base, dt) cat(trd((ad ^ ((dt) * 64)) + (base)), trd((ad ^ ((dt) * 64)) + (base) + 512))
  bf16x8 wpk[6][2];
#pragma unroll
  for (int j = 0; j < 6; ++j) { wpk[j][0] = WDF((j * 4 + kq) * 2048, 0); wpk[j][1] = WDF((j * 4 + kq) * 2048, 1); }
  f32x4 ya[8];
#pragma unroll
  for (int c = 0; c < 8; ++c) ya[c] = f32x4{0.f, 0.f, 0.f, 0.f};
  const int vch = (w >> 1) & 1, vrh = w & 1, c15 = lane & 15;
  const unsigned cA = L0 + L_CV + vch * 16640 + (c15 & 3) * 4160 + (4 * (c15 >> 2) + 8 * G + 128 * vrh) * 2;
  const unsigned cB = L0 + L_UR + vch * 4608 + (240 - 16 * c15 + 8 * G + 128 * vrh) * 2;
  bf16x8 bfr[2][8], afr[2][2];
#define VLD8(a) (*(const volatile LAS s16x4*)(size_t)(a))
#define CA(set, rho, p) do { afr[set][0] = cat(VLD8(cA + (rho) * 64 + (p) * 512), VLD8(cA + (rho) * 64 + (p) * 512 + 8));                 \
    afr[set][1] = cat(VLD8(cA + (rho) * 64 + (7 - (p)) * 512), VLD8(cA + (rho) * 64 + (7 - (p)) * 512 + 8)); } while (0)
#define CB(rho, mu) bfr[(rho) & 1][mu] = lds_ld<bf16x8>(cB + (rho) * 64 + (7 - (mu)) * 512)
#define PINY() do { _Pragma("unroll") for (int c = 0; c < 8; ++c) PIN(ya[c]); } while (0)
#define CPAIR(rho, p) do { if ((p) < 3) CA(((p) + 1) & 1, rho, (p) + 1); else if ((rho) < 3) CA(0, (rho) + 1, 0);                          \
    if ((rho) < 3) { CB((rho) + 1, 2 * (p)); CB((rho) + 1, 2 * (p) + 1); } SB();                                                          \
    _Pragma("unroll") for (int mu = 0; mu + (p) < 8; ++mu) {                                                                              \
      ya[(p) + mu] = MFMA16(afr[(p) & 1][0], bfr[(rho) & 1][mu], ya[(p) + mu]);                                                            \
      if (mu <= (p)) ya[7 - (p) + mu] = MFMA16(afr[(p) & 1][1], bfr[(rho) & 1][mu], ya[7 - (p) + mu]); }                                    \
    PINY(); SB(); } while (0)
#define CONV(rho) do { CPAIR(rho, 0); CPAIR(rho, 1); CPAIR(rho, 2); CPAIR(rho, 3); } while (0)
  STAMP(24);
  if (w < 4) {
    CA(0, 0, 0);
#pragma unroll
    for (int mu = 0; mu < 8; ++mu) CB(0, mu);
    SB();
    CONV(0); CONV(1);
  }
  STAMP(25);
  WAIT_BAR(0);
  if (tid == 256 && cg < 15) { u32x2 e = {elo, ehi}; st8_dev(rW, WS_FH + (unsigned)((rt * 16 + cg) * 128), e); }
  STAMP(8);
#pragma unroll
  for (int j = 0; j < 6; ++j) WDU(14 + j, (j * 4 + kq) * 2048);
  const int ftok = 64 * r64 + 8 * w + l3;
  const unsigned vxo = (unsigned)(ftok * 1024 + ct * 128 + l7 * 16), vs2 = WS_ST + (unsigned)(ftok * 8);
  u32x4 xo = ld16(rX, vxo);
  u32x2 st2 = ld8_dev(rW, vs2);
  STAMP(26);
  if (w < 4) { CONV(2); CONV(3); }
  STAMP(27);
  LBAR();
  WDU(20, L_WD4 + kq * 2048); WDU(21, L_WD4 + (4 + kq) * 2048);
  if (w < 4) {
#pragma unroll
    for (int c = 0; c < 4; ++c) lds_st<f32x4>(L0 + L_CV + (vch * 2 + (vrh ^ 1)) * 4096 + c * 1024 + lane * 16, sel4(vrh, ya[c], ya[4 + c]));
  }
  LBAR();
  if (w < 4) {
    const unsigned xb = L0 + L_CV + (vch * 2 + vrh) * 4096;
    const float dsc = vch ? bhi(dpair) : blo(dpair);
    u32x2 zp[4];
#pragma unroll
    for (int c = 0; c < 4; ++c) {
      const f32x4 o = lds_ld<f32x4>(xb + c * 1024 + lane * 16), y = sel4(vrh, ya[4 + c], ya[c]);
      const u32x2 ur = lds_ld<u32x2>(L0 + L_UR + vch * 4608 + (2044 - 256 * (4 * vrh + c) - 16 * c15 - 4 * G) * 2);
      const float z0 = rbf(y[0] + o[0]) + rbf(bhi(ur.y) * dsc), z1 = rbf(y[1] + o[1]) + rbf(blo(ur.y) * dsc);
      const float z2 = rbf(y[2] + o[2]) + rbf(bhi(ur.x) * dsc), z3 = rbf(y[3] + o[3]) + rbf(blo(ur.x) * dsc);
      zp[c] = u32x2{cvtpk(z0, z1), cvtpk(z2, z3)};
    }
#pragma unroll
    for (int c = 0; c < 4; ++c) lds_st<u32x2>(xb + c * 512 + c15 * 32 + G * 8, zp[c]);
#pragma unroll
    for (int i = 0; i < 2; ++i)
      st16_dev(rW, WS_ZT + (unsigned)((2 * aa + vch) * 4096 + vrh * 2048 + i * 1024 + lane * 16), lds_ld<u32x4>(xb + i * 1024 + lane * 16));
  }
  STAMP(9);
  if (w >= 4) {
    const int gf = ks0 / 6, gl = (ks0 + 20 + (kq < 2)) / 6, gi = min(gf + (lane & 7), gl);
    const unsigned vfh = WS_FH + (unsigned)(((r64 >> 1) * 16 + gi) * 128);
    u32x2 fh = ld8_dev(rW, vfh); WAITV(0); PIN(fh);
    spin_flags(rW, vfh, fh, elo, ehi);
    *(volatile LAS unsigned*)(size_t)(L0 + L_SYNC + kq * 4) = 1u;
  } else {
    for (unsigned n = 0; *(volatile LAS unsigned*)(size_t)(L0 + L_SYNC + kq * 4) == 0u && n < SPIN_MAX * 16; ++n) __builtin_amdgcn_s_sleep(1);
  }
  STAMP(10);
  u32x4 hf[22];
  const unsigned vh = WS_H + (unsigned)(((2 * r64 + tt2) * 86 + ks0) * 1024 + lane * 16);
#define HL(j) hf[j] = ld16_dev(rW, ((j) < 21 || kq < 2) ? vh : OOB, (j) * 1024)
#pragma unroll
  for (int j = 0; j < 10; ++j) HL(j);
  STAMP(28);
  const unsigned vfz = WS_FZ + (unsigned)((32 * ct + (lane & 31)) * 128);
  u32x2 fz;
  f32x16 fa0, fa1;
#pragma unroll
  for (int r = 0; r < 16; ++r) { fa0[r] = 0.f; fa1[r] = 0.f; }
#define G2M(j, N, A0, A1) do { asm volatile("s_waitcnt vmcnt(" #N ")" : "+v"(hf[j])::"memory");                     \
    fa0 = MFMA32(A0, __builtin_bit_cast(bf16x8, hf[j]), fa0); fa1 = MFMA32(A1, __builtin_bit_cast(bf16x8, hf[j]), fa1); PIN(fa0); PIN(fa1); SB(); \
    if ((j) + 10 < 22) HL(((j) + 10) % 22); } while (0)
#define G2P(j, N) G2M(j, N, wpk[j][0], wpk[j][1])
#define G2R(slot, base) do { wpk[slot][0] = WDF(base, 0); wpk[slot][1] = WDF(base, 1); } while (0)
#define UA(j) (((j) * 4 + kq) * 2048)
  G2P(0, 9);
  STAMP(29);
  if (w < 4 && lane == 0) {
    if (__hip_atomic_fetch_add((LAS unsigned*)(size_t)(L0 + L_SYNC + 16), 1u, __ATOMIC_RELAXED, __HIP_MEMORY_SCOPE_WORKGROUP) == 3u) {
      u32x2 e = {elo, ehi}; st8_dev(rW, WS_FZ + (unsigned)(aa * 128), e); }
  }
  G2P(1, 9); G2P(2, 9); G2P(3, 9);
  G2R(0, UA(6));  G2P(4, 9);
  G2R(1, UA(7));  G2P(5, 9);
  G2R(2, UA(8));  G2M(6, 9, wpk[0][0], wpk[0][1]);
  G2R(3, UA(9));  G2M(7, 9, wpk[1][0], wpk[1][1]);
  G2R(0, UA(10)); G2M(8, 9, wpk[2][0], wpk[2][1]);
  G2R(1, UA(11)); G2M(9, 9, wpk[3][0], wpk[3][1]);
  G2R(2, UA(12)); G2M(10, 9, wpk[0][0], wpk[0][1]);
  G2R(3, UA(13)); G2M(11, 9, wpk[1][0], wpk[1][1]);
  asm volatile("s_barrier" ::: "memory");
  G2R(0, UA(0));  G2M(12, 9, wpk[2][0], wpk[2][1]);
  G2R(1, UA(1));  G2M(13, 8, wpk[3][0], wpk[3][1]);
  G2R(2, UA(2));  G2M(14, 7, wpk[0][0], wpk[0][1]);
  G2R(3, UA(3));  G2M(15, 6, wpk[1][0], wpk[1][1]);
  fz = ld8_dev(rW, vfz);
  G2R(0, UA(4));  G2M(16, 6, wpk[2][0], wpk[2][1]);
  G2R(1, UA(5));  G2M(17, 5, wpk[3][0], wpk[3][1]);
  G2R(2, L_WD4 + kq * 2048);       G2M(18, 4, wpk[0][0], wpk[0][1]);
  G2R(3, L_WD4 + (4 + kq) * 2048); G2M(19, 3, wpk[1][0], wpk[1][1]);
  G2M(20, 2, wpk[2][0], wpk[2][1]);
  G2M(21, 1, wpk[3][0], wpk[3][1]);
  STAMP(11);
  asm volatile("s_waitcnt vmcnt(0)" : "+v"(xo), "+v"(st2), "+v"(fz)::"memory");
  spin_flags(rW, vfz, fz, elo, ehi);
  u32x4 zr = ld16_dev(rW, WS_ZT + (unsigned)((64 * ct + 8 * w + l3) * 4096 + r64 * 128 + l7 * 16), 0);
  st2 = spin_tag(rW, vs2, st2, elo);
  STAMP(30);
  LBAR();
  STAMP(31);
  {
    const int tok = 32 * tt2 + (lane & 31);
    const unsigned pb = L0 + kq * 16384 + tok * 256;
#pragma unroll
    for (int i = 0; i < 4; ++i) {
      lds_st<f32x4>(pb + (((2 * i + hh) ^ (tok & 7)) << 4), f32x4{fa0[4 * i], fa0[4 * i + 1], fa0[4 * i + 2], fa0[4 * i + 3]});
      lds_st<f32x4>(pb + (((8 + 2 * i + hh) ^ (tok & 7)) << 4), f32x4{fa1[4 * i], fa1[4 * i + 1], fa1[4 * i + 2], fa1[4 * i + 3]});
    }
    asm volatile("s_waitcnt vmcnt(0)" : "+v"(zr)::"memory");
    const int r = 8 * w + l3, f = ((r >> 1) & 1) | (((r >> 3) & 3) << 1);
    lds_st<u32x4>(L0 + L_ZT + r * 128 + ((l7 ^ f) << 4), zr);
  }
  LBAR();
  STAMP(12);
  {
    u32x2 zt[2];
    const int o = 4 * (G >> 1) + pp;
#pragma unroll
    for (int j = 0; j < 2; ++j) { const int r = 8 * o + 4 * j + qq, f = ((r >> 1) & 1) | (((r >> 3) & 3) << 1);
      zt[j] = __builtin_bit_cast(u32x2, trd(L0 + L_ZT + r * 128 + ((w ^ f) << 4) + 8 * (G & 1))); }
    const int tl = 8 * w + l3;
    f32x4 p0 = {0.f, 0.f, 0.f, 0.f}, p1 = p0;
#pragma unroll
    for (int k = 0; k < 4; ++k) {
      p0 += lds_ld<f32x4>(L0 + k * 16384 + tl * 256 + (((2 * l7) ^ (tl & 7)) << 4));
      p1 += lds_ld<f32x4>(L0 + k * 16384 + tl * 256 + (((2 * l7 + 1) ^ (tl & 7)) << 4));
    }
    const int src = 16 * (((l7 >> 2) << 1) | (l3 >> 2)) + 4 * (l7 & 3) + (l3 & 3);
    u32x4 zq;
    zq[0] = (unsigned)__builtin_amdgcn_ds_bpermute(src * 4, (int)zt[0].x); zq[1] = (unsigned)__builtin_amdgcn_ds_bpermute(src * 4, (int)zt[0].y);
    zq[2] = (unsigned)__builtin_amdgcn_ds_bpermute(src * 4, (int)zt[1].x); zq[3] = (unsigned)__builtin_amdgcn_ds_bpermute(src * 4, (int)zt[1].y);
    const float mean = blo(st2.x), rs = __builtin_amdgcn_rcpf(bhi(st2.x));
    float o8[8];
#pragma unroll
    for (int i = 0; i < 8; ++i) {
      const float xv = (i & 1) ? bhi(xo[i >> 1]) : blo(xo[i >> 1]), zv = (i & 1) ? bhi(zq[i >> 1]) : blo(zq[i >> 1]);
      const float F = i < 4 ? p0[i & 3] : p1[i & 3];
      const float gs = F * __builtin_amdgcn_rcpf(1.0f + __builtin_amdgcn_exp2f(F * -1.4426950408889634f));
      o8[i] = __builtin_fmaf((xv - mean) * rs * zv, gs, xv);
    }
    const u32x4 ov = {cvtpk(o8[0], o8[1]), cvtpk(o8[2], o8[3]), cvtpk(o8[4], o8[5]), cvtpk(o8[6], o8[7])};
    *(GAS u32x4*)((GAS char*)out + vxo) = ov;
  }
  if (b == 0 && w == 0) {
#pragma unroll
    for (int i = 0; i < 4; ++i) {
      const unsigned v = WS_FA + (unsigned)((64 * i + lane) * 128);
      u32x2 f = ld8_dev(rW, v); WAITV(0); PIN(f);
      spin_flags(rW, v, f, elo, ehi);
    }
    if (lane == 0) { u32x2 e = {elo, ehi}; st8_dev(rW, WS_GEN, e); }
  }
}

extern "C" void kernel_launch(void* const* d_in, const int* in_sizes, int n_in, void* d_out, int out_size, void* d_ws, size_t ws_size,
                              hipStream_t stream) {
  (void)in_sizes; (void)n_in; (void)out_size; (void)ws_size;
  (void)hipFuncSetAttribute((const void*)h3_geglu_fused, hipFuncAttributeMaxDynamicSharedMemorySize, LDS_BYTES);
  h3_geglu_fused<<<256, 512, LDS_BYTES, stream>>>((const u16*)d_in[0], (const u16*)d_in[1], (const u16*)d_in[2], (const u16*)d_in[3],
                                                  (const u16*)d_in[4], (const u16*)d_in[5], (const u16*)d_in[7], (const u16*)d_in[8],
                                                  (const u16*)d_in[9], (u16*)d_out, (char*)d_ws);
}
```

```cpp
#include <hip/hip_runtime.h>
#include <hip/hip_bf16.h>

typedef unsigned short u16;
typedef unsigned long long u64;
typedef __attribute__((ext_vector_type(8))) short bf16x8;
typedef __attribute__((ext_vector_type(4))) short s16x4;
typedef __attribute__((ext_vector_type(2))) __bf16 bf16x2_t;
typedef __attribute__((ext_vector_type(4))) float f32x4;
typedef __attribute__((ext_vector_type(16))) float f32x16;
typedef __attribute__((ext_vector_type(4))) unsigned u32x4;
typedef __attribute__((ext_vector_type(2))) unsigned u32x2;
#define LAS __attribute__((address_space(3)))
#define GAS __attribute__((address_space(1)))
typedef __amdgpu_buffer_rsrc_t rsrc_t;

constexpr int S_ = 2048, D_ = 512, DF = 1376;
constexpr unsigned WS_GEN = 0, WS_FA = 4096, WS_FH = WS_FA + 256 * 128, WS_FZ = WS_FH + 256 * 128;
constexpr unsigned WS_ST = WS_FZ + 256 * 128;
constexpr unsigned WS_KVT = 1u << 20;
constexpr unsigned WS_ZT = WS_KVT + (2u << 20);
constexpr unsigned WS_H = WS_ZT + (2u << 20);
constexpr unsigned WS_BYTES = WS_H + 64 * 86 * 1024;
constexpr int SLOT = 40960, L_KA = 3 * SLOT, L_VA = L_KA + 9216;
constexpr int L_SYNC = 163840 - 32;
constexpr int L_CV = 114688, L_UR = L_CV + 33280, L_WD4 = 131072, L_ZT = 65536;
constexpr int LDS_BYTES = 163840;
constexpr unsigned OOB = 0x80000000u;
constexpr unsigned SPIN_MAX = 1u << 18;

#define MFMA32(a, b, c) __builtin_amdgcn_mfma_f32_32x32x16_bf16(a, b, c, 0, 0, 0)
#define MFMA16(a, b, c) __builtin_amdgcn_mfma_f32_16x16x32_bf16(a, b, c, 0, 0, 0)
#define WAIT_BAR(N) asm volatile("s_waitcnt vmcnt(" #N ") lgkmcnt(0)\n\ts_barrier" ::: "memory")
#define LBAR() asm volatile("s_waitcnt lgkmcnt(0)\n\ts_barrier" ::: "memory")
#define WAITV(N) asm volatile("s_waitcnt vmcnt(" #N ")" ::: "memory")
#define SB() __builtin_amdgcn_sched_barrier(0)

__device__ __forceinline__ rsrc_t mk_rsrc(const void* p, unsigned bytes) {
  return __builtin_amdgcn_make_buffer_rsrc((void*)p, (short)0, (int)bytes, 0x00020000);
}
__device__ __forceinline__ void dma16(rsrc_t rs, unsigned voff, unsigned soff, unsigned lds) {
  asm volatile("s_mov_b32 m0, %0\n\ts_nop 0\n\tbuffer_load_dwordx4 %1, %2, %3 offen lds" ::"s"(lds), "v"(voff), "s"(rs), "s"(soff) : "memory");
}
__device__ __forceinline__ u32x4 ld16(rsrc_t rs, unsigned voff) {
  u32x4 r; asm volatile("buffer_load_dwordx4 %0, %1, %2, 0 offen" : "=v"(r) : "v"(voff), "s"(rs) : "memory"); return r;
}
__device__ __forceinline__ u32x4 ld16_dev(rsrc_t rs, unsigned voff, unsigned soff) {
  u32x4 r; asm volatile("buffer_load_dwordx4 %0, %1, %2, %3 offen sc1" : "=v"(r) : "v"(voff), "s"(rs), "s"(soff) : "memory"); return r;
}
__device__ __forceinline__ u32x2 ld8(rsrc_t rs, unsigned voff) {
  u32x2 r; asm volatile("buffer_load_dwordx2 %0, %1, %2, 0 offen" : "=v"(r) : "v"(voff), "s"(rs) : "memory"); return r;
}
__device__ __forceinline__ u32x2 ld8_dev(rsrc_t rs, unsigned voff) {
  u32x2 r; asm volatile("buffer_load_dwordx2 %0, %1, %2, 0 offen sc1" : "=v"(r) : "v"(voff), "s"(rs) : "memory"); return r;
}
#define PIN(x) asm volatile("" : "+v"(x))
__device__ __forceinline__ void st16_dev(rsrc_t rs, unsigned voff, u32x4 v) { __builtin_amdgcn_raw_buffer_store_b128(v, rs, voff, 0, 16); }
__device__ __forceinline__ void st8_dev(rsrc_t rs, unsigned voff, u32x2 v) { __builtin_amdgcn_raw_buffer_store_b64(v, rs, voff, 0, 16); }

__device__ __forceinline__ unsigned cvtpk(float lo, float hi) {
  bf16x2_t r = {(__bf16)lo, (__bf16)hi};
  return __builtin_bit_cast(unsigned, r);
}
__device__ __forceinline__ float blo(unsigned w) { return __uint_as_float(w << 16); }
__device__ __forceinline__ float bhi(unsigned w) { return __uint_as_float(w & 0xffff0000u); }
__device__ __forceinline__ float rbf(float f) { return blo(cvtpk(f, f)); }
__device__ __forceinline__ s16x4 trd(unsigned a) { return __builtin_amdgcn_ds_read_tr16_b64_v4i16((LAS s16x4*)(size_t)a); }
__device__ __forceinline__ bf16x8 cat(s16x4 a, s16x4 b) { return __builtin_shufflevector(a, b, 0, 1, 2, 3, 4, 5, 6, 7); }
template <class T> __device__ __forceinline__ T lds_ld(unsigned a) { return *(const LAS T*)(size_t)a; }
template <class T> __device__ __forceinline__ void lds_st(unsigned a, T v) { *(LAS T*)(size_t)a = v; }
template <int CTRL> __device__ __forceinline__ float dpp_add(float x) {
  return x + __uint_as_float(__builtin_amdgcn_update_dpp(0u, __float_as_uint(x), CTRL, 0xf, 0xf, true));
}
__device__ __forceinline__ float wave_sum(float x) {
  x = dpp_add<0x128>(x); x = dpp_add<0x124>(x); x = dpp_add<0x122>(x); x = dpp_add<0x121>(x);
  auto a = __builtin_amdgcn_permlane16_swap(__float_as_uint(x), __float_as_uint(x), false, false);
  x = __uint_as_float(a[0]) + __uint_as_float(a[1]);
  auto b = __builtin_amdgcn_permlane32_swap(__float_as_uint(x), __float_as_uint(x), false, false);
  return __uint_as_float(b[0]) + __uint_as_float(b[1]);
}
__device__ __forceinline__ f32x4 sel4(bool c, f32x4 a, f32x4 b) {
  asm volatile("" : "+v"(a), "+v"(b));
  return f32x4{c ? a[0] : b[0], c ? a[1] : b[1], c ? a[2] : b[2], c ? a[3] : b[3]};
}
__device__ __forceinline__ void spin_flags(rsrc_t rs, unsigned voff, u32x2 f, unsigned lo, unsigned hi) {
  for (unsigned n = 0; !__all(f.x == lo && f.y == hi) && n < SPIN_MAX; ++n) {
    __builtin_amdgcn_s_sleep(1);
    f = ld8_dev(rs, voff); WAITV(0); PIN(f);
  }
}
__device__ __forceinline__ u32x2 spin_tag(rsrc_t rs, unsigned voff, u32x2 f, unsigned tag) {
  for (unsigned n = 0; !__all(f.y == tag) && n < SPIN_MAX; ++n) {
    __builtin_amdgcn_s_sleep(1);
    f = ld8_dev(rs, voff); WAITV(0); PIN(f);
  }
  return f;
}
#define STAMP(i) do { if ((i) == 0) { prb0 = __builtin_amdgcn_s_memrealtime(); prb1 = prb0; } if ((i) == 0) { SB(); prb1 = __builtin_amdgcn_s_memrealtime(); SB(); } } while (0)

__global__ __launch_bounds__(512, 2) void h3_geglu_fused(const u16* __restrict__ x, const u16* __restrict__ kin, const u16* __restrict__ vin,
                                                         const u16* __restrict__ shk, const u16* __restrict__ ssk, const u16* __restrict__ ssd,
                                                         const u16* __restrict__ wg, const u16* __restrict__ wu, const u16* __restrict__ wd,
                                                         u16* __restrict__ out, char* __restrict__ ws) {
  extern __shared__ __attribute__((aligned(16))) char smem[];
  const unsigned L0 = (unsigned)(size_t)(LAS char*)smem;
  const int tid = threadIdx.x, lane = tid & 63;
  const int w = __builtin_amdgcn_readfirstlane(tid >> 6);
  u64 prb0, prb1;
  STAMP(0);
  const int b = blockIdx.x, xcd = b & 7, idx = b >> 3, rh = xcd & 1, q4 = xcd >> 1;
  const int pan = 4 * rh + q4, aa = 32 * pan + idx;
  const int l3 = lane >> 3, l7 = lane & 7, G = lane >> 4, qq = (lane >> 2) & 3, pp = lane & 3, hh = lane >> 5;
  const rsrc_t rX = mk_rsrc(x, S_ * D_ * 2), rK = mk_rsrc(kin, S_ * D_ * 2), rV = mk_rsrc(vin, S_ * D_ * 2);
  const rsrc_t rW = mk_rsrc(ws, WS_BYTES);
  {
    const unsigned kb = (unsigned)((64 * idx + l3) * 1024 + pan * 128), fl = (unsigned)(l7 ^ ((l3 >> 1) & 1));
    dma16(rK, kb + ((fl ^ (((w + 1) & 3) << 1)) << 4), w * 8192, L0 + L_KA + (w + 1) * 1024);
    dma16(rV, kb + ((fl ^ ((w & 3) << 1)) << 4), w * 8192, L0 + L_VA + w * 1024);
    if (w == 0) dma16(rK, (idx == 0) ? OOB : kb - 8192 + (fl << 4), 0, L0 + L_KA);
  }
  u32x4 xrow = ld16(rX, (unsigned)((8 * aa + w) * 1024 + lane * 16));
  u32x2 shv = ld8(mk_rsrc(shk, D_ * 4 * 2), (unsigned)((64 * pan + 8 * w + 4 * (G & 1) + pp) * 8));
  u32x2 gen = ld8_dev(rW, WS_GEN);
  const int cg = idx < 24 ? ((q4 * 7 + 1) >> 1) + (idx >> 3) : idx < 28 ? (q4 < 2 ? 3 : 10) : 14 + ((idx >> 1) & 1);
  const int rt = 8 * rh + (idx < 24 ? (idx & 7) : idx < 28 ? (q4 & 1) * 4 + (idx & 3) : 2 * q4 + (idx & 1));
  const unsigned vx = (cg < 15) ? (unsigned)((128 * rt + 8 * w + l3) * 1024 + ((l7 ^ ((4 * w + (l3 >> 1)) & 7)) << 4)) : OOB;
#define DX(c, j) dma16(rX, vx, (c) * 128 + (j) * 65536, L0 + ((c) & 3) * SLOT + (w + 8 * (j)) * 1024)
  DX(0, 0); DX(0, 1);
  unsigned vw[3];
#pragma unroll
  for (int j = 0; j < 3; ++j) {
    const int n2 = w + 8 * j, m = n2 >= 12;
    const unsigned P = 64 * (n2 - 12 * m) + lane, k = (P * 43691u) >> 19, col = 96 * cg + 8 * ((P - 12 * k) ^ (2 * m));
    vw[j] = (col < DF) ? k * (DF * 2) + col * 2 : OOB;
  }
  const rsrc_t rG = mk_rsrc(wg, D_ * DF * 2), rU = mk_rsrc(wu, D_ * DF * 2), rM = mk_rsrc(w < 4 ? wg : wu, D_ * DF * 2);
#define DW(c, j) dma16((j) == 0 ? rG : (j) == 1 ? rM : rU, vw[j], (c) * 64 * DF * 2, L0 + ((c) & 3) * SLOT + 16384 + (w + 8 * (j)) * 1024)
#define CHUNK(c) do { DX(c, 0); DW(c, 0); DX(c, 1); DW(c, 1); DW(c, 2); } while (0)
  STAMP(14);
  DW(0, 0); DW(0, 1); DW(0, 2);
  CHUNK(1); CHUNK(2);
  STAMP(15);
  unsigned dpair = ((const unsigned*)ssd)[aa];
  asm volatile("" : "+s"(dpair));
  const rsrc_t rHk = mk_rsrc(ssk, D_ * S_ * 2), rD = mk_rsrc(wd, DF * D_ * 2);
  asm volatile("s_waitcnt vmcnt(15)\n\ts_barrier" : "+v"(gen), "+v"(xrow), "+v"(shv)::"memory");
  STAMP(1);
  s16x4 kq_[3], vq_[2];
  {
    const int o = 4 * (G >> 1) + pp;
#pragma unroll
    for (int j = 0; j < 3; ++j) { const int r = 8 * o + 4 + 4 * j + qq, f = ((r >> 1) & 1) | (((r >> 3) & 3) << 1);
      kq_[j] = trd(L0 + L_KA + r * 128 + ((w ^ f) << 4) + 8 * (G & 1)); }
#pragma unroll
    for (int j = 0; j < 2; ++j) { const int r = 8 * o + 4 * j + qq, f = ((r >> 1) & 1) | (((r >> 3) & 3) << 1);
      vq_[j] = trd(L0 + L_VA + r * 128 + ((w ^ f) << 4) + 8 * (G & 1)); }
  }
  LBAR();
  CHUNK(3);
  const unsigned elo = __builtin_amdgcn_readfirstlane(gen.x) + 1u;
  const unsigned ehi = __builtin_amdgcn_readfirstlane(gen.y) + (elo == 0u ? 1u : 0u);
  const int cch = w >> 2, ce = tid & 255;
  {
    float kk[12], vv[8];
#pragma unroll
    for (int j = 0; j < 3; ++j) { const u32x2 t = __builtin_bit_cast(u32x2, kq_[j]); kk[4 * j] = blo(t.x); kk[4 * j + 1] = bhi(t.x); kk[4 * j + 2] = blo(t.y); kk[4 * j + 3] = bhi(t.y); }
#pragma unroll
    for (int j = 0; j < 2; ++j) { const u32x2 t = __builtin_bit_cast(u32x2, vq_[j]); vv[4 * j] = blo(t.x); vv[4 * j + 1] = bhi(t.x); vv[4 * j + 2] = blo(t.y); vv[4 * j + 3] = bhi(t.y); }
    const float s0 = blo(shv.x), s1 = bhi(shv.x), s2 = blo(shv.y), s3 = bhi(shv.y);
    float r[8];
#pragma unroll
    for (int i = 0; i < 8; ++i) r[i] = (((kk[i + 1] * s3 + kk[i + 2] * s2) + kk[i + 3] * s1) + kk[i + 4] * s0) * vv[i];
    u32x4 pk = {cvtpk(r[7], r[6]), cvtpk(r[5], r[4]), cvtpk(r[3], r[2]), cvtpk(r[1], r[0])};
    const int o = 7 - l7, src = 16 * (((o >> 2) << 1) | (l3 >> 2)) + 4 * (o & 3) + (l3 & 3);
    u32x4 po;
#pragma unroll
    for (int i = 0; i < 4; ++i) po[i] = (unsigned)__builtin_amdgcn_ds_bpermute(src * 4, (int)pk[i]);
    st16_dev(rW, WS_KVT + (unsigned)((64 * pan + 8 * w + l3) * 4096 + (1984 - 64 * idx + 8 * l7) * 2), po);
    float xv[8];
#pragma unroll
    for (int i = 0; i < 4; ++i) { xv[2 * i] = blo(xrow[i]); xv[2 * i + 1] = bhi(xrow[i]); }
    float sm = ((xv[0] + xv[1]) + (xv[2] + xv[3])) + ((xv[4] + xv[5]) + (xv[6] + xv[7]));
    const float mean = wave_sum(sm) * (1.0f / D_);
    float sq = 0.f;
#pragma unroll
    for (int i = 0; i < 8; ++i) { const float d = xv[i] - mean; sq += d * d; }
    const float var = rbf(wave_sum(sq) * (1.0f / D_));
    const float sd = __builtin_sqrtf(rbf(var + 1e-5f));
    u32x2 g = {cvtpk(mean, sd), elo};
    st8_dev(rW, lane == 0 ? WS_ST + (unsigned)((8 * aa + w) * 8) : OOB, g);
  }
  STAMP(2);
  const int tg = w & 3, ch = w >> 2;
  unsigned ax[4], aw[3][4];
  {
    const int tr = 32 * tg + (lane & 31), m = G & 1;
#pragma unroll
    for (int j = 0; j < 4; ++j) {
      const int s = (j + tg) & 3;
      ax[j] = L0 + tr * 128 + (((2 * s + hh) ^ ((tr >> 1) & 7)) << 4);
#pragma unroll
      for (int i = 0; i < 3; ++i)
        aw[i][j] = L0 + 16384 + m * 12288 + (16 * s + 8 * hh + qq) * 192 + ((2 * ((3 * ch + i) ^ m) + (pp & 1)) << 4) + 8 * (pp >> 1);
    }
  }
  f32x16 acc[3];
  float cs[3] = {0.f, 0.f, 0.f};
#pragma unroll
  for (int i = 0; i < 3; ++i)
#pragma unroll
    for (int r = 0; r < 16; ++r) acc[i][r] = 0.f;
  const bf16x2_t ones = {(__bf16)1.0f, (__bf16)1.0f};
  bf16x8 xf[2], wf[2][3];
#define RD(c, j) do { const unsigned so_ = ((c) & 3) * SLOT;                                                      \
    _Pragma("unroll") for (int i = 0; i < 3; ++i) {                                                               \
      wf[(j) & 1][i] = cat(trd(aw[i][j] + so_), trd(aw[i][j] + so_ + 768));                                        \
      if (i == 0) xf[(j) & 1] = lds_ld<bf16x8>(ax[j] + so_); } SB(); } while (0)
#define PINACC() do { PIN(acc[0]); PIN(acc[1]); PIN(acc[2]); PIN(cs[0]); PIN(cs[1]); PIN(cs[2]); } while (0)
#define MM(j) do { _Pragma("unroll") for (int i = 0; i < 3; ++i) {                                                \
      acc[i] = MFMA32(wf[(j) & 1][i], xf[(j) & 1], acc[i]);                                                        \
      if ((j) == 0) { const u32x4 u_ = __builtin_bit_cast(u32x4, wf[0][i]);                                        \
        _Pragma("unroll") for (int e = 0; e < 4; ++e) { const unsigned t_ = u_[e];                                 \
          cs[i] = __builtin_amdgcn_fdot2_f32_bf16(__builtin_bit_cast(bf16x2_t, t_), ones, cs[i], false); } } }     \
    PINACC(); SB(); } while (0)
#define COMPUTE(c, D0, D1, D2, D3) do { RD(c, 0); if ((c) > 0) MM(3); D0; RD(c, 1); MM(0); D1; RD(c, 2); MM(1); D2; RD(c, 3); MM(2); D3; } while (0)
#define COMPUTE_N(c) COMPUTE(c, { DX(c + 3, 0); DW(c + 3, 0); }, DX(c + 3, 1), DW(c + 3, 1), DW(c + 3, 2))
  const int kq = w & 3, tt2 = w >> 2, r64 = 4 * pan + (idx >> 3), ct = idx & 7;
  const int ks0 = 22 * kq - (kq == 3 ? 1 : 0);
  const unsigned vd = (unsigned)((16 * ks0 + 8 * tt2 + l3) * 1024 + ct * 128 + ((l7 ^ (((l3 >> 1) & 1) << 2)) << 4));
#define WDU(j, lds) dma16(rD, vd, (j) * 16384, L0 + (lds) + tt2 * 1024)
#define WDA(j) WDU(j, ((j) * 4 + kq) * 2048)

  WAIT_BAR(17); STAMP(16); COMPUTE(0, {}, {}, {}, {}); STAMP(17);
  WAIT_BAR(12); STAMP(18); COMPUTE_N(1); STAMP(20);
  WAIT_BAR(12); STAMP(21); COMPUTE_N(2); STAMP(22);
  WAIT_BAR(10); STAMP(23);
  if (tid == 0) { u32x2 e = {elo, ehi}; st8_dev(rW, WS_FA + (unsigned)(aa * 128), e); }
  COMPUTE_N(3);
  STAMP(3);
  WAIT_BAR(10); COMPUTE_N(4);
  WAIT_BAR(10);
  const unsigned vst = WS_ST + (unsigned)((128 * rt + 32 * tg + (lane & 31)) * 8);
  u32x2 stg, fa;
  COMPUTE(5, { stg = ld8_dev(rW, vst); WDA(0); }, WDA(1), WDA(2), WDA(3));
  WAIT_BAR(10);
  const unsigned vfa = WS_FA + (unsigned)((32 * pan + (lane & 31)) * 128);
  COMPUTE(6, { fa = ld8_dev(rW, vfa); WDA(4); }, { WDA(5); WDA(6); }, { WDA(7); WDA(8); }, WDA(9));
  asm volatile("s_waitcnt vmcnt(11) lgkmcnt(0)\n\ts_barrier" : "+v"(stg)::"memory");
  STAMP(4);
  RD(7, 0); MM(3); WDA(10); RD(7, 1); MM(0); WDA(11);
#pragma unroll
  for (int i = 0; i < 3; ++i) lds_st<float>(L0 + L_CV + ((ch * 3 + i) * 4 + tg) * 256 + lane * 4, cs[i]);
  RD(7, 2); MM(1); WDA(12);
  LBAR();
  u32x4 hcur, hprv;
  {
    float c4[3][4];
    RD(7, 3);
#pragma unroll
    for (int i = 0; i < 3; ++i)
#pragma unroll
      for (int t = 0; t < 4; ++t) c4[i][t] = lds_ld<float>(L0 + L_CV + ((ch * 3 + i) * 4 + t) * 256 + lane * 4);
    SB();
    MM(2); WDA(13);
    LBAR();
    MM(3);
    hcur = ld16(rHk, (unsigned)((2 * aa + cch) * 4096 + ce * 16));
    hprv = ld16(rHk, ce ? (unsigned)((2 * aa + cch) * 4096 + ce * 16 - 16) : OOB);
    stg = spin_tag(rW, vst, stg, elo);
    const unsigned mn = (stg.x & 0xffffu) ^ 0x8000u;
    const u32x4 bm = {mn | (mn << 16), mn, 0u, 0u};
#pragma unroll
    for (int i = 0; i < 3; ++i) {
      const float c = (c4[i][0] + c4[i][1]) + (c4[i][2] + c4[i][3]);
      const float c1 = c - rbf(c), c2 = c1 - rbf(c1);
      const u32x4 am = {cvtpk(c, c1), cvtpk(c2, 0.f), 0u, 0u};
      acc[i] = MFMA32(__builtin_bit_cast(bf16x8, am), __builtin_bit_cast(bf16x8, bm), acc[i]);
    }
    PINACC();
  }
  STAMP(5);
  u32x4 urow;
  {
    const float sd = bhi(stg.x), rs = __builtin_amdgcn_rcpf(sd);
    const float k0 = rs * -2.3022081983f, k1 = k0 * rs * rs * 0.044715f, s2 = sd * sd;
#pragma unroll
    for (int i = 0; i < 3; ++i) {
      float hv[8];
#pragma unroll
      for (int r = 0; r < 8; ++r) {
        const float g = acc[i][r], u = acc[i][r + 8];
        const float e = __builtin_amdgcn_exp2f(g * __builtin_fmaf(g * g, k1, k0));
        hv[r] = (g * u) * __builtin_amdgcn_rcpf(__builtin_fmaf(e, s2, s2));
      }
      const u32x4 hp = {cvtpk(hv[0], hv[1]), cvtpk(hv[2], hv[3]), cvtpk(hv[4], hv[5]), cvtpk(hv[6], hv[7])};
      const int ks = 6 * cg + 3 * ch + i;
      st16_dev(rW, ks < 86 ? WS_H + (unsigned)(((4 * rt + tg) * 86 + ks) * 1024 + lane * 16) : OOB, hp);
      if (i == 0) {
        asm volatile("s_waitcnt vmcnt(13)" : "+v"(fa)::"memory");
        spin_flags(rW, vfa, fa, elo, ehi);
        urow = ld16_dev(rW, WS_KVT + (unsigned)((2 * aa + cch) * 4096 + ce * 16), 0);
      }
    }
  }
  STAMP(6);
  asm volatile("s_waitcnt vmcnt(2)" : "+v"(urow), "+v"(hcur), "+v"(hprv)::"memory");
  {
    const unsigned hb = L0 + L_CV + cch * 16640;
    auto put = [&](u32x4 pv, u32x4 cu, int blk) {
      const unsigned a0 = __builtin_amdgcn_alignbit(pv[1], pv[0], 16), a1 = __builtin_amdgcn_alignbit(pv[2], pv[1], 16),
                     a2 = __builtin_amdgcn_alignbit(pv[3], pv[2], 16), a3 = __builtin_amdgcn_alignbit(cu[0], pv[3], 16),
                     a4 = __builtin_amdgcn_alignbit(cu[1], cu[0], 16);
      lds_st<u32x4>(hb + blk * 16, u32x4{a0, a1, a2, a3});
      lds_st<u32x4>(hb + 4160 + blk * 16, u32x4{pv[1], pv[2], pv[3], cu[0]});
      lds_st<u32x4>(hb + 8320 + blk * 16, u32x4{a1, a2, a3, a4});
      lds_st<u32x4>(hb + 12480 + blk * 16, u32x4{pv[2], pv[3], cu[0], cu[1]});
    };
    put(hprv, hcur, ce + 1);
    const u32x4 zero4 = {0u, 0u, 0u, 0u};
    if (ce == 0) put(zero4, zero4, 0);
    if (ce == 255) put(hcur, zero4, 257);
    lds_st<u32x4>(L0 + L_UR + cch * 4608 + ce * 16, urow);
    if (ce < 30) lds_st<u32x4>(L0 + L_UR + cch * 4608 + 4096 + ce * 16, zero4);
    if (tid < 2) lds_st<u32x4>(L0 + L_SYNC + tid * 16, zero4);
  }
  LBAR();
  STAMP(7);
  const unsigned ad = L0 + (8 * hh + qq) * 128 + ((((4 * ((qq >> 1) & 1)) + 2 * (G & 1) + (pp >> 1))) << 4) + 8 * (pp & 1);
#define WDF(base, dt) cat(trd((ad ^ ((dt) * 64)) + (base)), trd((ad ^ ((dt) * 64)) + (base) + 512))
  bf16x8 wpk[6][2];
#pragma unroll
  for (int j = 0; j < 6; ++j) { wpk[j][0] = WDF((j * 4 + kq) * 2048, 0); wpk[j][1] = WDF((j * 4 + kq) * 2048, 1); }
  f32x4 ya[8];
#pragma unroll
  for (int c = 0; c < 8; ++c) ya[c] = f32x4{0.f, 0.f, 0.f, 0.f};
  const int vch = (w >> 1) & 1, vrh = w & 1, c15 = lane & 15;
  const unsigned cA = L0 + L_CV + vch * 16640 + (c15 & 3) * 4160 + (4 * (c15 >> 2) + 8 * G + 128 * vrh) * 2;
  const unsigned cB = L0 + L_UR + vch * 4608 + (240 - 16 * c15 + 8 * G + 128 * vrh) * 2;
  bf16x8 bfr[2][8], afr[2][2];
#define VLD8(a) (*(const volatile LAS s16x4*)(size_t)(a))
#define CA(set, rho, p) do { afr[set][0] = cat(VLD8(cA + (rho) * 64 + (p) * 512), VLD8(cA + (rho) * 64 + (p) * 512 + 8));                 \
    afr[set][1] = cat(VLD8(cA + (rho) * 64 + (7 - (p)) * 512), VLD8(cA + (rho) * 64 + (7 - (p)) * 512 + 8)); } while (0)
#define CB(rho, mu) bfr[(rho) & 1][mu] = lds_ld<bf16x8>(cB + (rho) * 64 + (7 - (mu)) * 512)
#define PINY() do { _Pragma("unroll") for (int c = 0; c < 8; ++c) PIN(ya[c]); } while (0)
#define CPAIR(rho, p) do { if ((p) < 3) CA(((p) + 1) & 1, rho, (p) + 1); else if ((rho) < 3) CA(0, (rho) + 1, 0);                          \
    if ((rho) < 3) { CB((rho) + 1, 2 * (p)); CB((rho) + 1, 2 * (p) + 1); } SB();                                                          \
    _Pragma("unroll") for (int mu = 0; mu + (p) < 8; ++mu) {                                                                              \
      ya[(p) + mu] = MFMA16(afr[(p) & 1][0], bfr[(rho) & 1][mu], ya[(p) + mu]);                                                            \
      if (mu <= (p)) ya[7 - (p) + mu] = MFMA16(afr[(p) & 1][1], bfr[(rho) & 1][mu], ya[7 - (p) + mu]); }                                    \
    PINY(); SB(); } while (0)
#define CONV(rho) do { CPAIR(rho, 0); CPAIR(rho, 1); CPAIR(rho, 2); CPAIR(rho, 3); } while (0)
  STAMP(24);
  if (w < 4) {
    CA(0, 0, 0);
#pragma unroll
    for (int mu = 0; mu < 8; ++mu) CB(0, mu);
    SB();
    CONV(0); CONV(1);
  }
  STAMP(25);
  WAIT_BAR(0);
  if (tid == 256 && cg < 15) { u32x2 e = {elo, ehi}; st8_dev(rW, WS_FH + (unsigned)((rt * 16 + cg) * 128), e); }
  STAMP(8);
#pragma unroll
  for (int j = 0; j < 6; ++j) WDU(14 + j, (j * 4 + kq) * 2048);
  const int ftok = 64 * r64 + 8 * w + l3;
  const unsigned vxo = (unsigned)(ftok * 1024 + ct * 128 + l7 * 16), vs2 = WS_ST + (unsigned)(ftok * 8);
  u32x4 xo = ld16(rX, vxo);
  u32x2 st2 = ld8_dev(rW, vs2);
  STAMP(26);
  if (w < 4) { CONV(2); CONV(3); }
  STAMP(27);
  LBAR();
  WDU(20, L_WD4 + kq * 2048); WDU(21, L_WD4 + (4 + kq) * 2048);
  if (w < 4) {
#pragma unroll
    for (int c = 0; c < 4; ++c) lds_st<f32x4>(L0 + L_CV + (vch * 2 + (vrh ^ 1)) * 4096 + c * 1024 + lane * 16, sel4(vrh, ya[c], ya[4 + c]));
  }
  LBAR();
  if (w < 4) {
    const unsigned xb = L0 + L_CV + (vch * 2 + vrh) * 4096;
    const float dsc = vch ? bhi(dpair) : blo(dpair);
    u32x2 zp[4];
#pragma unroll
    for (int c = 0; c < 4; ++c) {
      const f32x4 o = lds_ld<f32x4>(xb + c * 1024 + lane * 16), y = sel4(vrh, ya[4 + c], ya[c]);
      const u32x2 ur = lds_ld<u32x2>(L0 + L_UR + vch * 4608 + (2044 - 256 * (4 * vrh + c) - 16 * c15 - 4 * G) * 2);
      const float z0 = rbf(y[0] + o[0]) + rbf(bhi(ur.y) * dsc), z1 = rbf(y[1] + o[1]) + rbf(blo(ur.y) * dsc);
      const float z2 = rbf(y[2] + o[2]) + rbf(bhi(ur.x) * dsc), z3 = rbf(y[3] + o[3]) + rbf(blo(ur.x) * dsc);
      zp[c] = u32x2{cvtpk(z0, z1), cvtpk(z2, z3)};
    }
#pragma unroll
    for (int c = 0; c < 4; ++c) lds_st<u32x2>(xb + c * 512 + c15 * 32 + G * 8, zp[c]);
#pragma unroll
    for (int i = 0; i < 2; ++i)
      st16_dev(rW, WS_ZT + (unsigned)((2 * aa + vch) * 4096 + vrh * 2048 + i * 1024 + lane * 16), lds_ld<u32x4>(xb + i * 1024 + lane * 16));
  }
  STAMP(9);
  if (w >= 4) {
    const int gf = ks0 / 6, gl = (ks0 + 20 + (kq < 2)) / 6, gi = min(gf + (lane & 7), gl);
    const unsigned vfh = WS_FH + (unsigned)(((r64 >> 1) * 16 + gi) * 128);
    u32x2 fh = ld8_dev(rW, vfh); WAITV(0); PIN(fh);
    spin_flags(rW, vfh, fh, elo, ehi);
    *(volatile LAS unsigned*)(size_t)(L0 + L_SYNC + kq * 4) = 1u;
  } else {
    for (unsigned n = 0; *(volatile LAS unsigned*)(size_t)(L0 + L_SYNC + kq * 4) == 0u && n < SPIN_MAX * 16; ++n) __builtin_amdgcn_s_sleep(1);
  }
  STAMP(10);
  u32x4 hf[22];
  const unsigned vh = WS_H + (unsigned)(((2 * r64 + tt2) * 86 + ks0) * 1024 + lane * 16);
#define HL(j) hf[j] = ld16_dev(rW, ((j) < 21 || kq < 2) ? vh : OOB, (j) * 1024)
#pragma unroll
  for (int j = 0; j < 10; ++j) HL(j);
  STAMP(28);
  const unsigned vfz = WS_FZ + (unsigned)((32 * ct + (lane & 31)) * 128);
  u32x2 fz;
  f32x16 fa0, fa1;
#pragma unroll
  for (int r = 0; r < 16; ++r) { fa0[r] = 0.f; fa1[r] = 0.f; }
#define G2M(j, N, A0, A1) do { asm volatile("s_waitcnt vmcnt(" #N ")" : "+v"(hf[j])::"memory");                     \
    fa0 = MFMA32(A0, __builtin_bit_cast(bf16x8, hf[j]), fa0); fa1 = MFMA32(A1, __builtin_bit_cast(bf16x8, hf[j]), fa1); PIN(fa0); PIN(fa1); SB(); \
    if ((j) + 10 < 22) HL(((j) + 10) % 22); } while (0)
#define G2P(j, N) G2M(j, N, wpk[j][0], wpk[j][1])
#define G2R(slot, base) do { wpk[slot][0] = WDF(base, 0); wpk[slot][1] = WDF(base, 1); } while (0)
#define UA(j) (((j) * 4 + kq) * 2048)
  G2P(0, 9);
  STAMP(29);
  if (w < 4 && lane == 0) {
    if (__hip_atomic_fetch_add((LAS unsigned*)(size_t)(L0 + L_SYNC + 16), 1u, __ATOMIC_RELAXED, __HIP_MEMORY_SCOPE_WORKGROUP) == 3u) {
      u32x2 e = {elo, ehi}; st8_dev(rW, WS_FZ + (unsigned)(aa * 128), e); }
  }
  G2P(1, 9); G2P(2, 9); G2P(3, 9);
  G2R(0, UA(6));  G2P(4, 9);
  G2R(1, UA(7));  G2P(5, 9);
  G2R(2, UA(8));  G2M(6, 9, wpk[0][0], wpk[0][1]);
  G2R(3, UA(9));  G2M(7, 9, wpk[1][0], wpk[1][1]);
  G2R(0, UA(10)); G2M(8, 9, wpk[2][0], wpk[2][1]);
  G2R(1, UA(11)); G2M(9, 9, wpk[3][0], wpk[3][1]);
  G2R(2, UA(12)); G2M(10, 9, wpk[0][0], wpk[0][1]);
  G2R(3, UA(13)); G2M(11, 9, wpk[1][0], wpk[1][1]);
  asm volatile("s_barrier" ::: "memory");
  G2R(0, UA(0));  G2M(12, 9, wpk[2][0], wpk[2][1]);
  G2R(1, UA(1));  G2M(13, 8, wpk[3][0], wpk[3][1]);
  G2R(2, UA(2));  G2M(14, 7, wpk[0][0], wpk[0][1]);
  G2R(3, UA(3));  G2M(15, 6, wpk[1][0], wpk[1][1]);
  fz = ld8_dev(rW, vfz);
  G2R(0, UA(4));  G2M(16, 6, wpk[2][0], wpk[2][1]);
  G2R(1, UA(5));  G2M(17, 5, wpk[3][0], wpk[3][1]);
  G2R(2, L_WD4 + kq * 2048);       G2M(18, 4, wpk[0][0], wpk[0][1]);
  G2R(3, L_WD4 + (4 + kq) * 2048); G2M(19, 3, wpk[1][0], wpk[1][1]);
  G2M(20, 2, wpk[2][0], wpk[2][1]);
  G2M(21, 1, wpk[3][0], wpk[3][1]);
  STAMP(11);
  asm volatile("s_waitcnt vmcnt(0)" : "+v"(xo), "+v"(st2), "+v"(fz)::"memory");
  spin_flags(rW, vfz, fz, elo, ehi);
  u32x4 zr = ld16_dev(rW, WS_ZT + (unsigned)((64 * ct + 8 * w + l3) * 4096 + r64 * 128 + l7 * 16), 0);
  st2 = spin_tag(rW, vs2, st2, elo);
  STAMP(30);
  LBAR();
  STAMP(31);
  {
    const int tok = 32 * tt2 + (lane & 31);
    const unsigned pb = L0 + kq * 16384 + tok * 256;
#pragma unroll
    for (int i = 0; i < 4; ++i) {
      lds_st<f32x4>(pb + (((2 * i + hh) ^ (tok & 7)) << 4), f32x4{fa0[4 * i], fa0[4 * i + 1], fa0[4 * i + 2], fa0[4 * i + 3]});
      lds_st<f32x4>(pb + (((8 + 2 * i + hh) ^ (tok & 7)) << 4), f32x4{fa1[4 * i], fa1[4 * i + 1], fa1[4 * i + 2], fa1[4 * i + 3]});
    }
    asm volatile("s_waitcnt vmcnt(0)" : "+v"(zr)::"memory");
    const int r = 8 * w + l3, f = ((r >> 1) & 1) | (((r >> 3) & 3) << 1);
    lds_st<u32x4>(L0 + L_ZT + r * 128 + ((l7 ^ f) << 4), zr);
  }
  LBAR();
  STAMP(12);
  {
    u32x2 zt[2];
    const int o = 4 * (G >> 1) + pp;
#pragma unroll
    for (int j = 0; j < 2; ++j) { const int r = 8 * o + 4 * j + qq, f = ((r >> 1) & 1) | (((r >> 3) & 3) << 1);
      zt[j] = __builtin_bit_cast(u32x2, trd(L0 + L_ZT + r * 128 + ((w ^ f) << 4) + 8 * (G & 1))); }
    const int tl = 8 * w + l3;
    f32x4 p0 = {0.f, 0.f, 0.f, 0.f}, p1 = p0;
#pragma unroll
    for (int k = 0; k < 4; ++k) {
      p0 += lds_ld<f32x4>(L0 + k * 16384 + tl * 256 + (((2 * l7) ^ (tl & 7)) << 4));
      p1 += lds_ld<f32x4>(L0 + k * 16384 + tl * 256 + (((2 * l7 + 1) ^ (tl & 7)) << 4));
    }
    const int src = 16 * (((l7 >> 2) << 1) | (l3 >> 2)) + 4 * (l7 & 3) + (l3 & 3);
    u32x4 zq;
    zq[0] = (unsigned)__builtin_amdgcn_ds_bpermute(src * 4, (int)zt[0].x); zq[1] = (unsigned)__builtin_amdgcn_ds_bpermute(src * 4, (int)zt[0].y);
    zq[2] = (unsigned)__builtin_amdgcn_ds_bpermute(src * 4, (int)zt[1].x); zq[3] = (unsigned)__builtin_amdgcn_ds_bpermute(src * 4, (int)zt[1].y);
    const float mean = blo(st2.x), rs = __builtin_amdgcn_rcpf(bhi(st2.x));
    float o8[8];
#pragma unroll
    for (int i = 0; i < 8; ++i) {
      const float xv = (i & 1) ? bhi(xo[i >> 1]) : blo(xo[i >> 1]), zv = (i & 1) ? bhi(zq[i >> 1]) : blo(zq[i >> 1]);
      const float F = i < 4 ? p0[i & 3] : p1[i & 3];
      const float gs = F * __builtin_amdgcn_rcpf(1.0f + __builtin_amdgcn_exp2f(F * -1.4426950408889634f));
      o8[i] = __builtin_fmaf((xv - mean) * rs * zv, gs, xv);
    }
    const u32x4 ov = {cvtpk(o8[0], o8[1]), cvtpk(o8[2], o8[3]), cvtpk(o8[4], o8[5]), cvtpk(o8[6], o8[7])};
    *(GAS u32x4*)((GAS char*)out + vxo) = ov;
  }
  if (b == 0 && w == 0) {
#pragma unroll
    for (int i = 0; i < 4; ++i) {
      const unsigned v = WS_FA + (unsigned)((64 * i + lane) * 128);
      u32x2 f = ld8_dev(rW, v); WAITV(0); PIN(f);
      spin_flags(rW, v, f, elo, ehi);
    }
    if (lane == 0) { u32x2 e = {elo, ehi}; st8_dev(rW, WS_GEN, e); }
  }
  WAITV(0); STAMP(13);
  { const u64 tgt = prb0 + 4000 + 4 * (prb1 - prb0); while (__builtin_amdgcn_s_memrealtime() < tgt) __builtin_amdgcn_s_sleep(1); }
}

extern "C" void kernel_launch(void* const* d_in, const int* in_sizes, int n_in, void* d_out, int out_size, void* d_ws, size_t ws_size,
                              hipStream_t stream) {
  (void)in_sizes; (void)n_in; (void)out_size; (void)ws_size;
  (void)hipFuncSetAttribute((const void*)h3_geglu_fused, hipFuncAttributeMaxDynamicSharedMemorySize, LDS_BYTES);
  h3_geglu_fused<<<256, 512, LDS_BYTES, stream>>>((const u16*)d_in[0], (const u16*)d_in[1], (const u16*)d_in[2], (const u16*)d_in[3],
                                                  (const u16*)d_in[4], (const u16*)d_in[5], (const u16*)d_in[7], (const u16*)d_in[8],
                                                  (const u16*)d_in[9], (u16*)d_out, (char*)d_ws);
}
```

```cpp
#include <hip/hip_runtime.h>
#include <hip/hip_bf16.h>

typedef unsigned short u16;
typedef unsigned long long u64;
typedef __attribute__((ext_vector_type(8))) short bf16x8;
typedef __attribute__((ext_vector_type(4))) short s16x4;
typedef __attribute__((ext_vector_type(2))) __bf16 bf16x2_t;
typedef __attribute__((ext_vector_type(4))) float f32x4;
typedef __attribute__((ext_vector_type(16))) float f32x16;
typedef __attribute__((ext_vector_type(4))) unsigned u32x4;
typedef __attribute__((ext_vector_type(2))) unsigned u32x2;
#define LAS __attribute__((address_space(3)))
#define GAS __attribute__((address_space(1)))
typedef __amdgpu_buffer_rsrc_t rsrc_t;

constexpr int S_ = 2048, D_ = 512, DF = 1376;
constexpr unsigned WS_GEN = 0, WS_FA = 4096, WS_FH = WS_FA + 256 * 128, WS_FZ = WS_FH + 256 * 128;
constexpr unsigned WS_ST = WS_FZ + 256 * 128;
constexpr unsigned WS_KVT = 1u << 20;
constexpr unsigned WS_ZT = WS_KVT + (2u << 20);
constexpr unsigned WS_H = WS_ZT + (2u << 20);
constexpr unsigned WS_BYTES = WS_H + 64 * 86 * 1024;
constexpr int SLOT = 40960, L_KA = 3 * SLOT, L_VA = L_KA + 9216;
constexpr int L_SYNC = 163840 - 32;
constexpr int L_CV = 114688, L_UR = L_CV + 33280, L_WD4 = 131072, L_ZT = 65536;
constexpr int LDS_BYTES = 163840;
constexpr unsigned OOB = 0x80000000u;
constexpr unsigned SPIN_MAX = 1u << 18;

#define MFMA32(a, b, c) __builtin_amdgcn_mfma_f32_32x32x16_bf16(a, b, c, 0, 0, 0)
#define MFMA16(a, b, c) __builtin_amdgcn_mfma_f32_16x16x32_bf16(a, b, c, 0, 0, 0)
#define WAIT_BAR(N) asm volatile("s_waitcnt vmcnt(" #N ") lgkmcnt(0)\n\ts_barrier" ::: "memory")
#define LBAR() asm volatile("s_waitcnt lgkmcnt(0)\n\ts_barrier" ::: "memory")
#define WAITV(N) asm volatile("s_waitcnt vmcnt(" #N ")" ::: "memory")
#define SB() __builtin_amdgcn_sched_barrier(0)

__device__ __forceinline__ rsrc_t mk_rsrc(const void* p, unsigned bytes) {
  return __builtin_amdgcn_make_buffer_rsrc((void*)p, (short)0, (int)bytes, 0x00020000);
}
__device__ __forceinline__ void dma16(rsrc_t rs, unsigned voff, unsigned soff, unsigned lds) {
  asm volatile("s_mov_b32 m0, %0\n\ts_nop 0\n\tbuffer_load_dwordx4 %1, %2, %3 offen lds" ::"s"(lds), "v"(voff), "s"(rs), "s"(soff) : "memory");
}
__device__ __forceinline__ u32x4 ld16(rsrc_t rs, unsigned voff) {
  u32x4 r; asm volatile("buffer_load_dwordx4 %0, %1, %2, 0 offen" : "=v"(r) : "v"(voff), "s"(rs) : "memory"); return r;
}
__device__ __forceinline__ u32x4 ld16_dev(rsrc_t rs, unsigned voff, unsigned soff) {
  u32x4 r; asm volatile("buffer_load_dwordx4 %0, %1, %2, %3 offen sc1" : "=v"(r) : "v"(voff), "s"(rs), "s"(soff) : "memory"); return r;
}
__device__ __forceinline__ u32x2 ld8(rsrc_t rs, unsigned voff) {
  u32x2 r; asm volatile("buffer_load_dwordx2 %0, %1, %2, 0 offen" : "=v"(r) : "v"(voff), "s"(rs) : "memory"); return r;
}
__device__ __forceinline__ u32x2 ld8_dev(rsrc_t rs, unsigned voff) {
  u32x2 r; asm volatile("buffer_load_dwordx2 %0, %1, %2, 0 offen sc1" : "=v"(r) : "v"(voff), "s"(rs) : "memory"); return r;
}
#define PIN(x) asm volatile("" : "+v"(x))
__device__ __forceinline__ void st16_dev(rsrc_t rs, unsigned voff, u32x4 v) { __builtin_amdgcn_raw_buffer_store_b128(v, rs, voff, 0, 16); }
__device__ __forceinline__ void st8_dev(rsrc_t rs, unsigned voff, u32x2 v) { __builtin_amdgcn_raw_buffer_store_b64(v, rs, voff, 0, 16); }

__device__ __forceinline__ unsigned cvtpk(float lo, float hi) {
  bf16x2_t r = {(__bf16)lo, (__bf16)hi};
  return __builtin_bit_cast(unsigned, r);
}
__device__ __forceinline__ float blo(unsigned w) { return __uint_as_float(w << 16); }
__device__ __forceinline__ float bhi(unsigned w) { return __uint_as_float(w & 0xffff0000u); }
__device__ __forceinline__ float rbf(float f) { return blo(cvtpk(f, f)); }
__device__ __forceinline__ s16x4 trd(unsigned a) { return __builtin_amdgcn_ds_read_tr16_b64_v4i16((LAS s16x4*)(size_t)a); }
__device__ __forceinline__ bf16x8 cat(s16x4 a, s16x4 b) { return __builtin_shufflevector(a, b, 0, 1, 2, 3, 4, 5, 6, 7); }
template <class T> __device__ __forceinline__ T lds_ld(unsigned a) { return *(const LAS T*)(size_t)a; }
template <class T> __device__ __forceinline__ void lds_st(unsigned a, T v) { *(LAS T*)(size_t)a = v; }
template <int CTRL> __device__ __forceinline__ float dpp_add(float x) {
  return x + __uint_as_float(__builtin_amdgcn_update_dpp(0u, __float_as_uint(x), CTRL, 0xf, 0xf, true));
}
__device__ __forceinline__ float wave_sum(float x) {
  x = dpp_add<0x128>(x); x = dpp_add<0x124>(x); x = dpp_add<0x122>(x); x = dpp_add<0x121>(x);
  auto a = __builtin_amdgcn_permlane16_swap(__float_as_uint(x), __float_as_uint(x), false, false);
  x = __uint_as_float(a[0]) + __uint_as_float(a[1]);
  auto b = __builtin_amdgcn_permlane32_swap(__float_as_uint(x), __float_as_uint(x), false, false);
  return __uint_as_float(b[0]) + __uint_as_float(b[1]);
}
__device__ __forceinline__ f32x4 sel4(bool c, f32x4 a, f32x4 b) {
  asm volatile("" : "+v"(a), "+v"(b));
  return f32x4{c ? a[0] : b[0], c ? a[1] : b[1], c ? a[2] : b[2], c ? a[3] : b[3]};
}
__device__ __forceinline__ void spin_flags(rsrc_t rs, unsigned voff, u32x2 f, unsigned lo, unsigned hi) {
  for (unsigned n = 0; !__all(f.x == lo && f.y == hi) && n < SPIN_MAX; ++n) {
    __builtin_amdgcn_s_sleep(1);
    f = ld8_dev(rs, voff); WAITV(0); PIN(f);
  }
}
__device__ __forceinline__ u32x2 spin_tag(rsrc_t rs, unsigned voff, u32x2 f, unsigned tag) {
  for (unsigned n = 0; !__all(f.y == tag) && n < SPIN_MAX; ++n) {
    __builtin_amdgcn_s_sleep(1);
    f = ld8_dev(rs, voff); WAITV(0); PIN(f);
  }
  return f;
}
#define STAMP(i)

__global__ __launch_bounds__(512, 2) void h3_geglu_fused(const u16* __restrict__ x, const u16* __restrict__ kin, const u16* __restrict__ vin,
                                                         const u16* __restrict__ shk, const u16* __restrict__ ssk, const u16* __restrict__ ssd,
                                                         const u16* __restrict__ wg, const u16* __restrict__ wu, const u16* __restrict__ wd,
                                                         u16* __restrict__ out, char* __restrict__ ws) {
  extern __shared__ __attribute__((aligned(16))) char smem[];
  const unsigned L0 = (unsigned)(size_t)(LAS char*)smem;
  const int tid = threadIdx.x, lane = tid & 63;
  const int w = __builtin_amdgcn_readfirstlane(tid >> 6);
  STAMP(0);
  const int b = blockIdx.x, xcd = b & 7, idx = b >> 3, rh = xcd & 1, q4 = xcd >> 1;
  const int pan = 4 * rh + q4, aa = 32 * pan + idx;
  const int l3 = lane >> 3, l7 = lane & 7, G = lane >> 4, qq = (lane >> 2) & 3, pp = lane & 3, hh = lane >> 5;
  const rsrc_t rX = mk_rsrc(x, S_ * D_ * 2), rK = mk_rsrc(kin, S_ * D_ * 2), rV = mk_rsrc(vin, S_ * D_ * 2);
  const rsrc_t rW = mk_rsrc(ws, WS_BYTES);
  {
    const unsigned kb = (unsigned)((64 * idx + l3) * 1024 + pan * 128), fl = (unsigned)(l7 ^ ((l3 >> 1) & 1));
    dma16(rK, kb + ((fl ^ (((w + 1) & 3) << 1)) << 4), w * 8192, L0 + L_KA + (w + 1) * 1024);
    dma16(rV, kb + ((fl ^ ((w & 3) << 1)) << 4), w * 8192, L0 + L_VA + w * 1024);
    if (w == 0) dma16(rK, (idx == 0) ? OOB : kb - 8192 + (fl << 4), 0, L0 + L_KA);
  }
  u32x4 xrow = ld16(rX, (unsigned)((8 * aa + w) * 1024 + lane * 16));
  u32x2 shv = ld8(mk_rsrc(shk, D_ * 4 * 2), (unsigned)((64 * pan + 8 * w + 4 * (G & 1) + pp) * 8));
  u32x2 gen = ld8_dev(rW, WS_GEN);
  const int cg = idx < 24 ? ((q4 * 7 + 1) >> 1) + (idx >> 3) : idx < 28 ? (q4 < 2 ? 3 : 10) : 14 + ((idx >> 1) & 1);
  const int rt = 8 * rh + (idx < 24 ? (idx & 7) : idx < 28 ? (q4 & 1) * 4 + (idx & 3) : 2 * q4 + (idx & 1));
  const unsigned vx = (cg < 15) ? (unsigned)((128 * rt + 8 * w + l3) * 1024 + ((l7 ^ ((4 * w + (l3 >> 1)) & 7)) << 4)) : OOB;
#define DX(c, j) dma16(rX, vx, (c) * 128 + (j) * 65536, L0 + ((c) & 3) * SLOT + (w + 8 * (j)) * 1024)
  DX(0, 0); DX(0, 1);
  unsigned vw[3];
#pragma unroll
  for (int j = 0; j < 3; ++j) {
    const int n2 = w + 8 * j, m = n2 >= 12;
    const unsigned P = 64 * (n2 - 12 * m) + lane, k = (P * 43691u) >> 19, col = 96 * cg + 8 * ((P - 12 * k) ^ (2 * m));
    vw[j] = (col < DF) ? k * (DF * 2) + col * 2 : OOB;
  }
  const rsrc_t rG = mk_rsrc(wg, D_ * DF * 2), rU = mk_rsrc(wu, D_ * DF * 2), rM = mk_rsrc(w < 4 ? wg : wu, D_ * DF * 2);
#define DW(c, j) dma16((j) == 0 ? rG : (j) == 1 ? rM : rU, vw[j], (c) * 64 * DF * 2, L0 + ((c) & 3) * SLOT + 16384 + (w + 8 * (j)) * 1024)
#define CHUNK(c) do { DX(c, 0); DW(c, 0); DX(c, 1); DW(c, 1); DW(c, 2); } while (0)
  STAMP(14);
  DW(0, 0); DW(0, 1); DW(0, 2);
  CHUNK(1); CHUNK(2);
  STAMP(15);
  unsigned dpair = ((const unsigned*)ssd)[aa];
  asm volatile("" : "+s"(dpair));
  const rsrc_t rHk = mk_rsrc(ssk, D_ * S_ * 2), rD = mk_rsrc(wd, DF * D_ * 2);
  asm volatile("s_waitcnt vmcnt(15)\n\ts_barrier" : "+v"(gen), "+v"(xrow), "+v"(shv)::"memory");
  STAMP(1);
  s16x4 kq_[3], vq_[2];
  {
    const int o = 4 * (G >> 1) + pp;
#pragma unroll
    for (int j = 0; j < 3; ++j) { const int r = 8 * o + 4 + 4 * j + qq, f = ((r >> 1) & 1) | (((r >> 3) & 3) << 1);
      kq_[j] = trd(L0 + L_KA + r * 128 + ((w ^ f) << 4) + 8 * (G & 1)); }
#pragma unroll
    for (int j = 0; j < 2; ++j) { const int r = 8 * o + 4 * j + qq, f = ((r >> 1) & 1) | (((r >> 3) & 3) << 1);
      vq_[j] = trd(L0 + L_VA + r * 128 + ((w ^ f) << 4) + 8 * (G & 1)); }
  }
  LBAR();
  CHUNK(3);
  const unsigned elo = __builtin_amdgcn_readfirstlane(gen.x) + 1u;
  const unsigned ehi = __builtin_amdgcn_readfirstlane(gen.y) + (elo == 0u ? 1u : 0u);
  const int cch = w >> 2, ce = tid & 255;
  {
    float kk[12], vv[8];
#pragma unroll
    for (int j = 0; j < 3; ++j) { const u32x2 t = __builtin_bit_cast(u32x2, kq_[j]); kk[4 * j] = blo(t.x); kk[4 * j + 1] = bhi(t.x); kk[4 * j + 2] = blo(t.y); kk[4 * j + 3] = bhi(t.y); }
#pragma unroll
    for (int j = 0; j < 2; ++j) { const u32x2 t = __builtin_bit_cast(u32x2, vq_[j]); vv[4 * j] = blo(t.x); vv[4 * j + 1] = bhi(t.x); vv[4 * j + 2] = blo(t.y); vv[4 * j + 3] = bhi(t.y); }
    const float s0 = blo(shv.x), s1 = bhi(shv.x), s2 = blo(shv.y), s3 = bhi(shv.y);
    float r[8];
#pragma unroll
    for (int i = 0; i < 8; ++i) r[i] = (((kk[i + 1] * s3 + kk[i + 2] * s2) + kk[i + 3] * s1) + kk[i + 4] * s0) * vv[i];
    u32x4 pk = {cvtpk(r[7], r[6]), cvtpk(r[5], r[4]), cvtpk(r[3], r[2]), cvtpk(r[1], r[0])};
    const int o = 7 - l7, src = 16 * (((o >> 2) << 1) | (l3 >> 2)) + 4 * (o & 3) + (l3 & 3);
    u32x4 po;
#pragma unroll
    for (int i = 0; i < 4; ++i) po[i] = (unsigned)__builtin_amdgcn_ds_bpermute(src * 4, (int)pk[i]);
    st16_dev(rW, WS_KVT + (unsigned)((64 * pan + 8 * w + l3) * 4096 + (1984 - 64 * idx + 8 * l7) * 2), po);
    float xv[8];
#pragma unroll
    for (int i = 0; i < 4; ++i) { xv[2 * i] = blo(xrow[i]); xv[2 * i + 1] = bhi(xrow[i]); }
    float sm = ((xv[0] + xv[1]) + (xv[2] + xv[3])) + ((xv[4] + xv[5]) + (xv[6] + xv[7]));
    const float mean = wave_sum(sm) * (1.0f / D_);
    float sq = 0.f;
#pragma unroll
    for (int i = 0; i < 8; ++i) { const float d = xv[i] - mean; sq += d * d; }
    const float var = rbf(wave_sum(sq) * (1.0f / D_));
    const float sd = __builtin_sqrtf(rbf(var + 1e-5f));
    u32x2 g = {cvtpk(mean, sd), elo};
    st8_dev(rW, lane == 0 ? WS_ST + (unsigned)((8 * aa + w) * 8) : OOB, g);
  }
  STAMP(2);
  const int tg = w & 3, ch = w >> 2;
  unsigned ax[4], aw[3][4];
  {
    const int tr = 32 * tg + (lane & 31), m = G & 1;
#pragma unroll
    for (int j = 0; j < 4; ++j) {
      const int s = (j + tg) & 3;
      ax[j] = L0 + tr * 128 + (((2 * s + hh) ^ ((tr >> 1) & 7)) << 4);
#pragma unroll
      for (int i = 0; i < 3; ++i)
        aw[i][j] = L0 + 16384 + m * 12288 + (16 * s + 8 * hh + qq) * 192 + ((2 * ((3 * ch + i) ^ m) + (pp & 1)) << 4) + 8 * (pp >> 1);
    }
  }
  f32x16 acc[3];
  float cs[3] = {0.f, 0.f, 0.f};
#pragma unroll
  for (int i = 0; i < 3; ++i)
#pragma unroll
    for (int r = 0; r < 16; ++r) acc[i][r] = 0.f;
  const bf16x2_t ones = {(__bf16)1.0f, (__bf16)1.0f};
  bf16x8 xf[2], wf[2][3];
#define RD(c, j) do { const unsigned so_ = ((c) & 3) * SLOT;                                                      \
    _Pragma("unroll") for (int i = 0; i < 3; ++i) {                                                               \
      wf[(j) & 1][i] = cat(trd(aw[i][j] + so_), trd(aw[i][j] + so_ + 768));                                        \
      if (i == 0) xf[(j) & 1] = lds_ld<bf16x8>(ax[j] + so_); } SB(); } while (0)
#define PINACC() do { PIN(acc[0]); PIN(acc[1]); PIN(acc[2]); PIN(cs[0]); PIN(cs[1]); PIN(cs[2]); } while (0)
#define MM(j) do { _Pragma("unroll") for (int i = 0; i < 3; ++i) {                                                \
      acc[i] = MFMA32(wf[(j) & 1][i], xf[(j) & 1], acc[i]);                                                        \
      if ((j) == 0) { const u32x4 u_ = __builtin_bit_cast(u32x4, wf[0][i]);                                        \
        _Pragma("unroll") for (int e = 0; e < 4; ++e) { const unsigned t_ = u_[e];                                 \
          cs[i] = __builtin_amdgcn_fdot2_f32_bf16(__builtin_bit_cast(bf16x2_t, t_), ones, cs[i], false); } } }     \
    PINACC(); SB(); } while (0)
#define COMPUTE(c, D0, D1, D2, D3) do { RD(c, 0); if ((c) > 0) MM(3); D0; RD(c, 1); MM(0); D1; RD(c, 2); MM(1); D2; RD(c, 3); MM(2); D3; } while (0)
#define COMPUTE_N(c) COMPUTE(c, { DX(c + 3, 0); DW(c + 3, 0); }, DX(c + 3, 1), DW(c + 3, 1), DW(c + 3, 2))
  const int kq = w & 3, tt2 = w >> 2, r64 = 4 * pan + (idx >> 3), ct = idx & 7;
  const int ks0 = 22 * kq - (kq == 3 ? 1 : 0);
  const unsigned vd = (unsigned)((16 * ks0 + 8 * tt2 + l3) * 1024 + ct * 128 + ((l7 ^ (((l3 >> 1) & 1) << 2)) << 4));
#define WDU(j, lds) dma16(rD, vd, (j) * 16384, L0 + (lds) + tt2 * 1024)
#define WDA(j) WDU(j, ((j) * 4 + kq) * 2048)

  WAIT_BAR(17); STAMP(16); COMPUTE(0, {}, {}, {}, {}); STAMP(17);
  WAIT_BAR(12); STAMP(18); COMPUTE_N(1); STAMP(20);
  WAIT_BAR(12); STAMP(21); COMPUTE_N(2); STAMP(22);
  WAIT_BAR(10); STAMP(23);
  if (tid == 0) { u32x2 e = {elo, ehi}; st8_dev(rW, WS_FA + (unsigned)(aa * 128), e); }
  COMPUTE_N(3);
  STAMP(3);
  WAIT_BAR(10); COMPUTE_N(4);
  WAIT_BAR(10);
  const unsigned vst = WS_ST + (unsigned)((128 * rt + 32 * tg + (lane & 31)) * 8);
  u32x2 stg, fa;
  COMPUTE(5, { stg = ld8_dev(rW, vst); WDA(0); }, WDA(1), WDA(2), WDA(3));
  WAIT_BAR(10);
  const unsigned vfa = WS_FA + (unsigned)((32 * pan + (lane & 31)) * 128);
  COMPUTE(6, { fa = ld8_dev(rW, vfa); WDA(4); }, { WDA(5); WDA(6); }, { WDA(7); WDA(8); }, WDA(9));
  asm volatile("s_waitcnt vmcnt(11) lgkmcnt(0)\n\ts_barrier" : "+v"(stg)::"memory");
  STAMP(4);
  RD(7, 0); MM(3); WDA(10); RD(7, 1); MM(0); WDA(11);
#pragma unroll
  for (int i = 0; i < 3; ++i) lds_st<float>(L0 + L_CV + ((ch * 3 + i) * 4 + tg) * 256 + lane * 4, cs[i]);
  RD(7, 2); MM(1); WDA(12);
  LBAR();
  u32x4 hcur, hprv;
  {
    float c4[3][4];
    RD(7, 3);
#pragma unroll
    for (int i = 0; i < 3; ++i)
#pragma unroll
      for (int t = 0; t < 4; ++t) c4[i][t] = lds_ld<float>(L0 + L_CV + ((ch * 3 + i) * 4 + t) * 256 + lane * 4);
    SB();
    MM(2); WDA(13);
    LBAR();
    MM(3);
    hcur = ld16(rHk, (unsigned)((2 * aa + cch) * 4096 + ce * 16));
    hprv = ld16(rHk, ce ? (unsigned)((2 * aa + cch) * 4096 + ce * 16 - 16) : OOB);
    stg = spin_tag(rW, vst, stg, elo);
    const unsigned mn = (stg.x & 0xffffu) ^ 0x8000u;
    const u32x4 bm = {mn | (mn << 16), mn, 0u, 0u};
#pragma unroll
    for (int i = 0; i < 3; ++i) {
      const float c = (c4[i][0] + c4[i][1]) + (c4[i][2] + c4[i][3]);
      const float c1 = c - rbf(c), c2 = c1 - rbf(c1);
      const u32x4 am = {cvtpk(c, c1), cvtpk(c2, 0.f), 0u, 0u};
      acc[i] = MFMA32(__builtin_bit_cast(bf16x8, am), __builtin_bit_cast(bf16x8, bm), acc[i]);
    }
    PINACC();
  }
  STAMP(5);
  u32x4 urow;
  {
    const float sd = bhi(stg.x), rs = __builtin_amdgcn_rcpf(sd);
    const float k0 = rs * -2.3022081983f, k1 = k0 * rs * rs * 0.044715f, s2 = sd * sd;
#pragma unroll
    for (int i = 0; i < 3; ++i) {
      float hv[8];
#pragma unroll
      for (int r = 0; r < 8; ++r) {
        const float g = acc[i][r], u = acc[i][r + 8];
        const float e = __builtin_amdgcn_exp2f(g * __builtin_fmaf(g * g, k1, k0));
        hv[r] = (g * u) * __builtin_amdgcn_rcpf(__builtin_fmaf(e, s2, s2));
      }
      const u32x4 hp = {cvtpk(hv[0], hv[1]), cvtpk(hv[2], hv[3]), cvtpk(hv[4], hv[5]), cvtpk(hv[6], hv[7])};
      const int ks = 6 * cg + 3 * ch + i;
      st16_dev(rW, ks < 86 ? WS_H + (unsigned)(((4 * rt + tg) * 86 + ks) * 1024 + lane * 16) : OOB, hp);
      if (i == 0) {
        asm volatile("s_waitcnt vmcnt(13)" : "+v"(fa)::"memory");
        spin_flags(rW, vfa, fa, elo, ehi);
        urow = ld16_dev(rW, WS_KVT + (unsigned)((2 * aa + cch) * 4096 + ce * 16), 0);
      }
    }
  }
  STAMP(6);
  asm volatile("s_waitcnt vmcnt(2)" : "+v"(urow), "+v"(hcur), "+v"(hprv)::"memory");
  {
    const unsigned hb = L0 + L_CV + cch * 16640;
    auto put = [&](u32x4 pv, u32x4 cu, int blk) {
      const unsigned a0 = __builtin_amdgcn_alignbit(pv[1], pv[0], 16), a1 = __builtin_amdgcn_alignbit(pv[2], pv[1], 16),
                     a2 = __builtin_amdgcn_alignbit(pv[3], pv[2], 16), a3 = __builtin_amdgcn_alignbit(cu[0], pv[3], 16),
                     a4 = __builtin_amdgcn_alignbit(cu[1], cu[0], 16);
      lds_st<u32x4>(hb + blk * 16, u32x4{a0, a1, a2, a3});
      lds_st<u32x4>(hb + 4160 + blk * 16, u32x4{pv[1], pv[2], pv[3], cu[0]});
      lds_st<u32x4>(hb + 8320 + blk * 16, u32x4{a1, a2, a3, a4});
      lds_st<u32x4>(hb + 12480 + blk * 16, u32x4{pv[2], pv[3], cu[0], cu[1]});
    };
    put(hprv, hcur, ce + 1);
    const u32x4 zero4 = {0u, 0u, 0u, 0u};
    if (ce == 0) put(zero4, zero4, 0);
    if (ce == 255) put(hcur, zero4, 257);
    lds_st<u32x4>(L0 + L_UR + cch * 4608 + ce * 16, urow);
    if (ce < 30) lds_st<u32x4>(L0 + L_UR + cch * 4608 + 4096 + ce * 16, zero4);
    if (tid < 2) lds_st<u32x4>(L0 + L_SYNC + tid * 16, zero4);
  }
  LBAR();
  STAMP(7);
  const unsigned ad = L0 + (8 * hh + qq) * 128 + ((((4 * ((qq >> 1) & 1)) + 2 * (G & 1) + (pp >> 1))) << 4) + 8 * (pp & 1);
#define WDF(base, dt) cat(trd((ad ^ ((dt) * 64)) + (base)), trd((ad ^ ((dt) * 64)) + (base) + 512))
  bf16x8 wpk[6][2];
#pragma unroll
  for (int j = 0; j < 6; ++j) { wpk[j][0] = WDF((j * 4 + kq) * 2048, 0); wpk[j][1] = WDF((j * 4 + kq) * 2048, 1); }
  f32x4 ya[8];
#pragma unroll
  for (int c = 0; c < 8; ++c) ya[c] = f32x4{0.f, 0.f, 0.f, 0.f};
  const int vch = (w >> 1) & 1, vrh = w & 1, c15 = lane & 15;
  const unsigned cA = L0 + L_CV + vch * 16640 + (c15 & 3) * 4160 + (4 * (c15 >> 2) + 8 * G + 128 * vrh) * 2;
  const unsigned cB = L0 + L_UR + vch * 4608 + (240 - 16 * c15 + 8 * G + 128 * vrh) * 2;
  bf16x8 bfr[2][8], afr[2][2];
#define VLD8(a) (*(const volatile LAS s16x4*)(size_t)(a))
#define CA(set, rho, p) do { afr[set][0] = cat(VLD8(cA + (rho) * 64 + (p) * 512), VLD8(cA + (rho) * 64 + (p) * 512 + 8));                 \
    afr[set][1] = cat(VLD8(cA + (rho) * 64 + (7 - (p)) * 512), VLD8(cA + (rho) * 64 + (7 - (p)) * 512 + 8)); } while (0)
#define CB(rho, mu) bfr[(rho) & 1][mu] = lds_ld<bf16x8>(cB + (rho) * 64 + (7 - (mu)) * 512)
#define PINY() do { _Pragma("unroll") for (int c = 0; c < 8; ++c) PIN(ya[c]); } while (0)
#define CPAIR(rho, p) do { if ((p) < 3) CA(((p) + 1) & 1, rho, (p) + 1); else if ((rho) < 3) CA(0, (rho) + 1, 0);                          \
    if ((rho) < 3) { CB((rho) + 1, 2 * (p)); CB((rho) + 1, 2 * (p) + 1); } SB();                                                          \
    _Pragma("unroll") for (int mu = 0; mu + (p) < 8; ++mu) {                                                                              \
      ya[(p) + mu] = MFMA16(afr[(p) & 1][0], bfr[(rho) & 1][mu], ya[(p) + mu]);                                                            \
      if (mu <= (p)) ya[7 - (p) + mu] = MFMA16(afr[(p) & 1][1], bfr[(rho) & 1][mu], ya[7 - (p) + mu]); }                                    \
    PINY(); SB(); } while (0)
#define CONV(rho) do { CPAIR(rho, 0); CPAIR(rho, 1); CPAIR(rho, 2); CPAIR(rho, 3); } while (0)
  STAMP(24);
  if (w < 4) {
    CA(0, 0, 0);
#pragma unroll
    for (int mu = 0; mu < 8; ++mu) CB(0, mu);
    SB();
    CONV(0); CONV(1);
  }
  STAMP(25);
  WAIT_BAR(0);
  if (tid == 256 && cg < 15) { u32x2 e = {elo, ehi}; st8_dev(rW, WS_FH + (unsigned)((rt * 16 + cg) * 128), e); }
  STAMP(8);
#pragma unroll
  for (int j = 0; j < 6; ++j) WDU(14 + j, (j * 4 + kq) * 2048);
  const int ftok = 64 * r64 + 8 * w + l3;
  const unsigned vxo = (unsigned)(ftok * 1024 + ct * 128 + l7 * 16), vs2 = WS_ST + (unsigned)(ftok * 8);
  u32x4 xo = ld16(rX, vxo);
  u32x2 st2 = ld8_dev(rW, vs2);
  STAMP(26);
  if (w < 4) { CONV(2); CONV(3); }
  STAMP(27);
  LBAR();
  WDU(20, L_WD4 + kq * 2048); WDU(21, L_WD4 + (4 + kq) * 2048);
  if (w < 4) {
#pragma unroll
    for (int c = 0; c < 4; ++c) lds_st<f32x4>(L0 + L_CV + (vch * 2 + (vrh ^ 1)) * 4096 + c * 1024 + lane * 16, sel4(vrh, ya[c], ya[4 + c]));
  }
  LBAR();
  if (w < 4) {
    const unsigned xb = L0 + L_CV + (vch * 2 + vrh) * 4096;
    const float dsc = vch ? bhi(dpair) : blo(dpair);
    u32x2 zp[4];
#pragma unroll
    for (int c = 0; c < 4; ++c) {
      const f32x4 o = lds_ld<f32x4>(xb + c * 1024 + lane * 16), y = sel4(vrh, ya[4 + c], ya[c]);
      const u32x2 ur = lds_ld<u32x2>(L0 + L_UR + vch * 4608 + (2044 - 256 * (4 * vrh + c) - 16 * c15 - 4 * G) * 2);
      const float z0 = rbf(y[0] + o[0]) + rbf(bhi(ur.y) * dsc), z1 = rbf(y[1] + o[1]) + rbf(blo(ur.y) * dsc);
      const float z2 = rbf(y[2] + o[2]) + rbf(bhi(ur.x) * dsc), z3 = rbf(y[3] + o[3]) + rbf(blo(ur.x) * dsc);
      zp[c] = u32x2{cvtpk(z0, z1), cvtpk(z2, z3)};
    }
#pragma unroll
    for (int c = 0; c < 4; ++c) lds_st<u32x2>(xb + c * 512 + c15 * 32 + G * 8, zp[c]);
#pragma unroll
    for (int i = 0; i < 2; ++i)
      st16_dev(rW, WS_ZT + (unsigned)((2 * aa + vch) * 4096 + vrh * 2048 + i * 1024 + lane * 16), lds_ld<u32x4>(xb + i * 1024 + lane * 16));
  }
  STAMP(9);
  if (w >= 4) {
    const int gf = ks0 / 6, gl = (ks0 + 20 + (kq < 2)) / 6, gi = min(gf + (lane & 7), gl);
    const unsigned vfh = WS_FH + (unsigned)(((r64 >> 1) * 16 + gi) * 128);
    u32x2 fh = ld8_dev(rW, vfh); WAITV(0); PIN(fh);
    spin_flags(rW, vfh, fh, elo, ehi);
    *(volatile LAS unsigned*)(size_t)(L0 + L_SYNC + kq * 4) = 1u;
  } else {
    for (unsigned n = 0; *(volatile LAS unsigned*)(size_t)(L0 + L_SYNC + kq * 4) == 0u && n < SPIN_MAX * 16; ++n) __builtin_amdgcn_s_sleep(1);
  }
  STAMP(10);
  u32x4 hf[22];
  const unsigned vh = WS_H + (unsigned)(((2 * r64 + tt2) * 86 + ks0) * 1024 + lane * 16);
#define HL(j) hf[j] = ld16_dev(rW, ((j) < 21 || kq < 2) ? vh : OOB, (j) * 1024)
#pragma unroll
  for (int j = 0; j < 10; ++j) HL(j);
  STAMP(28);
  const unsigned vfz = WS_FZ + (unsigned)((32 * ct + (lane & 31)) * 128);
  u32x2 fz;
  f32x16 fa0, fa1;
#pragma unroll
  for (int r = 0; r < 16; ++r) { fa0[r] = 0.f; fa1[r] = 0.f; }
#define G2M(j, N, A0, A1) do { asm volatile("s_waitcnt vmcnt(" #N ")" : "+v"(hf[j])::"memory");                     \
    fa0 = MFMA32(A0, __builtin_bit_cast(bf16x8, hf[j]), fa0); fa1 = MFMA32(A1, __builtin_bit_cast(bf16x8, hf[j]), fa1); PIN(fa0); PIN(fa1); SB(); \
    if ((j) + 10 < 22) HL(((j) + 10) % 22); } while (0)
#define G2P(j, N) G2M(j, N, wpk[j][0], wpk[j][1])
#define G2R(slot, base) do { wpk[slot][0] = WDF(base, 0); wpk[slot][1] = WDF(base, 1); } while (0)
#define UA(j) (((j) * 4 + kq) * 2048)
  G2P(0, 9);
  STAMP(29);
  if (w < 4 && lane == 0) {
    if (__hip_atomic_fetch_add((LAS unsigned*)(size_t)(L0 + L_SYNC + 16), 1u, __ATOMIC_RELAXED, __HIP_MEMORY_SCOPE_WORKGROUP) == 3u) {
      u32x2 e = {elo, ehi}; st8_dev(rW, WS_FZ + (unsigned)(aa * 128), e); }
  }
  G2P(1, 9); G2P(2, 9); G2P(3, 9);
  G2R(0, UA(6));  G2P(4, 9);
  G2R(1, UA(7));  G2P(5, 9);
  G2R(2, UA(8));  G2M(6, 9, wpk[0][0], wpk[0][1]);
  G2R(3, UA(9));  G2M(7, 9, wpk[1][0], wpk[1][1]);
  G2R(0, UA(10)); G2M(8, 9, wpk[2][0], wpk[2][1]);
  G2R(1, UA(11)); G2M(9, 9, wpk[3][0], wpk[3][1]);
  G2R(2, UA(12)); G2M(10, 9, wpk[0][0], wpk[0][1]);
  G2R(3, UA(13)); G2M(11, 9, wpk[1][0], wpk[1][1]);
  asm volatile("s_barrier" ::: "memory");
  G2R(0, UA(0));  G2M(12, 9, wpk[2][0], wpk[2][1]);
  G2R(1, UA(1));  G2M(13, 8, wpk[3][0], wpk[3][1]);
  G2R(2, UA(2));  G2M(14, 7, wpk[0][0], wpk[0][1]);
  G2R(3, UA(3));  G2M(15, 6, wpk[1][0], wpk[1][1]);
  fz = ld8_dev(rW, vfz);
  G2R(0, UA(4));  G2M(16, 6, wpk[2][0], wpk[2][1]);
  G2R(1, UA(5));  G2M(17, 5, wpk[3][0], wpk[3][1]);
  G2R(2, L_WD4 + kq * 2048);       G2M(18, 4, wpk[0][0], wpk[0][1]);
  G2R(3, L_WD4 + (4 + kq) * 2048); G2M(19, 3, wpk[1][0], wpk[1][1]);
  G2M(20, 2, wpk[2][0], wpk[2][1]);
  G2M(21, 1, wpk[3][0], wpk[3][1]);
  STAMP(11);
  asm volatile("s_waitcnt vmcnt(0)" : "+v"(xo), "+v"(st2), "+v"(fz)::"memory");
  spin_flags(rW, vfz, fz, elo, ehi);
  u32x4 zr = ld16_dev(rW, WS_ZT + (unsigned)((64 * ct + 8 * w + l3) * 4096 + r64 * 128 + l7 * 16), 0);
  st2 = spin_tag(rW, vs2, st2, elo);
  STAMP(30);
  LBAR();
  STAMP(31);
  {
    const int tok = 32 * tt2 + (lane & 31);
    const unsigned pb = L0 + kq * 16384 + tok * 256;
#pragma unroll
    for (int i = 0; i < 4; ++i) {
      lds_st<f32x4>(pb + (((2 * i + hh) ^ (tok & 7)) << 4), f32x4{fa0[4 * i], fa0[4 * i + 1], fa0[4 * i + 2], fa0[4 * i + 3]});
      lds_st<f32x4>(pb + (((8 + 2 * i + hh) ^ (tok & 7)) << 4), f32x4{fa1[4 * i], fa1[4 * i + 1], fa1[4 * i + 2], fa1[4 * i + 3]});
    }
    asm volatile("s_waitcnt vmcnt(0)" : "+v"(zr)::"memory");
    const int r = 8 * w + l3, f = ((r >> 1) & 1) | (((r >> 3) & 3) << 1);
    lds_st<u32x4>(L0 + L_ZT + r * 128 + ((l7 ^ f) << 4), zr);
  }
  LBAR();
  STAMP(12);
  {
    u32x2 zt[2];
    const int o = 4 * (G >> 1) + pp;
#pragma unroll
    for (int j = 0; j < 2; ++j) { const int r = 8 * o + 4 * j + qq, f = ((r >> 1) & 1) | (((r >> 3) & 3) << 1);
      zt[j] = __builtin_bit_cast(u32x2, trd(L0 + L_ZT + r * 128 + ((w ^ f) << 4) + 8 * (G & 1))); }
    const int tl = 8 * w + l3;
    f32x4 p0 = {0.f, 0.f, 0.f, 0.f}, p1 = p0;
#pragma unroll
    for (int k = 0; k < 4; ++k) {
      p0 += lds_ld<f32x4>(L0 + k * 16384 + tl * 256 + (((2 * l7) ^ (tl & 7)) << 4));
      p1 += lds_ld<f32x4>(L0 + k * 16384 + tl * 256 + (((2 * l7 + 1) ^ (tl & 7)) << 4));
    }
    const int src = 16 * (((l7 >> 2) << 1) | (l3 >> 2)) + 4 * (l7 & 3) + (l3 & 3);
    u32x4 zq;
    zq[0] = (unsigned)__builtin_amdgcn_ds_bpermute(src * 4, (int)zt[0].x); zq[1] = (unsigned)__builtin_amdgcn_ds_bpermute(src * 4, (int)zt[0].y);
    zq[2] = (unsigned)__builtin_amdgcn_ds_bpermute(src * 4, (int)zt[1].x); zq[3] = (unsigned)__builtin_amdgcn_ds_bpermute(src * 4, (int)zt[1].y);
    const float mean = blo(st2.x), rs = __builtin_amdgcn_rcpf(bhi(st2.x));
    float o8[8];
#pragma unroll
    for (int i = 0; i < 8; ++i) {
      const float xv = (i & 1) ? bhi(xo[i >> 1]) : blo(xo[i >> 1]), zv = (i & 1) ? bhi(zq[i >> 1]) : blo(zq[i >> 1]);
      const float F = i < 4 ? p0[i & 3] : p1[i & 3];
      const float gs = F * __builtin_amdgcn_rcpf(1.0f + __builtin_amdgcn_exp2f(F * -1.4426950408889634f));
      o8[i] = __builtin_fmaf((xv - mean) * rs * zv, gs, xv);
    }
    const u32x4 ov = {cvtpk(o8[0], o8[1]), cvtpk(o8[2], o8[3]), cvtpk(o8[4], o8[5]), cvtpk(o8[6], o8[7])};
    *(GAS u32x4*)((GAS char*)out + vxo) = ov;
  }
  if (b == 0 && w == 0) {
#pragma unroll
    for (int i = 0; i < 4; ++i) {
      const unsigned v = WS_FA + (unsigned)((64 * i + lane) * 128);
      u32x2 f = ld8_dev(rW, v); WAITV(0); PIN(f);
      spin_flags(rW, v, f, elo, ehi);
    }
    if (lane == 0) { u32x2 e = {elo, ehi}; st8_dev(rW, WS_GEN, e); }
  }
  WAITV(0); __syncthreads();
  if (tid == 0) ((GAS u64*)(ws + (20u<<20)))[b] = __builtin_amdgcn_s_memrealtime();
}

__global__ void tinyA(char* ws) { if (threadIdx.x == 0) *(GAS u64*)(ws + (20u<<20)) = __builtin_amdgcn_s_memrealtime(); }
__global__ void tinyB(char* ws, int n, int mul) {
  const u64 t0 = __builtin_amdgcn_s_memrealtime();
  u64 e = (int)threadIdx.x < n ? ((GAS u64*)(ws + (20u<<20)))[threadIdx.x] : 0;
  __shared__ u64 sm[256]; sm[threadIdx.x] = e; __syncthreads();
  u64 m = 0; for (int i = 0; i < 256; ++i) m = sm[i] > m ? sm[i] : m;
  const u64 tgt = t0 + 2000 + mul * (t0 - m); while (__builtin_amdgcn_s_memrealtime() < tgt) __builtin_amdgcn_s_sleep(1);
}
extern "C" void kernel_launch(void* const* d_in, const int* in_sizes, int n_in, void* d_out, int out_size, void* d_ws, size_t ws_size,
                              hipStream_t stream) {
  (void)in_sizes; (void)n_in; (void)out_size; (void)ws_size;
  (void)hipFuncSetAttribute((const void*)h3_geglu_fused, hipFuncAttributeMaxDynamicSharedMemorySize, LDS_BYTES);
  h3_geglu_fused<<<256, 512, LDS_BYTES, stream>>>((const u16*)d_in[0], (const u16*)d_in[1], (const u16*)d_in[2], (const u16*)d_in[3],
                                                  (const u16*)d_in[4], (const u16*)d_in[5], (const u16*)d_in[7], (const u16*)d_in[8],
                                                  (const u16*)d_in[9], (u16*)d_out, (char*)d_ws);
  tinyB<<<1, 256, 0, stream>>>((char*)d_ws, 256, 0);
}
```

```cpp
#include <hip/hip_runtime.h>
#include <hip/hip_bf16.h>

typedef unsigned short u16;
typedef unsigned long long u64;
typedef __attribute__((ext_vector_type(8))) short bf16x8;
typedef __attribute__((ext_vector_type(4))) short s16x4;
typedef __attribute__((ext_vector_type(2))) __bf16 bf16x2_t;
typedef __attribute__((ext_vector_type(2))) float f32x2;
typedef __attribute__((ext_vector_type(4))) float f32x4;
typedef __attribute__((ext_vector_type(16))) float f32x16;
typedef __attribute__((ext_vector_type(4))) unsigned u32x4;
typedef __attribute__((ext_vector_type(2))) unsigned u32x2;
#define LAS __attribute__((address_space(3)))
#define GAS __attribute__((address_space(1)))
typedef __amdgpu_buffer_rsrc_t rsrc_t;

constexpr int S_ = 2048, D_ = 512, DF = 1376;
constexpr unsigned WS_GEN = 0, WS_FA = 4096, WS_FH = WS_FA + 256 * 128, WS_FZ = WS_FH + 256 * 128;
constexpr unsigned WS_ST = WS_FZ + 256 * 128;
constexpr unsigned WS_KVT = 1u << 20;
constexpr unsigned WS_ZT = WS_KVT + (2u << 20);
constexpr unsigned WS_H = WS_ZT + (2u << 20);
constexpr unsigned WS_BYTES = WS_H + 64 * 86 * 1024;
constexpr int SLOT = 40960;
constexpr int L_CV = 3 * SLOT;
constexpr int L_KA = L_CV, L_VA = L_KA + 9216;
constexpr int L_B1 = 0, L_B2 = SLOT, L_B3 = 2 * SLOT;
constexpr int L_CS = 32768;
constexpr int L_ST = L_CS + 3072;
constexpr int L_SY = L_ST + 512;
constexpr int L_A = L_VA + 8192;
constexpr int L_U0 = 36864, L_U1 = L_CV + 33280, L_ZT = L_CV;
constexpr int LDS_BYTES = 163840;
constexpr unsigned OOB = 0x80000000u;
constexpr unsigned SPIN_MAX = 1u << 18;

#define LPRIO 2
#define XSLEEP 4
#define ROT1 ((rt + cg) & 7)
#define ROT2 ((ct * 11) >> 2)
#define HWIN 12
#define STEPMODE 1
#define NOSTATS 0
#define MFMA32(a, b, c) __builtin_amdgcn_mfma_f32_32x32x16_bf16(a, b, c, 0, 0, 0)
#define MFMA16(a, b, c) __builtin_amdgcn_mfma_f32_16x16x32_bf16(a, b, c, 0, 0, 0)
#define WAIT_BAR(N) asm volatile("s_waitcnt vmcnt(" #N ") lgkmcnt(0)\n\ts_barrier" ::: "memory")
#define LBAR() asm volatile("s_waitcnt lgkmcnt(0)\n\ts_barrier" ::: "memory")
#define WAITV(N) asm volatile("s_waitcnt vmcnt(" #N ")" ::: "memory")
#define SB() __builtin_amdgcn_sched_barrier(0)

__device__ __forceinline__ rsrc_t mk_rsrc(const void* p, unsigned bytes) {
  return __builtin_amdgcn_make_buffer_rsrc((void*)p, (short)0, (int)bytes, 0x00020000);
}
__device__ __forceinline__ void dma16(rsrc_t rs, unsigned voff, unsigned soff, unsigned lds) {
  asm volatile("s_mov_b32 m0, %0\n\ts_nop 0\n\tbuffer_load_dwordx4 %1, %2, %3 offen lds" ::"s"(lds), "v"(voff), "s"(rs), "s"(soff) : "memory");
}
__device__ __forceinline__ u32x4 ld16(rsrc_t rs, unsigned voff) {
  u32x4 r; asm volatile("buffer_load_dwordx4 %0, %1, %2, 0 offen" : "=v"(r) : "v"(voff), "s"(rs) : "memory"); return r;
}
__device__ __forceinline__ u32x4 ld16_dev(rsrc_t rs, unsigned voff, unsigned soff) {
  u32x4 r; asm volatile("buffer_load_dwordx4 %0, %1, %2, %3 offen sc1" : "=v"(r) : "v"(voff), "s"(rs), "s"(soff) : "memory"); return r;
}
__device__ __forceinline__ u32x2 ld8(rsrc_t rs, unsigned voff) {
  u32x2 r; asm volatile("buffer_load_dwordx2 %0, %1, %2, 0 offen" : "=v"(r) : "v"(voff), "s"(rs) : "memory"); return r;
}
__device__ __forceinline__ u32x2 ld8_dev(rsrc_t rs, unsigned voff) {
  u32x2 r; asm volatile("buffer_load_dwordx2 %0, %1, %2, 0 offen sc1" : "=v"(r) : "v"(voff), "s"(rs) : "memory"); return r;
}
#define PIN(x) asm volatile("" : "+v"(x))
__device__ __forceinline__ void st16_dev(rsrc_t rs, unsigned voff, u32x4 v) { __builtin_amdgcn_raw_buffer_store_b128(v, rs, voff, 0, 16); }
__device__ __forceinline__ void st8_dev(rsrc_t rs, unsigned voff, u32x2 v) { __builtin_amdgcn_raw_buffer_store_b64(v, rs, voff, 0, 16); }

__device__ __forceinline__ unsigned cvtpk(float lo, float hi) {
  bf16x2_t r = {(__bf16)lo, (__bf16)hi};
  return __builtin_bit_cast(unsigned, r);
}
__device__ __forceinline__ float blo(unsigned w) { return __uint_as_float(w << 16); }
__device__ __forceinline__ float bhi(unsigned w) { return __uint_as_float(w & 0xffff0000u); }
__device__ __forceinline__ float rbf(float f) { return blo(cvtpk(f, f)); }
__device__ __forceinline__ s16x4 trd(unsigned a) { return __builtin_amdgcn_ds_read_tr16_b64_v4i16((LAS s16x4*)(size_t)a); }
__device__ __forceinline__ bf16x8 cat(s16x4 a, s16x4 b) { return __builtin_shufflevector(a, b, 0, 1, 2, 3, 4, 5, 6, 7); }
template <class T> __device__ __forceinline__ T lds_ld(unsigned a) { return *(const LAS T*)(size_t)a; }
template <class T> __device__ __forceinline__ void lds_st(unsigned a, T v) { *(LAS T*)(size_t)a = v; }
template <int CTRL> __device__ __forceinline__ float dpp_add(float x) {
  return x + __uint_as_float(__builtin_amdgcn_update_dpp(0u, __float_as_uint(x), CTRL, 0xf, 0xf, true));
}
__device__ __forceinline__ float swap_add(float x, bool half) {
  if (half) { auto r = __builtin_amdgcn_permlane32_swap(__float_as_uint(x), __float_as_uint(x), false, false); return __uint_as_float(r[0]) + __uint_as_float(r[1]); }
  auto r = __builtin_amdgcn_permlane16_swap(__float_as_uint(x), __float_as_uint(x), false, false); return __uint_as_float(r[0]) + __uint_as_float(r[1]);
}
__device__ __forceinline__ void wave_sum4(float& a, float& b, float& c, float& d) {
  a = dpp_add<0x128>(a); b = dpp_add<0x128>(b); c = dpp_add<0x128>(c); d = dpp_add<0x128>(d);
  a = dpp_add<0x124>(a); b = dpp_add<0x124>(b); c = dpp_add<0x124>(c); d = dpp_add<0x124>(d);
  a = dpp_add<0x122>(a); b = dpp_add<0x122>(b); c = dpp_add<0x122>(c); d = dpp_add<0x122>(d);
  a = dpp_add<0x121>(a); b = dpp_add<0x121>(b); c = dpp_add<0x121>(c); d = dpp_add<0x121>(d);
  a = swap_add(a, false); b = swap_add(b, false); c = swap_add(c, false); d = swap_add(d, false);
  a = swap_add(a, true); b = swap_add(b, true); c = swap_add(c, true); d = swap_add(d, true);
}
__device__ __forceinline__ void lds_wait_ge(unsigned a, unsigned v) {
  for (unsigned n = 0; *(volatile LAS unsigned*)(size_t)a < v && n < SPIN_MAX * 16; ++n) __builtin_amdgcn_s_sleep(1);
  asm volatile("" ::: "memory");
}
__device__ __forceinline__ void lds_spin_ge(unsigned a, unsigned v) {
  for (unsigned n = 0; *(volatile LAS unsigned*)(size_t)a < v && n < SPIN_MAX * 64; ++n) {}
  asm volatile("" ::: "memory");
}
__device__ __forceinline__ unsigned lds_inc(unsigned a) {
  return __hip_atomic_fetch_add((LAS unsigned*)(size_t)a, 1u, __ATOMIC_RELAXED, __HIP_MEMORY_SCOPE_WORKGROUP);
}
__device__ __forceinline__ int fsw(int r) { return ((r >> 1) & 1) | (((r >> 3) & 3) << 1); }
__device__ __forceinline__ void spin_flags(rsrc_t rs, unsigned voff, u32x2 f, unsigned lo, unsigned hi) {
  for (unsigned n = 0; !__all(f.x == lo && f.y == hi) && n < SPIN_MAX; ++n) {
    __builtin_amdgcn_s_sleep(1);
    f = ld8_dev(rs, voff); WAITV(0); PIN(f);
  }
}
__device__ __forceinline__ u32x2 spin_tag(rsrc_t rs, unsigned voff, u32x2 f, unsigned tag) {
  for (unsigned n = 0; !__all(f.y == tag) && n < SPIN_MAX; ++n) {
    __builtin_amdgcn_s_sleep(1);
    f = ld8_dev(rs, voff); WAITV(0); PIN(f);
  }
  return f;
}
#define STAMP(i)

__global__ __launch_bounds__(512, 2) void h3_geglu_fused(const u16* __restrict__ x, const u16* __restrict__ kin, const u16* __restrict__ vin,
                                                         const u16* __restrict__ shk, const u16* __restrict__ ssk, const u16* __restrict__ ssd,
                                                         const u16* __restrict__ wg, const u16* __restrict__ wu, const u16* __restrict__ wd,
                                                         u16* __restrict__ out, char* __restrict__ ws) {
  extern __shared__ __attribute__((aligned(16))) char smem[];
  const unsigned L0 = (unsigned)(size_t)(LAS char*)smem;
  const int tid = threadIdx.x, lane = tid & 63;
  const int w = __builtin_amdgcn_readfirstlane(tid >> 6), wj = w & 3;
  STAMP(0);
  const int b = blockIdx.x, xcd = b & 7, idx = b >> 3, rh = xcd & 1, q4 = xcd >> 1;
  const int pan = 4 * rh + q4, aa = 32 * pan + idx;
  const int l3 = lane >> 3, l7 = lane & 7, G = lane >> 4, qq = (lane >> 2) & 3, pp = lane & 3, hh = lane >> 5, l31 = lane & 31;
  const rsrc_t rX = mk_rsrc(x, S_ * D_ * 2), rW = mk_rsrc(ws, WS_BYTES), rD = mk_rsrc(wd, DF * D_ * 2);
  const int cg = idx < 24 ? ((q4 * 7 + 1) >> 1) + (idx >> 3) : idx < 28 ? (q4 < 2 ? 3 : 10) : 14 + ((idx >> 1) & 1);
  const int rt = 8 * rh + (idx < 24 ? (idx & 7) : idx < 28 ? (q4 & 1) * 4 + (idx & 3) : 2 * q4 + (idx & 1));
  const int r64 = 4 * pan + (idx >> 3), ct = idx & 7;
  const int ks0 = 22 * wj - (wj == 3 ? 1 : 0);
  u32x4 xo; u32x2 st2;
  unsigned elo, ehi;
#define VD() (unsigned)((16 * ks0 + l3) * 1024 + ct * 128 + ((l7 ^ (((l3 >> 1) & 1) << 2)) << 4))
  const int rot2 = ROT2;
#define PH(j) ((j) + rot2 >= 22 ? (j) + rot2 - 22 : (j) + rot2)
  const int klast = wj < 2 ? 21 : 20;
#define WDU(j, lds) do { const unsigned v_ = vd | (PH(j) > klast ? OOB : 0u);                                       \
    dma16(rD, v_, PH(j) * 16384, L0 + (lds)); dma16(rD, v_, PH(j) * 16384 + 8192, L0 + (lds) + 1024); } while (0)
#define VXO() (unsigned)((64 * r64 + 8 * w + l3) * 1024 + ct * 128 + l7 * 16)
#define VS2() (WS_ST + (unsigned)((64 * r64 + 8 * w + l3) * 8))

  if (w >= 4) {
    __builtin_amdgcn_s_setprio(2);
    u32x2 gen = ld8_dev(rW, WS_GEN);
    const unsigned vx = (cg < 15) ? (unsigned)((128 * rt + 8 * wj + l3) * 1024 + ((l7 ^ ((4 * wj + (l3 >> 1)) & 7)) << 4)) : OOB;
    unsigned vg[3], vu[3];
#pragma unroll
    for (int r = 0; r < 3; ++r) {
      const unsigned P = 64 * r + lane, k = (P * 43691u) >> 19, col = 96 * cg + 8 * (P - 12 * k);
      vg[r] = (col < DF) ? (16 * wj + k) * (DF * 2) + col * 2 : OOB; vu[r] = vg[r] ^ 32u;
    }
    const rsrc_t rG = mk_rsrc(wg, D_ * DF * 2), rU = mk_rsrc(wu, D_ * DF * 2);
    const int rot1 = ROT1;
    unsigned dpair = ((const unsigned*)ssd)[aa];
    asm volatile("" : "+s"(dpair));
    if (tid == 256) lds_st<unsigned>(L0 + L_A, 0u);
    LBAR();
#pragma nounroll
    for (int c = 0; c < 8; ++c) {
      const int cc = (c + rot1) & 7;
      const unsigned sx = cc * 128, sw = cc * (64 * DF * 2), lx = L0 + (c & 3) * SLOT + wj * 1024, lw = L0 + (c & 3) * SLOT + 16384 + wj * 3072;
      dma16(rX, vx, sx, lx);                 dma16(rG, vg[0], sw, lw);        dma16(rU, vu[0], sw, lw + 12288);
      dma16(rX, vx, sx + 32768, lx + 4096);  dma16(rG, vg[1], sw, lw + 1024); dma16(rU, vu[1], sw, lw + 13312);
      if (c >= 2) asm volatile("s_waitcnt vmcnt(16)\n\ts_barrier" ::: "memory");
      if (c == 2) STAMP(16);
      if (c == 3) STAMP(18);
      if (c == 7) STAMP(23);
      if (c == 5 && tid == 256) {
        PIN(gen); const unsigned lo = gen.x + 1u;
        st8_dev(rW, WS_FA + (unsigned)(aa * 128), u32x2{lo, gen.y + (lo == 0u ? 1u : 0u)});
      }
      dma16(rX, vx, sx + 65536, lx + 8192);  dma16(rG, vg[2], sw, lw + 2048); dma16(rU, vu[2], sw, lw + 14336);
      dma16(rX, vx, sx + 98304, lx + 12288);
    }
    PIN(gen);
    elo = __builtin_amdgcn_readfirstlane(gen.x) + 1u;
    ehi = __builtin_amdgcn_readfirstlane(gen.y) + (elo == 0u ? 1u : 0u);
    const int ce = tid & 255;
    const unsigned vd = VD();
    const unsigned vfa = WS_FA + (unsigned)((32 * pan + l31) * 128), vst = WS_ST + (unsigned)((128 * rt + 64 * (wj & 1) + lane) * 8);
    const u32x4 zero4 = {0u, 0u, 0u, 0u};
    u32x2 stg = ld8_dev(rW, vst), fa = ld8_dev(rW, vfa);
#pragma unroll
    for (int j = 0; j < 4; ++j) WDU(j, L_B1 + wj * 8192 + j * 2048);
    if (ce < 4) lds_st<u32x4>(L0 + L_SY + ce * 16, zero4);
    WAIT_BAR(20);
#pragma unroll
    for (int j = 0; j < 5; ++j) WDU(4 + j, L_B2 + wj * 10240 + j * 2048);
    asm volatile("s_waitcnt vmcnt(18)\n\ts_barrier" : "+v"(stg), "+v"(fa)::"memory");
    STAMP(4);
    stg = spin_tag(rW, vst, stg, elo);
    if (wj < 2) lds_st<unsigned>(L0 + L_ST + (64 * wj + lane) * 4, stg.x);
    spin_flags(rW, vfa, fa, elo, ehi);
    u32x4 hc[2], hp[2], ur[2];
    {
      const rsrc_t rHk = mk_rsrc(ssk, D_ * S_ * 2);
#pragma unroll
      for (int c = 0; c < 2; ++c) {
        hc[c] = ld16(rHk, (unsigned)((2 * aa + c) * 4096 + ce * 16));
        hp[c] = ld16(rHk, ce ? (unsigned)((2 * aa + c) * 4096 + ce * 16 - 16) : OOB);
        ur[c] = ld16_dev(rW, WS_KVT + (unsigned)(2 * aa * 4096 + ce * 16), c * 4096);
      }
    }
#pragma unroll
    for (int j = 0; j < 2; ++j) WDU(9 + j, L_B3 + wj * 10240 + j * 2048);
    WAIT_BAR(10);
    STAMP(5);
#pragma unroll
    for (int j = 2; j < 5; ++j) WDU(9 + j, L_B3 + wj * 10240 + j * 2048);
    {
      asm volatile("s_waitcnt vmcnt(10)" : "+v"(hc[0]), "+v"(hc[1]), "+v"(hp[0]), "+v"(hp[1]), "+v"(ur[0]), "+v"(ur[1])::"memory");
      auto put = [&](unsigned hb, u32x4 pv, u32x4 cu, int blk) {
        const unsigned a0 = __builtin_amdgcn_alignbit(pv[1], pv[0], 16), a1 = __builtin_amdgcn_alignbit(pv[2], pv[1], 16),
                       a2 = __builtin_amdgcn_alignbit(pv[3], pv[2], 16), a3 = __builtin_amdgcn_alignbit(cu[0], pv[3], 16),
                       a4 = __builtin_amdgcn_alignbit(cu[1], cu[0], 16);
        lds_st<u32x4>(hb + blk * 16, u32x4{a0, a1, a2, a3});
        lds_st<u32x4>(hb + 4160 + blk * 16, u32x4{pv[1], pv[2], pv[3], cu[0]});
        lds_st<u32x4>(hb + 8320 + blk * 16, u32x4{a1, a2, a3, a4});
        lds_st<u32x4>(hb + 12480 + blk * 16, u32x4{pv[2], pv[3], cu[0], cu[1]});
      };
#pragma unroll
      for (int c = 0; c < 2; ++c) {
        put(L0 + L_CV + c * 16640, hp[c], hc[c], ce + 1);
        if (ce == 0 || ce == 255) put(L0 + L_CV + c * 16640, ce ? hc[c] : zero4, zero4, ce ? 257 : 0);
        lds_st<u32x4>(L0 + (c ? L_U1 : L_U0) + ce * 16, ur[c]);
      }
      asm volatile("s_waitcnt vmcnt(0) lgkmcnt(0)" ::: "memory");
      *(volatile LAS unsigned*)(size_t)(L0 + L_SY + wj * 4) = 1u;
      if (lane == 0) lds_inc(L0 + L_SY + 24);
    }
    STAMP(7);
    const int vch = wj >> 1, vrh = wj & 1, c15 = lane & 15;
    f32x4 ya[8];
#pragma unroll
    for (int c = 0; c < 8; ++c) ya[c] = f32x4{0.f, 0.f, 0.f, 0.f};
    {
      unsigned cA = L0 + L_CV + vch * 16640 + (c15 & 3) * 4160 + (4 * (c15 >> 2) + 8 * G + 128 * vrh) * 2;
      const unsigned ub = L0 + (vch ? L_U1 : L_U0);
      unsigned cB = ub + (240 - 16 * c15 + 8 * G + 128 * vrh) * 2;
      int lim = ((16 * c15 - 8 * G + 47) >> 5) - 4 * vrh;
      PIN(cA); PIN(cB);
      bf16x8 bfr[2][8]; s16x4 ah[2][4];
#define VLD8(a) (*(const volatile LAS s16x4*)(size_t)(a))
#define CA(set, r, p, i) ah[set][i] = VLD8(cA + (r) * 64 + (((i) & 2) ? 7 - (p) : (p)) * 512 + ((i) & 1) * 8)
#define CB(r, mu) bfr[(r) & 1][mu] = lds_ld<bf16x8>((mu) == 0 ? ((r) >= lim ? L0 + L_SY + 32 : cB + (r) * 64 + 3584) : cB + (r) * 64 + (7 - (mu)) * 512)
#define CM(r, p, m) do { if ((m) + (p) < 8) { ya[(p) + (m)] = MFMA16(cat(ah[(p) & 1][0], ah[(p) & 1][1]), bfr[(r) & 1][(m) & 7], ya[((p) + (m)) & 7]); PIN(ya[((p) + (m)) & 7]); } \
        else { ya[(m) - 1] = MFMA16(cat(ah[(p) & 1][2], ah[(p) & 1][3]), bfr[(r) & 1][((m) + (p)) & 7], ya[(m) - 1]); PIN(ya[(m) - 1]); } } while (0)
#define CPAIR(r, p) do { _Pragma("unroll") for (int m = 0; m < 9; ++m) { CM(r, p, m);                                               \
          if (m < 4) CA(((p) + 1) & 1, (p) < 3 ? (r) : (r) + 1, ((p) + 1) & 3, m); else if (m < 6) CB((r) + 1, 2 * (p) + m - 4); SB(); } } while (0)
#define CONV(r) do { CPAIR(r, 0); CPAIR(r, 1); CPAIR(r, 2); CPAIR(r, 3); } while (0)
      lds_wait_ge(L0 + L_SY + 24, 8u); __builtin_amdgcn_s_setprio(2);
      STAMP(24);
#pragma unroll
      for (int i = 0; i < 4; ++i) CA(0, 0, 0, i);
#pragma unroll
      for (int mu = 0; mu < 8; ++mu) CB(0, mu);
#pragma nounroll
      for (int it = 0; it < 2; ++it) {
        CONV(0); CPAIR(1, 0); CPAIR(1, 1); CPAIR(1, 2);
        if (it) { asm volatile("s_waitcnt lgkmcnt(0)" ::: "memory"); *(volatile LAS unsigned*)(size_t)(L0 + L_SY + 48 + wj * 4) = 1u; }
        CPAIR(1, 3);
        cA += 128; cB += 128; lim -= 2; PIN(cA); PIN(cB);
      }
    }
    STAMP(27);
    {
      const unsigned mine = L0 + L_SY + 48 + wj * 4, other = L0 + L_SY + 48 + (wj ^ 1) * 4;
      const unsigned xo_ = L0 + L_CV + vch * 16640 + (vrh ^ 1) * 4096 + lane * 16, xb = L0 + L_CV + vch * 16640 + vrh * 4096;
      const float dsc = vch ? bhi(dpair) : blo(dpair);
      u32x2 uq[4];
#pragma unroll
      for (int c = 0; c < 4; ++c) uq[c] = lds_ld<u32x2>(L0 + (vch ? L_U1 : L_U0) + (2044 - 256 * (4 * vrh + c) - 16 * c15 - 4 * G) * 2);
      lds_spin_ge(other, 1u);
      f32x4 y[4];
      if (vrh) {
        asm volatile("" ::: "memory");
#pragma unroll
        for (int c = 0; c < 4; ++c) { lds_st<f32x4>(xo_ + c * 1024, ya[c]); y[c] = ya[4 + c]; PIN(y[c]); }
      } else {
#pragma unroll
        for (int c = 0; c < 4; ++c) { lds_st<f32x4>(xo_ + c * 1024, ya[4 + c]); y[c] = ya[c]; PIN(y[c]); }
        asm volatile("" ::: "memory");
      }
      asm volatile("s_waitcnt lgkmcnt(0)" ::: "memory");
      *(volatile LAS unsigned*)(size_t)mine = 2u;
#pragma unroll
      for (int c = 0; c < 4; ++c)
#pragma unroll
        for (int e = 0; e < 4; ++e) {
          const unsigned uu = (e & 2) ? uq[c].x : uq[c].y;
          float s = __builtin_fmaf((e & 1) ? blo(uu) : bhi(uu), dsc, y[c][e]); PIN(s); y[c][e] = s;
        }
      lds_spin_ge(other, 2u);
      f32x4 o[4];
#pragma unroll
      for (int c = 0; c < 4; ++c) o[c] = lds_ld<f32x4>(xb + c * 1024 + lane * 16);
      SB();
      u32x2 zp[4];
#pragma unroll
      for (int c = 0; c < 4; ++c) {
        float z[4];
#pragma unroll
        for (int e = 0; e < 4; ++e) { z[e] = y[c][e] + o[c][e]; asm("" : "+v"(z[e])); }
        zp[c] = u32x2{cvtpk(z[0], z[1]), cvtpk(z[2], z[3])};
      }
#pragma unroll
      for (int c = 0; c < 4; ++c) lds_st<u32x2>(xb + c * 512 + c15 * 32 + G * 8, zp[c]);
#pragma unroll
      for (int i = 0; i < 2; ++i)
        st16_dev(rW, WS_ZT + (unsigned)((2 * aa + vch) * 4096 + vrh * 2048 + i * 1024 + lane * 16), lds_ld<u32x4>(xb + i * 1024 + lane * 16));
    }
    STAMP(26);
    xo = ld16(rX, VXO()); st2 = ld8_dev(rW, VS2());
    asm volatile("s_waitcnt vmcnt(2)" ::: "memory");
    if (lane == 0 && lds_inc(L0 + L_SY + 20) == 3u) { u32x2 e = {elo, ehi}; st8_dev(rW, WS_FZ + (unsigned)(aa * 128), e); }
    STAMP(9);
    if (b == 0 && wj == 0) {
#pragma nounroll
      for (int i = 0; i < 4; ++i) {
        const unsigned v = WS_FA + (unsigned)((64 * i + lane) * 128);
        spin_flags(rW, v, u32x2{~elo, 0u}, elo, ehi);
      }
      if (lane == 0) { u32x2 e = {elo, ehi}; st8_dev(rW, WS_GEN, e); }
    }
    {
      const unsigned vfz = WS_FZ + (unsigned)((32 * ct + l31) * 128);
      u32x2 fz = ld8_dev(rW, vfz);
      asm volatile("s_waitcnt vmcnt(0)" : "+v"(fz), "+v"(xo), "+v"(st2)::"memory");
      spin_flags(rW, vfz, fz, elo, ehi);
      STAMP(30);
      const unsigned vz = WS_ZT + (unsigned)((64 * ct + 16 * wj + l3) * 4096 + r64 * 128 + l7 * 16);
      u32x4 z0 = ld16_dev(rW, vz, 0), z1 = ld16_dev(rW, vz, 32768);
      lds_wait_ge(L0 + L_SY + 20, 4u);
      asm volatile("s_waitcnt vmcnt(0)" : "+v"(z0), "+v"(z1)::"memory");
      const int r = 16 * wj + l3;
      lds_st<u32x4>(L0 + L_ZT + r * 128 + ((l7 ^ fsw(r)) << 4), z0);
      lds_st<u32x4>(L0 + L_ZT + (r + 8) * 128 + ((l7 ^ fsw(r + 8)) << 4), z1);
    }
    STAMP(31);
  } else {
    __builtin_amdgcn_s_setprio(3);
    u32x2 sh[2]; u32x4 xr[2];
#pragma unroll
    for (int i = 0; i < 2; ++i) xr[i] = ld16(rX, (unsigned)((8 * aa + 2 * wj + i) * 1024 + lane * 16));
    {
      const rsrc_t rK = mk_rsrc(kin, S_ * D_ * 2), rV = mk_rsrc(vin, S_ * D_ * 2);
      const unsigned kb = (unsigned)((64 * idx + l3) * 1024 + pan * 128), fl = (unsigned)(l7 ^ ((l3 >> 1) & 1));
#pragma unroll
      for (int i = 0; i < 2; ++i) {
        const int g = 2 * wj + i;
        dma16(rK, kb + ((fl ^ (((g + 1) & 3) << 1)) << 4), g * 8192, L0 + L_KA + (g + 1) * 1024);
        dma16(rV, kb + ((fl ^ ((g & 3) << 1)) << 4), g * 8192, L0 + L_VA + g * 1024);
      }
      if (wj == 0) dma16(rK, idx == 0 ? OOB : kb - 8192 + (fl << 4), 0, L0 + L_KA);
    }
    u32x2 gen = ld8_dev(rW, WS_GEN);
    {
      const rsrc_t rSh = mk_rsrc(shk, D_ * 4 * 2);
#pragma unroll
      for (int i = 0; i < 2; ++i) sh[i] = ld8(rSh, (unsigned)((64 * pan + 16 * wj + 8 * i + 4 * (G & 1) + pp) * 8));
    }
    asm volatile("s_barrier" ::: "memory");
    STAMP(3);
    const int tp = wj & 1, ch = wj >> 1;
    f32x16 acc[2][3];
    f32x4 cs[3];
    unsigned ax[4], aw[2][3], ak[5];
    unsigned vkv = WS_KVT + (unsigned)((64 * pan + 16 * wj + l3) * 4096 + (1984 - 64 * idx + 8 * l7) * 2);
    unsigned vst0 = lane < 2 ? WS_ST + (unsigned)((8 * aa + 2 * wj + lane) * 8) : OOB;
    int src4 = 4 * (16 * ((((7 - l7) >> 2) << 1) | (l3 >> 2)) + 4 * ((7 - l7) & 3) + (l3 & 3));
    const unsigned so_ = ((lane & 15) == 4 * (G & 1)) ? 0x3f803f80u : 0u;
    bf16x8 sel = __builtin_bit_cast(bf16x8, u32x4{so_, so_, so_, so_});
    {
      const unsigned ax0 = L0 + (64 * tp + l31) * 128 + (((4 * tp + hh) ^ ((l31 >> 1) & 7)) << 4);
      const unsigned aw0 = L0 + 16384 + (G & 1) * 12288 + (8 * hh + qq) * 192 + ((pp & 1) << 4) + 8 * (pp >> 1);
#pragma unroll
      for (int s = 0; s < 4; ++s) { ax[s] = ax0 ^ (s << 5); PIN(ax[s]); }
#pragma unroll
      for (int i = 0; i < 3; ++i) {
        const unsigned a_ = aw0 + (((3 * ch + i) ^ (G & 1)) << 5);
        aw[0][i] = a_ + tp * 6144; aw[1][i] = a_ + 6144 - tp * 6144; PIN(aw[0][i]); PIN(aw[1][i]);
      }
#pragma unroll
      for (int j = 0; j < 5; ++j) { const int rr = 8 * (4 * (G >> 1) + pp) + qq + (j < 3 ? 4 + 4 * j : 4 * (j - 3));
        ak[j] = L0 + (j < 3 ? L_KA : L_VA) + rr * 128 + (((2 * wj) ^ fsw(rr)) << 4) + 8 * (G & 1); PIN(ak[j]); }
      PIN(vkv); PIN(vst0); PIN(src4); PIN(sel);
    }
    {
      asm volatile("s_waitcnt vmcnt(7)" : "+v"(xr[0]), "+v"(xr[1])::"memory");
      STAMP(14);
      const f32x2 x0 = {blo(__builtin_amdgcn_readfirstlane(xr[0][0])), blo(__builtin_amdgcn_readfirstlane(xr[1][0]))};
      f32x2 d[8];
#pragma unroll
      for (int e = 0; e < 4; ++e) { d[2 * e] = f32x2{blo(xr[0][e]), blo(xr[1][e])} - x0; d[2 * e + 1] = f32x2{bhi(xr[0][e]), bhi(xr[1][e])} - x0; }
      const f32x2 sm = ((d[0] + d[1]) + (d[2] + d[3])) + ((d[4] + d[5]) + (d[6] + d[7]));
      const f32x2 sq = ((d[0] * d[0] + d[1] * d[1]) + (d[2] * d[2] + d[3] * d[3])) + ((d[4] * d[4] + d[5] * d[5]) + (d[6] * d[6] + d[7] * d[7]));
      float s0 = sm[0], s1 = sm[1], q0 = sq[0], q1 = sq[1];
      wave_sum4(s0, s1, q0, q1);
      const float m = (lane ? s1 : s0) * (1.0f / D_), var = (lane ? q1 : q0) * (1.0f / D_) - m * m;
      unsigned ms0 = cvtpk(m + (lane ? x0[1] : x0[0]), __builtin_amdgcn_sqrtf(rbf(rbf(__builtin_fmaxf(var, 0.f)) + 1e-5f)));
      asm volatile("s_waitcnt vmcnt(0)" : "+v"(gen), "+v"(sh[0]), "+v"(sh[1]), "+v"(ms0)::"memory");
      STAMP(15);
      if (lane == 0) lds_inc(L0 + L_A);
      elo = __builtin_amdgcn_readfirstlane(gen.x) + 1u;
      ehi = __builtin_amdgcn_readfirstlane(gen.y) + (elo == 0u ? 1u : 0u);
      st8_dev(rW, vst0, u32x2{ms0, elo});
      f32x2 sk[4] = {{blo(sh[0].x), blo(sh[1].x)}, {bhi(sh[0].x), bhi(sh[1].x)}, {blo(sh[0].y), blo(sh[1].y)}, {bhi(sh[0].y), bhi(sh[1].y)}};
      PIN(sk[0]); PIN(sk[1]); PIN(sk[2]); PIN(sk[3]);
      lds_spin_ge(L0 + L_A, 4u);
      STAMP(1);
      u32x2 t[2][5];
#pragma unroll
      for (int j = 0; j < 5; ++j) { t[0][j] = __builtin_bit_cast(u32x2, trd(ak[j])); t[1][j] = __builtin_bit_cast(u32x2, trd(ak[j] ^ 16)); }
      SB();
      f32x2 kk[20], r[8];
#pragma unroll
      for (int j = 0; j < 5; ++j) {
        kk[4 * j] = f32x2{blo(t[0][j].x), blo(t[1][j].x)}; kk[4 * j + 1] = f32x2{bhi(t[0][j].x), bhi(t[1][j].x)};
        kk[4 * j + 2] = f32x2{blo(t[0][j].y), blo(t[1][j].y)}; kk[4 * j + 3] = f32x2{bhi(t[0][j].y), bhi(t[1][j].y)};
      }
#pragma unroll
      for (int e = 0; e < 8; ++e) r[e] = kk[e + 1] * sk[3];
#pragma unroll
      for (int j = 2; j >= 0; --j)
#pragma unroll
        for (int e = 0; e < 8; ++e) r[e] = __builtin_elementwise_fma(kk[e + 4 - j], sk[j], r[e]);
#pragma unroll
      for (int e = 0; e < 8; ++e) r[e] *= kk[12 + e];
      u32x4 po[2];
#pragma unroll
      for (int i = 0; i < 2; ++i)
#pragma unroll
        for (int e = 0; e < 4; ++e) po[i][e] = (unsigned)__builtin_amdgcn_ds_bpermute(src4, (int)cvtpk(r[7 - 2 * e][i], r[6 - 2 * e][i]));
      st16_dev(rW, vkv, po[0]); st16_dev(rW, vkv + 32768, po[1]);
      __builtin_amdgcn_s_setprio(1);
    }
    STAMP(2);
    {
      bf16x8 xf[2][2]; s16x4 wf[2][3][2];
#define PINA(t, i) PIN(acc[t][i])
#define M1(p, t, i) do { acc[t][i] = MFMA32(cat(wf[p][i][0], wf[p][i][1]), xf[p][t], acc[t][i]); PINA(t, i); } while (0)
#define MS(p, i, on) do { if (on) { cs[i] = MFMA16(sel, cat(wf[p][i][0], wf[p][i][1]), cs[i]); PIN(cs[i]); } } while (0)
#define RW(s, i) do { wf[(s) & 1][i][0] = trd(aw[(s) >> 1][i] + ((s) & 1) * 3072); wf[(s) & 1][i][1] = trd(aw[(s) >> 1][i] + ((s) & 1) * 3072 + 768); } while (0)
#define RX(s, t) xf[(s) & 1][t] = lds_ld<bf16x8>(ax[s] + (t) * 4096)
#define KEEP(p) do { asm volatile("" ::"v"(wf[p][0][0]), "v"(wf[p][0][1]), "v"(wf[p][1][0]), "v"(wf[p][1][1]), "v"(wf[p][2][0]), "v"(wf[p][2][1]), "v"(xf[p][0]), "v"(xf[p][1])); } while (0)
#define ADV(a) do { a += dl; PIN(a); } while (0)
#define STEP(p, s, on) do { M1(p, 0, 0); RW(s, 0); SB(); M1(p, 1, 0); RX(s, 0); RX(s, 1); SB(); M1(p, 0, 1); RW(s, 1); SB();  \
        M1(p, 1, 1); RW(s, 2); SB(); M1(p, 0, 2);                                                                             \
        if ((s) == 3) { ADV(ax[0]); ADV(ax[1]); ADV(ax[2]); ADV(ax[3]); ADV(aw[0][0]); } SB(); M1(p, 1, 2);                     \
        if ((s) == 3) { ADV(aw[0][1]); ADV(aw[0][2]); ADV(aw[1][0]); ADV(aw[1][1]); ADV(aw[1][2]); }                            \
        MS(p, 0, on); MS(p, 1, on); MS(p, 2, on); KEEP(p); SB(); } while (0)
      LBAR();
      STAMP(16);
      RW(0, 0); RX(0, 0); RX(0, 1); RW(0, 1); RW(0, 2); SB();
      unsigned dl = SLOT;
#pragma unroll
      for (int i = 0; i < 3; ++i) {
        cs[i] = f32x4{0.f, 0.f, 0.f, 0.f};
#pragma unroll
        for (int r = 0; r < 16; ++r) { acc[0][i][r] = 0.f; acc[1][i][r] = 0.f; }
      }
      STEP(0, 1, true);
#pragma nounroll
      for (int c = 0; c < 7; ++c) {
        dl = (c == 3) ? 0u - 3u * SLOT : (unsigned)SLOT;
        STEP(1, 2, true);
        if (c == 2) WAITV(0);
        STEP(0, 3, false);
        LBAR();
        STEP(1, 0, false); STEP(0, 1, true);
      }
      STAMP(4);
      STEP(1, 2, true);
#pragma unroll
      for (int i = 0; i < 3; ++i) lds_st<float>(L0 + L_CS + (wj * 3 + i) * 256 + lane * 4, cs[i][0]);
      STEP(0, 3, false);
      LBAR();
      float co[3]; unsigned ms[2];
#pragma unroll
      for (int i = 0; i < 3; ++i) co[i] = lds_ld<float>(L0 + L_CS + ((wj ^ 1) * 3 + i) * 256 + lane * 4);
#pragma unroll
      for (int t = 0; t < 2; ++t) ms[t] = lds_ld<unsigned>(L0 + L_ST + (64 * tp + 32 * t + l31) * 4);
      SB();
      M1(1, 0, 0); M1(1, 1, 0); M1(1, 0, 1); M1(1, 1, 1); M1(1, 0, 2); M1(1, 1, 2); SB();
      float k0[2], k1[2], s2[2];
      u32x4 bm[2];
#pragma unroll
      for (int t = 0; t < 2; ++t) {
        const unsigned mn = (ms[t] & 0xffffu) ^ 0x8000u;
        bm[t] = u32x4{mn | (mn << 16), mn, 0u, 0u};
        const float sd = bhi(ms[t]), rs = __builtin_amdgcn_rcpf(sd);
        k0[t] = rs * -2.3022081983f; k1[t] = k0[t] * rs * rs * 0.044715f; s2[t] = sd * sd;
      }
#pragma unroll
      for (int i = 0; i < 3; ++i) {
        const float c = cs[i][0] + co[i], c1 = c - rbf(c), c2 = c1 - rbf(c1);
        const u32x4 am = {cvtpk(c, c1), cvtpk(c2, 0.f), 0u, 0u};
#pragma unroll
        for (int t = 0; t < 2; ++t) { acc[t][i] = MFMA32(__builtin_bit_cast(bf16x8, am), __builtin_bit_cast(bf16x8, bm[t]), acc[t][i]); PINA(t, i); }
      }
      STAMP(5);
      const unsigned vhs = WS_H + (unsigned)(((4 * rt + 2 * tp) * 86 + 6 * cg + 3 * ch) * 1024 + lane * 16);
#pragma unroll
      for (int i = 0; i < 3; ++i) {
        const unsigned vo = (vhs + i * 1024) | (6 * cg + 3 * ch + i < 86 ? 0u : OOB);
#pragma unroll
        for (int t = 0; t < 2; ++t) {
          f32x2 g[4], zz[4], gu[4];
#pragma unroll
          for (int r = 0; r < 4; ++r) {
            g[r] = f32x2{acc[t][i][2 * r], acc[t][i][2 * r + 1]};
            zz[r] = g[r] * __builtin_elementwise_fma(g[r] * g[r], f32x2{k1[t], k1[t]}, f32x2{k0[t], k0[t]});
          }
#pragma unroll
          for (int r = 0; r < 4; ++r) {
            zz[r] = f32x2{__builtin_amdgcn_exp2f(zz[r][0]), __builtin_amdgcn_exp2f(zz[r][1])};
            gu[r] = g[r] * f32x2{acc[t][i][8 + 2 * r], acc[t][i][9 + 2 * r]};
          }
          u32x4 hq;
#pragma unroll
          for (int r = 0; r < 4; ++r) {
            const f32x2 dd = __builtin_elementwise_fma(zz[r], f32x2{s2[t], s2[t]}, f32x2{s2[t], s2[t]});
            const f32x2 hv = gu[r] * f32x2{__builtin_amdgcn_rcpf(dd[0]), __builtin_amdgcn_rcpf(dd[1])};
            hq[r] = cvtpk(hv[0], hv[1]);
          }
          st16_dev(rW, vo + t * (86 * 1024), hq);
        }
      }
    }
    if (lane == 0) lds_inc(L0 + L_SY + 24);
    STAMP(6);
    const unsigned ad = L0 + (8 * hh + qq) * 128 + ((4 * ((qq >> 1) & 1) + 2 * (G & 1) + (pp >> 1)) << 4) + 8 * (pp & 1);
    const unsigned a1 = ad + L_B1 + wj * 8192, a2 = ad + L_B2 + wj * 10240;
#define WDF(base, dt) cat(trd(((base) ^ ((dt) * 64))), trd(((base) ^ ((dt) * 64)) + 512))
#define UA(j) ((j) < 4 ? a1 + (j) * 2048 : (j) < 9 ? a2 + ((j) - 4) * 2048 : (j) < 14 ? a2 + SLOT + ((j) - 9) * 2048 : (j) < 18 ? a1 + ((j) - 14) * 2048 : a2 + ((j) - 18) * 2048)
    bf16x8 wpk[8][2];
    const unsigned vd = VD();
#pragma unroll
    for (int j = 0; j < 8; ++j) { wpk[j][0] = WDF(UA(j), 0); wpk[j][1] = WDF(UA(j), 1); }
#pragma unroll
    for (int j = 0; j < 8; ++j) { PIN(wpk[j][0]); PIN(wpk[j][1]); }
#pragma unroll
    for (int j = 0; j < 4; ++j) WDU(14 + j, L_B1 + wj * 8192 + j * 2048);
#pragma unroll
    for (int j = 0; j < 4; ++j) WDU(18 + j, L_B2 + wj * 10240 + j * 2048);
    xo = ld16(rX, VXO()); st2 = ld8_dev(rW, VS2());
    WAITV(18);
    if (lane == 0 && lds_inc(L0 + L_SY + 16) == 3u && cg < 15) { u32x2 e = {elo, ehi}; st8_dev(rW, WS_FH + (unsigned)((rt * 16 + cg) * 128), e); }
    STAMP(8);
    {
      const int gf = ks0 / 6, gl = (ks0 + 20 + (wj < 2)) / 6, gi = min(gf + l7, gl);
      const unsigned vfh = WS_FH + (unsigned)(((r64 >> 1) * 16 + gi) * 128);
      u32x2 fh = ld8_dev(rW, vfh);
      asm volatile("s_waitcnt vmcnt(0)" : "+v"(fh), "+v"(xo), "+v"(st2)::"memory");
      spin_flags(rW, vfh, fh, elo, ehi);
    }
    STAMP(10);
    u32x4 hf[22][2];
    const unsigned vh = WS_H + (unsigned)((2 * r64 * 86 + ks0) * 1024 + lane * 16);
#define HL(j, t) hf[j][t] = ld16_dev(rW, vh, (min(PH(j), klast) + 86 * (t)) * 1024)
#pragma unroll
    for (int j = 0; j < HWIN; ++j) { HL(j, 0); HL(j, 1); }
    STAMP(28);
    lds_wait_ge(L0 + L_SY + wj * 4, 1u);
    f32x16 fo[2][2];
#pragma unroll
    for (int r = 0; r < 16; ++r) { fo[0][0][r] = 0.f; fo[0][1][r] = 0.f; fo[1][0][r] = 0.f; fo[1][1][r] = 0.f; }
#define G2(j) do { if ((j) + 2 >= 8 && (j) + 2 < 22) { wpk[((j) + 2) & 7][0] = WDF(UA((j) + 2), 0); wpk[((j) + 2) & 7][1] = WDF(UA((j) + 2), 1); } \
      asm volatile("s_waitcnt vmcnt(%2)" : "+v"(hf[j][0]), "+v"(hf[j][1]) : "i"(2 * ((j) + HWIN - 1 < 21 ? HWIN - 1 : 21 - (j))) : "memory");                              \
      fo[0][0] = MFMA32(wpk[(j) & 7][0], __builtin_bit_cast(bf16x8, hf[j][0]), fo[0][0]);                             \
      fo[0][1] = MFMA32(wpk[(j) & 7][1], __builtin_bit_cast(bf16x8, hf[j][0]), fo[0][1]); PIN(fo[0][0]); PIN(fo[0][1]); SB(); \
      if ((j) + HWIN < 22) HL(((j) + HWIN) % 22, 0);                                                                         \
      fo[1][0] = MFMA32(wpk[(j) & 7][0], __builtin_bit_cast(bf16x8, hf[j][1]), fo[1][0]);                             \
      fo[1][1] = MFMA32(wpk[(j) & 7][1], __builtin_bit_cast(bf16x8, hf[j][1]), fo[1][1]); PIN(fo[1][0]); PIN(fo[1][1]); SB(); \
      if ((j) + HWIN < 22) HL(((j) + HWIN) % 22, 1); } while (0)
    G2(0); STAMP(29); G2(1); G2(2); G2(3); G2(4); G2(5); G2(6); G2(7); G2(8); G2(9); G2(10);
    G2(11); G2(12); G2(13); G2(14); G2(15); G2(16); G2(17); G2(18); G2(19); G2(20); G2(21);
    STAMP(11);
#pragma unroll
    for (int t = 0; t < 2; ++t)
#pragma unroll
      for (int dt = 0; dt < 2; ++dt)
#pragma unroll
        for (int i = 0; i < 4; ++i)
          lds_st<f32x4>(L0 + L_B2 + t * SLOT + wj * 10240 + l31 * 256 + (((8 * dt + 2 * i + hh) ^ l7) << 4),
                        f32x4{fo[t][dt][4 * i], fo[t][dt][4 * i + 1], fo[t][dt][4 * i + 2], fo[t][dt][4 * i + 3]});
  }
  st2 = spin_tag(rW, VS2(), st2, elo);
  LBAR();
  STAMP(12);
  {
    u32x2 zt[2];
    const int o = 4 * (G >> 1) + pp;
#pragma unroll
    for (int j = 0; j < 2; ++j) { const int r = 8 * o + 4 * j + qq;
      zt[j] = __builtin_bit_cast(u32x2, trd(L0 + L_ZT + r * 128 + ((w ^ fsw(r)) << 4) + 8 * (G & 1))); }
    const unsigned pb = L0 + L_B2 + (w >> 2) * SLOT + (8 * wj + l3) * 256;
    f32x4 p0 = {0.f, 0.f, 0.f, 0.f}, p1 = p0;
#pragma unroll
    for (int k = 0; k < 4; ++k) {
      p0 += lds_ld<f32x4>(pb + k * 10240 + (((2 * l7) ^ l3) << 4));
      p1 += lds_ld<f32x4>(pb + k * 10240 + (((2 * l7 + 1) ^ l3) << 4));
    }
    const int src = 16 * (((l7 >> 2) << 1) | (l3 >> 2)) + 4 * (l7 & 3) + (l3 & 3);
    u32x4 zq;
    zq[0] = (unsigned)__builtin_amdgcn_ds_bpermute(src * 4, (int)zt[0].x); zq[1] = (unsigned)__builtin_amdgcn_ds_bpermute(src * 4, (int)zt[0].y);
    zq[2] = (unsigned)__builtin_amdgcn_ds_bpermute(src * 4, (int)zt[1].x); zq[3] = (unsigned)__builtin_amdgcn_ds_bpermute(src * 4, (int)zt[1].y);
    const float mean = blo(st2.x), rs = __builtin_amdgcn_rcpf(bhi(st2.x));
    float o8[8];
#pragma unroll
    for (int i = 0; i < 8; ++i) {
      const float xv = (i & 1) ? bhi(xo[i >> 1]) : blo(xo[i >> 1]), zv = (i & 1) ? bhi(zq[i >> 1]) : blo(zq[i >> 1]);
      const float F = i < 4 ? p0[i & 3] : p1[i & 3];
      const float gs = F * __builtin_amdgcn_rcpf(1.0f + __builtin_amdgcn_exp2f(F * -1.4426950408889634f));
      o8[i] = __builtin_fmaf((xv - mean) * rs * zv, gs, xv);
    }
    const u32x4 ov = {cvtpk(o8[0], o8[1]), cvtpk(o8[2], o8[3]), cvtpk(o8[4], o8[5]), cvtpk(o8[6], o8[7])};
    *(GAS u32x4*)((GAS char*)out + VXO()) = ov;
  }
}

extern "C" void kernel_launch(void* const* d_in, const int* in_sizes, int n_in, void* d_out, int out_size, void* d_ws, size_t ws_size,
                              hipStream_t stream) {
  (void)in_sizes; (void)n_in; (void)out_size; (void)ws_size;
  (void)hipFuncSetAttribute((const void*)h3_geglu_fused, hipFuncAttributeMaxDynamicSharedMemorySize, LDS_BYTES);
  h3_geglu_fused<<<256, 512, LDS_BYTES, stream>>>((const u16*)d_in[0], (const u16*)d_in[1], (const u16*)d_in[2], (const u16*)d_in[3],
                                                  (const u16*)d_in[4], (const u16*)d_in[5], (const u16*)d_in[7], (const u16*)d_in[8],
                                                  (const u16*)d_in[9], (u16*)d_out, (char*)d_ws);
}
```

```cpp
#define HWIN 11
#include <hip/hip_runtime.h>
#include <hip/hip_bf16.h>

typedef unsigned short u16;
typedef unsigned long long u64;
typedef __attribute__((ext_vector_type(8))) short bf16x8;
typedef __attribute__((ext_vector_type(4))) short s16x4;
typedef __attribute__((ext_vector_type(2))) __bf16 bf16x2_t;
typedef __attribute__((ext_vector_type(2))) float f32x2;
typedef __attribute__((ext_vector_type(4))) float f32x4;
typedef __attribute__((ext_vector_type(16))) float f32x16;
typedef __attribute__((ext_vector_type(4))) unsigned u32x4;
typedef __attribute__((ext_vector_type(2))) unsigned u32x2;
#define LAS __attribute__((address_space(3)))
#define GAS __attribute__((address_space(1)))
typedef __amdgpu_buffer_rsrc_t rsrc_t;

constexpr int S_ = 2048, D_ = 512, DF = 1376;
constexpr unsigned WS_GEN = 0, WS_FA = 4096, WS_FH = WS_FA + 256 * 128, WS_FZ = WS_FH + 256 * 128;
constexpr unsigned WS_ST = WS_FZ + 256 * 128;
constexpr unsigned WS_KVT = 1u << 20;
constexpr unsigned WS_ZT = WS_KVT + (2u << 20);
constexpr unsigned WS_H = WS_ZT + (2u << 20);
constexpr unsigned WS_BYTES = WS_H + 64 * 86 * 1024;
constexpr int SLOT = 40960;
constexpr int L_CV = 3 * SLOT;
constexpr int L_KA = L_CV, L_VA = L_KA + 9216;
constexpr int L_B1 = 0, L_B2 = SLOT, L_B3 = 2 * SLOT;
constexpr int L_CS = 32768;
constexpr int L_ST = L_CS + 3072;
constexpr int L_SY = L_ST + 512;
constexpr int L_A = L_VA + 8192;
constexpr int L_U0 = 36864, L_U1 = L_CV + 33280, L_ZT = L_CV;
constexpr int LDS_BYTES = 163840;
constexpr unsigned OOB = 0x80000000u;
constexpr unsigned SPIN_MAX = 1u << 18;

#define LPRIO 2
#define XSLEEP 4
#define ROT1 ((rt + cg) & 7)
#define ROT2 ((ct * 11) >> 2)
#define STEPMODE 1
#define NOSTATS 0
#define MFMA32(a, b, c) __builtin_amdgcn_mfma_f32_32x32x16_bf16(a, b, c, 0, 0, 0)
#define MFMA16(a, b, c) __builtin_amdgcn_mfma_f32_16x16x32_bf16(a, b, c, 0, 0, 0)
#define WAIT_BAR(N) asm volatile("s_waitcnt vmcnt(" #N ") lgkmcnt(0)\n\ts_barrier" ::: "memory")
#define LBAR() asm volatile("s_waitcnt lgkmcnt(0)\n\ts_barrier" ::: "memory")
#define WAITV(N) asm volatile("s_waitcnt vmcnt(" #N ")" ::: "memory")
#define SB() __builtin_amdgcn_sched_barrier(0)

__device__ __forceinline__ rsrc_t mk_rsrc(const void* p, unsigned bytes) {
  return __builtin_amdgcn_make_buffer_rsrc((void*)p, (short)0, (int)bytes, 0x00020000);
}
__device__ __forceinline__ void dma16(rsrc_t rs, unsigned voff, unsigned soff, unsigned lds) {
  asm volatile("s_mov_b32 m0, %0\n\ts_nop 0\n\tbuffer_load_dwordx4 %1, %2, %3 offen lds" ::"s"(lds), "v"(voff), "s"(rs), "s"(soff) : "memory");
}
__device__ __forceinline__ u32x4 ld16(rsrc_t rs, unsigned voff) {
  u32x4 r; asm volatile("buffer_load_dwordx4 %0, %1, %2, 0 offen" : "=v"(r) : "v"(voff), "s"(rs) : "memory"); return r;
}
__device__ __forceinline__ u32x4 ld16_dev(rsrc_t rs, unsigned voff, unsigned soff) {
  u32x4 r; asm volatile("buffer_load_dwordx4 %0, %1, %2, %3 offen sc1" : "=v"(r) : "v"(voff), "s"(rs), "s"(soff) : "memory"); return r;
}
__device__ __forceinline__ u32x2 ld8(rsrc_t rs, unsigned voff) {
  u32x2 r; asm volatile("buffer_load_dwordx2 %0, %1, %2, 0 offen" : "=v"(r) : "v"(voff), "s"(rs) : "memory"); return r;
}
__device__ __forceinline__ u32x2 ld8_dev(rsrc_t rs, unsigned voff) {
  u32x2 r; asm volatile("buffer_load_dwordx2 %0, %1, %2, 0 offen sc1" : "=v"(r) : "v"(voff), "s"(rs) : "memory"); return r;
}
#define PIN(x) asm volatile("" : "+v"(x))
__device__ __forceinline__ void st16_dev(rsrc_t rs, unsigned voff, u32x4 v) { __builtin_amdgcn_raw_buffer_store_b128(v, rs, voff, 0, 16); }
__device__ __forceinline__ void st8_dev(rsrc_t rs, unsigned voff, u32x2 v) { __builtin_amdgcn_raw_buffer_store_b64(v, rs, voff, 0, 16); }

__device__ __forceinline__ unsigned cvtpk(float lo, float hi) {
  bf16x2_t r = {(__bf16)lo, (__bf16)hi};
  return __builtin_bit_cast(unsigned, r);
}
__device__ __forceinline__ float blo(unsigned w) { return __uint_as_float(w << 16); }
__device__ __forceinline__ float bhi(unsigned w) { return __uint_as_float(w & 0xffff0000u); }
__device__ __forceinline__ float rbf(float f) { return blo(cvtpk(f, f)); }
__device__ __forceinline__ s16x4 trd(unsigned a) { return __builtin_amdgcn_ds_read_tr16_b64_v4i16((LAS s16x4*)(size_t)a); }
__device__ __forceinline__ bf16x8 cat(s16x4 a, s16x4 b) { return __builtin_shufflevector(a, b, 0, 1, 2, 3, 4, 5, 6, 7); }
template <class T> __device__ __forceinline__ T lds_ld(unsigned a) { return *(const LAS T*)(size_t)a; }
template <class T> __device__ __forceinline__ void lds_st(unsigned a, T v) { *(LAS T*)(size_t)a = v; }
template <int CTRL> __device__ __forceinline__ float dpp_add(float x) {
  return x + __uint_as_float(__builtin_amdgcn_update_dpp(0u, __float_as_uint(x), CTRL, 0xf, 0xf, true));
}
__device__ __forceinline__ float swap_add(float x, bool half) {
  if (half) { auto r = __builtin_amdgcn_permlane32_swap(__float_as_uint(x), __float_as_uint(x), false, false); return __uint_as_float(r[0]) + __uint_as_float(r[1]); }
  auto r = __builtin_amdgcn_permlane16_swap(__float_as_uint(x), __float_as_uint(x), false, false); return __uint_as_float(r[0]) + __uint_as_float(r[1]);
}
__device__ __forceinline__ void wave_sum4(float& a, float& b, float& c, float& d) {
  a = dpp_add<0x128>(a); b = dpp_add<0x128>(b); c = dpp_add<0x128>(c); d = dpp_add<0x128>(d);
  a = dpp_add<0x124>(a); b = dpp_add<0x124>(b); c = dpp_add<0x124>(c); d = dpp_add<0x124>(d);
  a = dpp_add<0x122>(a); b = dpp_add<0x122>(b); c = dpp_add<0x122>(c); d = dpp_add<0x122>(d);
  a = dpp_add<0x121>(a); b = dpp_add<0x121>(b); c = dpp_add<0x121>(c); d = dpp_add<0x121>(d);
  a = swap_add(a, false); b = swap_add(b, false); c = swap_add(c, false); d = swap_add(d, false);
  a = swap_add(a, true); b = swap_add(b, true); c = swap_add(c, true); d = swap_add(d, true);
}
__device__ __forceinline__ void lds_wait_ge(unsigned a, unsigned v) {
  for (unsigned n = 0; *(volatile LAS unsigned*)(size_t)a < v && n < SPIN_MAX * 16; ++n) __builtin_amdgcn_s_sleep(1);
  asm volatile("" ::: "memory");
}
__device__ __forceinline__ void lds_spin_ge(unsigned a, unsigned v) {
  for (unsigned n = 0; *(volatile LAS unsigned*)(size_t)a < v && n < SPIN_MAX * 64; ++n) {}
  asm volatile("" ::: "memory");
}
__device__ __forceinline__ unsigned lds_inc(unsigned a) {
  return __hip_atomic_fetch_add((LAS unsigned*)(size_t)a, 1u, __ATOMIC_RELAXED, __HIP_MEMORY_SCOPE_WORKGROUP);
}
__device__ __forceinline__ int fsw(int r) { return ((r >> 1) & 1) | (((r >> 3) & 3) << 1); }
__device__ __forceinline__ void spin_flags(rsrc_t rs, unsigned voff, u32x2 f, unsigned lo, unsigned hi) {
  for (unsigned n = 0; !__all(f.x == lo && f.y == hi) && n < SPIN_MAX; ++n) {
    __builtin_amdgcn_s_sleep(1);
    f = ld8_dev(rs, voff); WAITV(0); PIN(f);
  }
}
__device__ __forceinline__ u32x2 spin_tag(rsrc_t rs, unsigned voff, u32x2 f, unsigned tag) {
  for (unsigned n = 0; !__all(f.y == tag) && n < SPIN_MAX; ++n) {
    __builtin_amdgcn_s_sleep(1);
    f = ld8_dev(rs, voff); WAITV(0); PIN(f);
  }
  return f;
}
#define STAMP(i) do { if ((i) == 0) { prb0 = __builtin_amdgcn_s_memrealtime(); prb1 = prb0; } if ((i) == 3) { SB(); prb1 = __builtin_amdgcn_s_memrealtime(); SB(); } } while (0)

__global__ __launch_bounds__(512, 2) void h3_geglu_fused(const u16* __restrict__ x, const u16* __restrict__ kin, const u16* __restrict__ vin,
                                                         const u16* __restrict__ shk, const u16* __restrict__ ssk, const u16* __restrict__ ssd,
                                                         const u16* __restrict__ wg, const u16* __restrict__ wu, const u16* __restrict__ wd,
                                                         u16* __restrict__ out, char* __restrict__ ws) {
  extern __shared__ __attribute__((aligned(16))) char smem[];
  const unsigned L0 = (unsigned)(size_t)(LAS char*)smem;
  const int tid = threadIdx.x, lane = tid & 63;
  const int w = __builtin_amdgcn_readfirstlane(tid >> 6), wj = w & 3;
  u64 prb0, prb1;
  STAMP(0);
  const int b = blockIdx.x, xcd = b & 7, idx = b >> 3, rh = xcd & 1, q4 = xcd >> 1;
  const int pan = 4 * rh + q4, aa = 32 * pan + idx;
  const int l3 = lane >> 3, l7 = lane & 7, G = lane >> 4, qq = (lane >> 2) & 3, pp = lane & 3, hh = lane >> 5, l31 = lane & 31;
  const rsrc_t rX = mk_rsrc(x, S_ * D_ * 2), rW = mk_rsrc(ws, WS_BYTES), rD = mk_rsrc(wd, DF * D_ * 2);
  const int cg = idx < 24 ? ((q4 * 7 + 1) >> 1) + (idx >> 3) : idx < 28 ? (q4 < 2 ? 3 : 10) : 14 + ((idx >> 1) & 1);
  const int rt = 8 * rh + (idx < 24 ? (idx & 7) : idx < 28 ? (q4 & 1) * 4 + (idx & 3) : 2 * q4 + (idx & 1));
  const int r64 = 4 * pan + (idx >> 3), ct = idx & 7;
  const int ks0 = 22 * wj - (wj == 3 ? 1 : 0);
  u32x4 xo; u32x2 st2;
  unsigned elo, ehi;
#define VD() (unsigned)((16 * ks0 + l3) * 1024 + ct * 128 + ((l7 ^ (((l3 >> 1) & 1) << 2)) << 4))
  const int rot2 = ROT2;
#define PH(j) ((j) + rot2 >= 22 ? (j) + rot2 - 22 : (j) + rot2)
  const int klast = wj < 2 ? 21 : 20;
#define WDU(j, lds) do { const unsigned v_ = vd | (PH(j) > klast ? OOB : 0u);                                       \
    dma16(rD, v_, PH(j) * 16384, L0 + (lds)); dma16(rD, v_, PH(j) * 16384 + 8192, L0 + (lds) + 1024); } while (0)
#define VXO() (unsigned)((64 * r64 + 8 * w + l3) * 1024 + ct * 128 + l7 * 16)
#define VS2() (WS_ST + (unsigned)((64 * r64 + 8 * w + l3) * 8))

  if (w >= 4) {
    __builtin_amdgcn_s_setprio(2);
    u32x2 gen = ld8_dev(rW, WS_GEN);
    const unsigned vx = (cg < 15) ? (unsigned)((128 * rt + 8 * wj + l3) * 1024 + ((l7 ^ ((4 * wj + (l3 >> 1)) & 7)) << 4)) : OOB;
    unsigned vg[3], vu[3];
#pragma unroll
    for (int r = 0; r < 3; ++r) {
      const unsigned P = 64 * r + lane, k = (P * 43691u) >> 19, col = 96 * cg + 8 * (P - 12 * k);
      vg[r] = (col < DF) ? (16 * wj + k) * (DF * 2) + col * 2 : OOB; vu[r] = vg[r] ^ 32u;
    }
    const rsrc_t rG = mk_rsrc(wg, D_ * DF * 2), rU = mk_rsrc(wu, D_ * DF * 2);
    const int rot1 = ROT1;
    unsigned dpair = ((const unsigned*)ssd)[aa];
    asm volatile("" : "+s"(dpair));
    if (tid == 256) lds_st<unsigned>(L0 + L_A, 0u);
    LBAR();
#pragma nounroll
    for (int c = 0; c < 8; ++c) {
      const int cc = (c + rot1) & 7;
      const unsigned sx = cc * 128, sw = cc * (64 * DF * 2), lx = L0 + (c & 3) * SLOT + wj * 1024, lw = L0 + (c & 3) * SLOT + 16384 + wj * 3072;
      dma16(rX, vx, sx, lx);                 dma16(rG, vg[0], sw, lw);        dma16(rU, vu[0], sw, lw + 12288);
      dma16(rX, vx, sx + 32768, lx + 4096);  dma16(rG, vg[1], sw, lw + 1024); dma16(rU, vu[1], sw, lw + 13312);
      if (c >= 2) asm volatile("s_waitcnt vmcnt(16)\n\ts_barrier" ::: "memory");
      if (c == 2) STAMP(16);
      if (c == 3) STAMP(18);
      if (c == 7) STAMP(23);
      if (c == 5 && tid == 256) {
        PIN(gen); const unsigned lo = gen.x + 1u;
        st8_dev(rW, WS_FA + (unsigned)(aa * 128), u32x2{lo, gen.y + (lo == 0u ? 1u : 0u)});
      }
      dma16(rX, vx, sx + 65536, lx + 8192);  dma16(rG, vg[2], sw, lw + 2048); dma16(rU, vu[2], sw, lw + 14336);
      dma16(rX, vx, sx + 98304, lx + 12288);
    }
    PIN(gen);
    elo = __builtin_amdgcn_readfirstlane(gen.x) + 1u;
    ehi = __builtin_amdgcn_readfirstlane(gen.y) + (elo == 0u ? 1u : 0u);
    const int ce = tid & 255;
    const unsigned vd = VD();
    const unsigned vfa = WS_FA + (unsigned)((32 * pan + l31) * 128), vst = WS_ST + (unsigned)((128 * rt + 64 * (wj & 1) + lane) * 8);
    const u32x4 zero4 = {0u, 0u, 0u, 0u};
    u32x2 stg = ld8_dev(rW, vst), fa = ld8_dev(rW, vfa);
#pragma unroll
    for (int j = 0; j < 4; ++j) WDU(j, L_B1 + wj * 8192 + j * 2048);
    if (ce < 4) lds_st<u32x4>(L0 + L_SY + ce * 16, zero4);
    WAIT_BAR(20);
#pragma unroll
    for (int j = 0; j < 5; ++j) WDU(4 + j, L_B2 + wj * 10240 + j * 2048);
    asm volatile("s_waitcnt vmcnt(18)\n\ts_barrier" : "+v"(stg), "+v"(fa)::"memory");
    STAMP(4);
    stg = spin_tag(rW, vst, stg, elo);
    if (wj < 2) lds_st<unsigned>(L0 + L_ST + (64 * wj + lane) * 4, stg.x);
    spin_flags(rW, vfa, fa, elo, ehi);
    u32x4 hc[2], hp[2], ur[2];
    {
      const rsrc_t rHk = mk_rsrc(ssk, D_ * S_ * 2);
#pragma unroll
      for (int c = 0; c < 2; ++c) {
        hc[c] = ld16(rHk, (unsigned)((2 * aa + c) * 4096 + ce * 16));
        hp[c] = ld16(rHk, ce ? (unsigned)((2 * aa + c) * 4096 + ce * 16 - 16) : OOB);
        ur[c] = ld16_dev(rW, WS_KVT + (unsigned)(2 * aa * 4096 + ce * 16), c * 4096);
      }
    }
#pragma unroll
    for (int j = 0; j < 2; ++j) WDU(9 + j, L_B3 + wj * 10240 + j * 2048);
    WAIT_BAR(10);
    STAMP(5);
#pragma unroll
    for (int j = 2; j < 5; ++j) WDU(9 + j, L_B3 + wj * 10240 + j * 2048);
    {
      asm volatile("s_waitcnt vmcnt(10)" : "+v"(hc[0]), "+v"(hc[1]), "+v"(hp[0]), "+v"(hp[1]), "+v"(ur[0]), "+v"(ur[1])::"memory");
      auto put = [&](unsigned hb, u32x4 pv, u32x4 cu, int blk) {
        const unsigned a0 = __builtin_amdgcn_alignbit(pv[1], pv[0], 16), a1 = __builtin_amdgcn_alignbit(pv[2], pv[1], 16),
                       a2 = __builtin_amdgcn_alignbit(pv[3], pv[2], 16), a3 = __builtin_amdgcn_alignbit(cu[0], pv[3], 16),
                       a4 = __builtin_amdgcn_alignbit(cu[1], cu[0], 16);
        lds_st<u32x4>(hb + blk * 16, u32x4{a0, a1, a2, a3});
        lds_st<u32x4>(hb + 4160 + blk * 16, u32x4{pv[1], pv[2], pv[3], cu[0]});
        lds_st<u32x4>(hb + 8320 + blk * 16, u32x4{a1, a2, a3, a4});
        lds_st<u32x4>(hb + 12480 + blk * 16, u32x4{pv[2], pv[3], cu[0], cu[1]});
      };
#pragma unroll
      for (int c = 0; c < 2; ++c) {
        put(L0 + L_CV + c * 16640, hp[c], hc[c], ce + 1);
        if (ce == 0 || ce == 255) put(L0 + L_CV + c * 16640, ce ? hc[c] : zero4, zero4, ce ? 257 : 0);
        lds_st<u32x4>(L0 + (c ? L_U1 : L_U0) + ce * 16, ur[c]);
      }
      asm volatile("s_waitcnt vmcnt(0) lgkmcnt(0)" ::: "memory");
      *(volatile LAS unsigned*)(size_t)(L0 + L_SY + wj * 4) = 1u;
      if (lane == 0) lds_inc(L0 + L_SY + 24);
    }
    STAMP(7);
    const int vch = wj >> 1, vrh = wj & 1, c15 = lane & 15;
    f32x4 ya[8];
#pragma unroll
    for (int c = 0; c < 8; ++c) ya[c] = f32x4{0.f, 0.f, 0.f, 0.f};
    {
      unsigned cA = L0 + L_CV + vch * 16640 + (c15 & 3) * 4160 + (4 * (c15 >> 2) + 8 * G + 128 * vrh) * 2;
      const unsigned ub = L0 + (vch ? L_U1 : L_U0);
      unsigned cB = ub + (240 - 16 * c15 + 8 * G + 128 * vrh) * 2;
      int lim = ((16 * c15 - 8 * G + 47) >> 5) - 4 * vrh;
      PIN(cA); PIN(cB);
      bf16x8 bfr[2][8]; s16x4 ah[2][4];
#define VLD8(a) (*(const volatile LAS s16x4*)(size_t)(a))
#define CA(set, r, p, i) ah[set][i] = VLD8(cA + (r) * 64 + (((i) & 2) ? 7 - (p) : (p)) * 512 + ((i) & 1) * 8)
#define CB(r, mu) bfr[(r) & 1][mu] = lds_ld<bf16x8>((mu) == 0 ? ((r) >= lim ? L0 + L_SY + 32 : cB + (r) * 64 + 3584) : cB + (r) * 64 + (7 - (mu)) * 512)
#define CM(r, p, m) do { if ((m) + (p) < 8) { ya[(p) + (m)] = MFMA16(cat(ah[(p) & 1][0], ah[(p) & 1][1]), bfr[(r) & 1][(m) & 7], ya[((p) + (m)) & 7]); PIN(ya[((p) + (m)) & 7]); } \
        else { ya[(m) - 1] = MFMA16(cat(ah[(p) & 1][2], ah[(p) & 1][3]), bfr[(r) & 1][((m) + (p)) & 7], ya[(m) - 1]); PIN(ya[(m) - 1]); } } while (0)
#define CPAIR(r, p) do { _Pragma("unroll") for (int m = 0; m < 9; ++m) { CM(r, p, m);                                               \
          if (m < 4) CA(((p) + 1) & 1, (p) < 3 ? (r) : (r) + 1, ((p) + 1) & 3, m); else if (m < 6) CB((r) + 1, 2 * (p) + m - 4); SB(); } } while (0)
#define CONV(r) do { CPAIR(r, 0); CPAIR(r, 1); CPAIR(r, 2); CPAIR(r, 3); } while (0)
      lds_wait_ge(L0 + L_SY + 24, 8u); __builtin_amdgcn_s_setprio(2);
      STAMP(24);
#pragma unroll
      for (int i = 0; i < 4; ++i) CA(0, 0, 0, i);
#pragma unroll
      for (int mu = 0; mu < 8; ++mu) CB(0, mu);
#pragma nounroll
      for (int it = 0; it < 2; ++it) {
        CONV(0); CPAIR(1, 0); CPAIR(1, 1); CPAIR(1, 2);
        if (it) { asm volatile("s_waitcnt lgkmcnt(0)" ::: "memory"); *(volatile LAS unsigned*)(size_t)(L0 + L_SY + 48 + wj * 4) = 1u; }
        CPAIR(1, 3);
        cA += 128; cB += 128; lim -= 2; PIN(cA); PIN(cB);
      }
    }
    STAMP(27);
    {
      const unsigned mine = L0 + L_SY + 48 + wj * 4, other = L0 + L_SY + 48 + (wj ^ 1) * 4;
      const unsigned xo_ = L0 + L_CV + vch * 16640 + (vrh ^ 1) * 4096 + lane * 16, xb = L0 + L_CV + vch * 16640 + vrh * 4096;
      const float dsc = vch ? bhi(dpair) : blo(dpair);
      u32x2 uq[4];
#pragma unroll
      for (int c = 0; c < 4; ++c) uq[c] = lds_ld<u32x2>(L0 + (vch ? L_U1 : L_U0) + (2044 - 256 * (4 * vrh + c) - 16 * c15 - 4 * G) * 2);
      lds_spin_ge(other, 1u);
      f32x4 y[4];
      if (vrh) {
        asm volatile("" ::: "memory");
#pragma unroll
        for (int c = 0; c < 4; ++c) { lds_st<f32x4>(xo_ + c * 1024, ya[c]); y[c] = ya[4 + c]; PIN(y[c]); }
      } else {
#pragma unroll
        for (int c = 0; c < 4; ++c) { lds_st<f32x4>(xo_ + c * 1024, ya[4 + c]); y[c] = ya[c]; PIN(y[c]); }
        asm volatile("" ::: "memory");
      }
      asm volatile("s_waitcnt lgkmcnt(0)" ::: "memory");
      *(volatile LAS unsigned*)(size_t)mine = 2u;
#pragma unroll
      for (int c = 0; c < 4; ++c)
#pragma unroll
        for (int e = 0; e < 4; ++e) {
          const unsigned uu = (e & 2) ? uq[c].x : uq[c].y;
          float s = __builtin_fmaf((e & 1) ? blo(uu) : bhi(uu), dsc, y[c][e]); PIN(s); y[c][e] = s;
        }
      lds_spin_ge(other, 2u);
      f32x4 o[4];
#pragma unroll
      for (int c = 0; c < 4; ++c) o[c] = lds_ld<f32x4>(xb + c * 1024 + lane * 16);
      SB();
      u32x2 zp[4];
#pragma unroll
      for (int c = 0; c < 4; ++c) {
        float z[4];
#pragma unroll
        for (int e = 0; e < 4; ++e) { z[e] = y[c][e] + o[c][e]; asm("" : "+v"(z[e])); }
        zp[c] = u32x2{cvtpk(z[0], z[1]), cvtpk(z[2], z[3])};
      }
#pragma unroll
      for (int c = 0; c < 4; ++c) lds_st<u32x2>(xb + c * 512 + c15 * 32 + G * 8, zp[c]);
#pragma unroll
      for (int i = 0; i < 2; ++i)
        st16_dev(rW, WS_ZT + (unsigned)((2 * aa + vch) * 4096 + vrh * 2048 + i * 1024 + lane * 16), lds_ld<u32x4>(xb + i * 1024 + lane * 16));
    }
    STAMP(26);
    xo = ld16(rX, VXO()); st2 = ld8_dev(rW, VS2());
    asm volatile("s_waitcnt vmcnt(2)" ::: "memory");
    if (lane == 0 && lds_inc(L0 + L_SY + 20) == 3u) { u32x2 e = {elo, ehi}; st8_dev(rW, WS_FZ + (unsigned)(aa * 128), e); }
    STAMP(9);
    if (b == 0 && wj == 0) {
#pragma nounroll
      for (int i = 0; i < 4; ++i) {
        const unsigned v = WS_FA + (unsigned)((64 * i + lane) * 128);
        spin_flags(rW, v, u32x2{~elo, 0u}, elo, ehi);
      }
      if (lane == 0) { u32x2 e = {elo, ehi}; st8_dev(rW, WS_GEN, e); }
    }
    {
      const unsigned vfz = WS_FZ + (unsigned)((32 * ct + l31) * 128);
      u32x2 fz = ld8_dev(rW, vfz);
      asm volatile("s_waitcnt vmcnt(0)" : "+v"(fz), "+v"(xo), "+v"(st2)::"memory");
      spin_flags(rW, vfz, fz, elo, ehi);
      STAMP(30);
      const unsigned vz = WS_ZT + (unsigned)((64 * ct + 16 * wj + l3) * 4096 + r64 * 128 + l7 * 16);
      u32x4 z0 = ld16_dev(rW, vz, 0), z1 = ld16_dev(rW, vz, 32768);
      lds_wait_ge(L0 + L_SY + 20, 4u);
      asm volatile("s_waitcnt vmcnt(0)" : "+v"(z0), "+v"(z1)::"memory");
      const int r = 16 * wj + l3;
      lds_st<u32x4>(L0 + L_ZT + r * 128 + ((l7 ^ fsw(r)) << 4), z0);
      lds_st<u32x4>(L0 + L_ZT + (r + 8) * 128 + ((l7 ^ fsw(r + 8)) << 4), z1);
    }
    STAMP(31);
  } else {
    __builtin_amdgcn_s_setprio(3);
    u32x2 sh[2]; u32x4 xr[2];
#pragma unroll
    for (int i = 0; i < 2; ++i) xr[i] = ld16(rX, (unsigned)((8 * aa + 2 * wj + i) * 1024 + lane * 16));
    {
      const rsrc_t rK = mk_rsrc(kin, S_ * D_ * 2), rV = mk_rsrc(vin, S_ * D_ * 2);
      const unsigned kb = (unsigned)((64 * idx + l3) * 1024 + pan * 128), fl = (unsigned)(l7 ^ ((l3 >> 1) & 1));
#pragma unroll
      for (int i = 0; i < 2; ++i) {
        const int g = 2 * wj + i;
        dma16(rK, kb + ((fl ^ (((g + 1) & 3) << 1)) << 4), g * 8192, L0 + L_KA + (g + 1) * 1024);
        dma16(rV, kb + ((fl ^ ((g & 3) << 1)) << 4), g * 8192, L0 + L_VA + g * 1024);
      }
      if (wj == 0) dma16(rK, idx == 0 ? OOB : kb - 8192 + (fl << 4), 0, L0 + L_KA);
    }
    u32x2 gen = ld8_dev(rW, WS_GEN);
    {
      const rsrc_t rSh = mk_rsrc(shk, D_ * 4 * 2);
#pragma unroll
      for (int i = 0; i < 2; ++i) sh[i] = ld8(rSh, (unsigned)((64 * pan + 16 * wj + 8 * i + 4 * (G & 1) + pp) * 8));
    }
    asm volatile("s_barrier" ::: "memory");
    STAMP(3);
    const int tp = wj & 1, ch = wj >> 1;
    f32x16 acc[2][3];
    f32x4 cs[3];
    unsigned ax[4], aw[2][3], ak[5];
    unsigned vkv = WS_KVT + (unsigned)((64 * pan + 16 * wj + l3) * 4096 + (1984 - 64 * idx + 8 * l7) * 2);
    unsigned vst0 = lane < 2 ? WS_ST + (unsigned)((8 * aa + 2 * wj + lane) * 8) : OOB;
    int src4 = 4 * (16 * ((((7 - l7) >> 2) << 1) | (l3 >> 2)) + 4 * ((7 - l7) & 3) + (l3 & 3));
    const unsigned so_ = ((lane & 15) == 4 * (G & 1)) ? 0x3f803f80u : 0u;
    bf16x8 sel = __builtin_bit_cast(bf16x8, u32x4{so_, so_, so_, so_});
    {
      const unsigned ax0 = L0 + (64 * tp + l31) * 128 + (((4 * tp + hh) ^ ((l31 >> 1) & 7)) << 4);
      const unsigned aw0 = L0 + 16384 + (G & 1) * 12288 + (8 * hh + qq) * 192 + ((pp & 1) << 4) + 8 * (pp >> 1);
#pragma unroll
      for (int s = 0; s < 4; ++s) { ax[s] = ax0 ^ (s << 5); PIN(ax[s]); }
#pragma unroll
      for (int i = 0; i < 3; ++i) {
        const unsigned a_ = aw0 + (((3 * ch + i) ^ (G & 1)) << 5);
        aw[0][i] = a_ + tp * 6144; aw[1][i] = a_ + 6144 - tp * 6144; PIN(aw[0][i]); PIN(aw[1][i]);
      }
#pragma unroll
      for (int j = 0; j < 5; ++j) { const int rr = 8 * (4 * (G >> 1) + pp) + qq + (j < 3 ? 4 + 4 * j : 4 * (j - 3));
        ak[j] = L0 + (j < 3 ? L_KA : L_VA) + rr * 128 + (((2 * wj) ^ fsw(rr)) << 4) + 8 * (G & 1); PIN(ak[j]); }
      PIN(vkv); PIN(vst0); PIN(src4); PIN(sel);
    }
    {
      asm volatile("s_waitcnt vmcnt(7)" : "+v"(xr[0]), "+v"(xr[1])::"memory");
      STAMP(14);
      const f32x2 x0 = {blo(__builtin_amdgcn_readfirstlane(xr[0][0])), blo(__builtin_amdgcn_readfirstlane(xr[1][0]))};
      f32x2 d[8];
#pragma unroll
      for (int e = 0; e < 4; ++e) { d[2 * e] = f32x2{blo(xr[0][e]), blo(xr[1][e])} - x0; d[2 * e + 1] = f32x2{bhi(xr[0][e]), bhi(xr[1][e])} - x0; }
      const f32x2 sm = ((d[0] + d[1]) + (d[2] + d[3])) + ((d[4] + d[5]) + (d[6] + d[7]));
      const f32x2 sq = ((d[0] * d[0] + d[1] * d[1]) + (d[2] * d[2] + d[3] * d[3])) + ((d[4] * d[4] + d[5] * d[5]) + (d[6] * d[6] + d[7] * d[7]));
      float s0 = sm[0], s1 = sm[1], q0 = sq[0], q1 = sq[1];
      wave_sum4(s0, s1, q0, q1);
      const float m = (lane ? s1 : s0) * (1.0f / D_), var = (lane ? q1 : q0) * (1.0f / D_) - m * m;
      unsigned ms0 = cvtpk(m + (lane ? x0[1] : x0[0]), __builtin_amdgcn_sqrtf(rbf(rbf(__builtin_fmaxf(var, 0.f)) + 1e-5f)));
      asm volatile("s_waitcnt vmcnt(0)" : "+v"(gen), "+v"(sh[0]), "+v"(sh[1]), "+v"(ms0)::"memory");
      STAMP(15);
      if (lane == 0) lds_inc(L0 + L_A);
      elo = __builtin_amdgcn_readfirstlane(gen.x) + 1u;
      ehi = __builtin_amdgcn_readfirstlane(gen.y) + (elo == 0u ? 1u : 0u);
      st8_dev(rW, vst0, u32x2{ms0, elo});
      f32x2 sk[4] = {{blo(sh[0].x), blo(sh[1].x)}, {bhi(sh[0].x), bhi(sh[1].x)}, {blo(sh[0].y), blo(sh[1].y)}, {bhi(sh[0].y), bhi(sh[1].y)}};
      PIN(sk[0]); PIN(sk[1]); PIN(sk[2]); PIN(sk[3]);
      lds_spin_ge(L0 + L_A, 4u);
      STAMP(1);
      u32x2 t[2][5];
#pragma unroll
      for (int j = 0; j < 5; ++j) { t[0][j] = __builtin_bit_cast(u32x2, trd(ak[j])); t[1][j] = __builtin_bit_cast(u32x2, trd(ak[j] ^ 16)); }
      SB();
      f32x2 kk[20], r[8];
#pragma unroll
      for (int j = 0; j < 5; ++j) {
        kk[4 * j] = f32x2{blo(t[0][j].x), blo(t[1][j].x)}; kk[4 * j + 1] = f32x2{bhi(t[0][j].x), bhi(t[1][j].x)};
        kk[4 * j + 2] = f32x2{blo(t[0][j].y), blo(t[1][j].y)}; kk[4 * j + 3] = f32x2{bhi(t[0][j].y), bhi(t[1][j].y)};
      }
#pragma unroll
      for (int e = 0; e < 8; ++e) r[e] = kk[e + 1] * sk[3];
#pragma unroll
      for (int j = 2; j >= 0; --j)
#pragma unroll
        for (int e = 0; e < 8; ++e) r[e] = __builtin_elementwise_fma(kk[e + 4 - j], sk[j], r[e]);
#pragma unroll
      for (int e = 0; e < 8; ++e) r[e] *= kk[12 + e];
      u32x4 po[2];
#pragma unroll
      for (int i = 0; i < 2; ++i)
#pragma unroll
        for (int e = 0; e < 4; ++e) po[i][e] = (unsigned)__builtin_amdgcn_ds_bpermute(src4, (int)cvtpk(r[7 - 2 * e][i], r[6 - 2 * e][i]));
      st16_dev(rW, vkv, po[0]); st16_dev(rW, vkv + 32768, po[1]);
      __builtin_amdgcn_s_setprio(1);
    }
    STAMP(2);
    {
      bf16x8 xf[2][2]; s16x4 wf[2][3][2];
#define PINA(t, i) PIN(acc[t][i])
#define M1(p, t, i) do { acc[t][i] = MFMA32(cat(wf[p][i][0], wf[p][i][1]), xf[p][t], acc[t][i]); PINA(t, i); } while (0)
#define MS(p, i, on) do { if (on) { cs[i] = MFMA16(sel, cat(wf[p][i][0], wf[p][i][1]), cs[i]); PIN(cs[i]); } } while (0)
#define RW(s, i) do { wf[(s) & 1][i][0] = trd(aw[(s) >> 1][i] + ((s) & 1) * 3072); wf[(s) & 1][i][1] = trd(aw[(s) >> 1][i] + ((s) & 1) * 3072 + 768); } while (0)
#define RX(s, t) xf[(s) & 1][t] = lds_ld<bf16x8>(ax[s] + (t) * 4096)
#define KEEP(p) do { asm volatile("" ::"v"(wf[p][0][0]), "v"(wf[p][0][1]), "v"(wf[p][1][0]), "v"(wf[p][1][1]), "v"(wf[p][2][0]), "v"(wf[p][2][1]), "v"(xf[p][0]), "v"(xf[p][1])); } while (0)
#define ADV(a) do { a += dl; PIN(a); } while (0)
#define STEP(p, s, on) do { M1(p, 0, 0); RW(s, 0); SB(); M1(p, 1, 0); RX(s, 0); RX(s, 1); SB(); M1(p, 0, 1); RW(s, 1); SB();  \
        M1(p, 1, 1); RW(s, 2); SB(); M1(p, 0, 2);                                                                             \
        if ((s) == 3) { ADV(ax[0]); ADV(ax[1]); ADV(ax[2]); ADV(ax[3]); ADV(aw[0][0]); } SB(); M1(p, 1, 2);                     \
        if ((s) == 3) { ADV(aw[0][1]); ADV(aw[0][2]); ADV(aw[1][0]); ADV(aw[1][1]); ADV(aw[1][2]); }                            \
        MS(p, 0, on); MS(p, 1, on); MS(p, 2, on); KEEP(p); SB(); } while (0)
      LBAR();
      STAMP(16);
      RW(0, 0); RX(0, 0); RX(0, 1); RW(0, 1); RW(0, 2); SB();
      unsigned dl = SLOT;
#pragma unroll
      for (int i = 0; i < 3; ++i) {
        cs[i] = f32x4{0.f, 0.f, 0.f, 0.f};
#pragma unroll
        for (int r = 0; r < 16; ++r) { acc[0][i][r] = 0.f; acc[1][i][r] = 0.f; }
      }
      STEP(0, 1, true);
#pragma nounroll
      for (int c = 0; c < 7; ++c) {
        dl = (c == 3) ? 0u - 3u * SLOT : (unsigned)SLOT;
        STEP(1, 2, true);
        if (c == 2) WAITV(0);
        STEP(0, 3, false);
        LBAR();
        STEP(1, 0, false); STEP(0, 1, true);
      }
      STAMP(4);
      STEP(1, 2, true);
#pragma unroll
      for (int i = 0; i < 3; ++i) lds_st<float>(L0 + L_CS + (wj * 3 + i) * 256 + lane * 4, cs[i][0]);
      STEP(0, 3, false);
      LBAR();
      float co[3]; unsigned ms[2];
#pragma unroll
      for (int i = 0; i < 3; ++i) co[i] = lds_ld<float>(L0 + L_CS + ((wj ^ 1) * 3 + i) * 256 + lane * 4);
#pragma unroll
      for (int t = 0; t < 2; ++t) ms[t] = lds_ld<unsigned>(L0 + L_ST + (64 * tp + 32 * t + l31) * 4);
      SB();
      M1(1, 0, 0); M1(1, 1, 0); M1(1, 0, 1); M1(1, 1, 1); M1(1, 0, 2); M1(1, 1, 2); SB();
      float k0[2], k1[2], s2[2];
      u32x4 bm[2];
#pragma unroll
      for (int t = 0; t < 2; ++t) {
        const unsigned mn = (ms[t] & 0xffffu) ^ 0x8000u;
        bm[t] = u32x4{mn | (mn << 16), mn, 0u, 0u};
        const float sd = bhi(ms[t]), rs = __builtin_amdgcn_rcpf(sd);
        k0[t] = rs * -2.3022081983f; k1[t] = k0[t] * rs * rs * 0.044715f; s2[t] = sd * sd;
      }
#pragma unroll
      for (int i = 0; i < 3; ++i) {
        const float c = cs[i][0] + co[i], c1 = c - rbf(c), c2 = c1 - rbf(c1);
        const u32x4 am = {cvtpk(c, c1), cvtpk(c2, 0.f), 0u, 0u};
#pragma unroll
        for (int t = 0; t < 2; ++t) { acc[t][i] = MFMA32(__builtin_bit_cast(bf16x8, am), __builtin_bit_cast(bf16x8, bm[t]), acc[t][i]); PINA(t, i); }
      }
      STAMP(5);
      const unsigned vhs = WS_H + (unsigned)(((4 * rt + 2 * tp) * 86 + 6 * cg + 3 * ch) * 1024 + lane * 16);
#pragma unroll
      for (int i = 0; i < 3; ++i) {
        const unsigned vo = (vhs + i * 1024) | (6 * cg + 3 * ch + i < 86 ? 0u : OOB);
#pragma unroll
        for (int t = 0; t < 2; ++t) {
          f32x2 g[4], zz[4], gu[4];
#pragma unroll
          for (int r = 0; r < 4; ++r) {
            g[r] = f32x2{acc[t][i][2 * r], acc[t][i][2 * r + 1]};
            zz[r] = g[r] * __builtin_elementwise_fma(g[r] * g[r], f32x2{k1[t], k1[t]}, f32x2{k0[t], k0[t]});
          }
#pragma unroll
          for (int r = 0; r < 4; ++r) {
            zz[r] = f32x2{__builtin_amdgcn_exp2f(zz[r][0]), __builtin_amdgcn_exp2f(zz[r][1])};
            gu[r] = g[r] * f32x2{acc[t][i][8 + 2 * r], acc[t][i][9 + 2 * r]};
          }
          u32x4 hq;
#pragma unroll
          for (int r = 0; r < 4; ++r) {
            const f32x2 dd = __builtin_elementwise_fma(zz[r], f32x2{s2[t], s2[t]}, f32x2{s2[t], s2[t]});
            const f32x2 hv = gu[r] * f32x2{__builtin_amdgcn_rcpf(dd[0]), __builtin_amdgcn_rcpf(dd[1])};
            hq[r] = cvtpk(hv[0], hv[1]);
          }
          st16_dev(rW, vo + t * (86 * 1024), hq);
        }
      }
    }
    if (lane == 0) lds_inc(L0 + L_SY + 24);
    STAMP(6);
    const unsigned ad = L0 + (8 * hh + qq) * 128 + ((4 * ((qq >> 1) & 1) + 2 * (G & 1) + (pp >> 1)) << 4) + 8 * (pp & 1);
    const unsigned a1 = ad + L_B1 + wj * 8192, a2 = ad + L_B2 + wj * 10240;
#define WDF(base, dt) cat(trd(((base) ^ ((dt) * 64))), trd(((base) ^ ((dt) * 64)) + 512))
#define UA(j) ((j) < 4 ? a1 + (j) * 2048 : (j) < 9 ? a2 + ((j) - 4) * 2048 : (j) < 14 ? a2 + SLOT + ((j) - 9) * 2048 : (j) < 18 ? a1 + ((j) - 14) * 2048 : a2 + ((j) - 18) * 2048)
    bf16x8 wpk[8][2];
    const unsigned vd = VD();
#pragma unroll
    for (int j = 0; j < 8; ++j) { wpk[j][0] = WDF(UA(j), 0); wpk[j][1] = WDF(UA(j), 1); }
#pragma unroll
    for (int j = 0; j < 8; ++j) { PIN(wpk[j][0]); PIN(wpk[j][1]); }
#pragma unroll
    for (int j = 0; j < 4; ++j) WDU(14 + j, L_B1 + wj * 8192 + j * 2048);
#pragma unroll
    for (int j = 0; j < 4; ++j) WDU(18 + j, L_B2 + wj * 10240 + j * 2048);
    xo = ld16(rX, VXO()); st2 = ld8_dev(rW, VS2());
    WAITV(18);
    if (lane == 0 && lds_inc(L0 + L_SY + 16) == 3u && cg < 15) { u32x2 e = {elo, ehi}; st8_dev(rW, WS_FH + (unsigned)((rt * 16 + cg) * 128), e); }
    STAMP(8);
    {
      const int gf = ks0 / 6, gl = (ks0 + 20 + (wj < 2)) / 6, gi = min(gf + l7, gl);
      const unsigned vfh = WS_FH + (unsigned)(((r64 >> 1) * 16 + gi) * 128);
      u32x2 fh = ld8_dev(rW, vfh);
      asm volatile("s_waitcnt vmcnt(0)" : "+v"(fh), "+v"(xo), "+v"(st2)::"memory");
      spin_flags(rW, vfh, fh, elo, ehi);
    }
    STAMP(10);
    u32x4 hf[22][2];
    const unsigned vh = WS_H + (unsigned)((2 * r64 * 86 + ks0) * 1024 + lane * 16);
#define HL(j, t) hf[j][t] = ld16_dev(rW, vh, (min(PH(j), klast) + 86 * (t)) * 1024)
#pragma unroll
    for (int j = 0; j < HWIN; ++j) { HL(j, 0); HL(j, 1); }
    STAMP(28);
    lds_wait_ge(L0 + L_SY + wj * 4, 1u);
    f32x16 fo[2][2];
#pragma unroll
    for (int r = 0; r < 16; ++r) { fo[0][0][r] = 0.f; fo[0][1][r] = 0.f; fo[1][0][r] = 0.f; fo[1][1][r] = 0.f; }
#define G2(j) do { if ((j) + 2 >= 8 && (j) + 2 < 22) { wpk[((j) + 2) & 7][0] = WDF(UA((j) + 2), 0); wpk[((j) + 2) & 7][1] = WDF(UA((j) + 2), 1); } \
      asm volatile("s_waitcnt vmcnt(%2)" : "+v"(hf[j][0]), "+v"(hf[j][1]) : "i"(2 * ((j) + HWIN - 1 < 21 ? HWIN - 1 : 21 - (j))) : "memory");                              \
      fo[0][0] = MFMA32(wpk[(j) & 7][0], __builtin_bit_cast(bf16x8, hf[j][0]), fo[0][0]);                             \
      fo[0][1] = MFMA32(wpk[(j) & 7][1], __builtin_bit_cast(bf16x8, hf[j][0]), fo[0][1]); PIN(fo[0][0]); PIN(fo[0][1]); SB(); \
      if ((j) + HWIN < 22) HL(((j) + HWIN) % 22, 0);                                                                         \
      fo[1][0] = MFMA32(wpk[(j) & 7][0], __builtin_bit_cast(bf16x8, hf[j][1]), fo[1][0]);                             \
      fo[1][1] = MFMA32(wpk[(j) & 7][1], __builtin_bit_cast(bf16x8, hf[j][1]), fo[1][1]); PIN(fo[1][0]); PIN(fo[1][1]); SB(); \
      if ((j) + HWIN < 22) HL(((j) + HWIN) % 22, 1); } while (0)
    G2(0); STAMP(29); G2(1); G2(2); G2(3); G2(4); G2(5); G2(6); G2(7); G2(8); G2(9); G2(10);
    G2(11); G2(12); G2(13); G2(14); G2(15); G2(16); G2(17); G2(18); G2(19); G2(20); G2(21);
    STAMP(11);
#pragma unroll
    for (int t = 0; t < 2; ++t)
#pragma unroll
      for (int dt = 0; dt < 2; ++dt)
#pragma unroll
        for (int i = 0; i < 4; ++i)
          lds_st<f32x4>(L0 + L_B2 + t * SLOT + wj * 10240 + l31 * 256 + (((8 * dt + 2 * i + hh) ^ l7) << 4),
                        f32x4{fo[t][dt][4 * i], fo[t][dt][4 * i + 1], fo[t][dt][4 * i + 2], fo[t][dt][4 * i + 3]});
  }
  st2 = spin_tag(rW, VS2(), st2, elo);
  LBAR();
  STAMP(12);
  {
    u32x2 zt[2];
    const int o = 4 * (G >> 1) + pp;
#pragma unroll
    for (int j = 0; j < 2; ++j) { const int r = 8 * o + 4 * j + qq;
      zt[j] = __builtin_bit_cast(u32x2, trd(L0 + L_ZT + r * 128 + ((w ^ fsw(r)) << 4) + 8 * (G & 1))); }
    const unsigned pb = L0 + L_B2 + (w >> 2) * SLOT + (8 * wj + l3) * 256;
    f32x4 p0 = {0.f, 0.f, 0.f, 0.f}, p1 = p0;
#pragma unroll
    for (int k = 0; k < 4; ++k) {
      p0 += lds_ld<f32x4>(pb + k * 10240 + (((2 * l7) ^ l3) << 4));
      p1 += lds_ld<f32x4>(pb + k * 10240 + (((2 * l7 + 1) ^ l3) << 4));
    }
    const int src = 16 * (((l7 >> 2) << 1) | (l3 >> 2)) + 4 * (l7 & 3) + (l3 & 3);
    u32x4 zq;
    zq[0] = (unsigned)__builtin_amdgcn_ds_bpermute(src * 4, (int)zt[0].x); zq[1] = (unsigned)__builtin_amdgcn_ds_bpermute(src * 4, (int)zt[0].y);
    zq[2] = (unsigned)__builtin_amdgcn_ds_bpermute(src * 4, (int)zt[1].x); zq[3] = (unsigned)__builtin_amdgcn_ds_bpermute(src * 4, (int)zt[1].y);
    const float mean = blo(st2.x), rs = __builtin_amdgcn_rcpf(bhi(st2.x));
    float o8[8];
#pragma unroll
    for (int i = 0; i < 8; ++i) {
      const float xv = (i & 1) ? bhi(xo[i >> 1]) : blo(xo[i >> 1]), zv = (i & 1) ? bhi(zq[i >> 1]) : blo(zq[i >> 1]);
      const float F = i < 4 ? p0[i & 3] : p1[i & 3];
      const float gs = F * __builtin_amdgcn_rcpf(1.0f + __builtin_amdgcn_exp2f(F * -1.4426950408889634f));
      o8[i] = __builtin_fmaf((xv - mean) * rs * zv, gs, xv);
    }
    const u32x4 ov = {cvtpk(o8[0], o8[1]), cvtpk(o8[2], o8[3]), cvtpk(o8[4], o8[5]), cvtpk(o8[6], o8[7])};
    *(GAS u32x4*)((GAS char*)out + VXO()) = ov;
  }
  WAITV(0); STAMP(13);
  { const u64 tgt = prb0 + 4000 + 4 * (prb1 - prb0); while (__builtin_amdgcn_s_memrealtime() < tgt) __builtin_amdgcn_s_sleep(1); }
}

extern "C" void kernel_launch(void* const* d_in, const int* in_sizes, int n_in, void* d_out, int out_size, void* d_ws, size_t ws_size,
                              hipStream_t stream) {
  (void)in_sizes; (void)n_in; (void)out_size; (void)ws_size;
  (void)hipFuncSetAttribute((const void*)h3_geglu_fused, hipFuncAttributeMaxDynamicSharedMemorySize, LDS_BYTES);
  h3_geglu_fused<<<256, 512, LDS_BYTES, stream>>>((const u16*)d_in[0], (const u16*)d_in[1], (const u16*)d_in[2], (const u16*)d_in[3],
                                                  (const u16*)d_in[4], (const u16*)d_in[5], (const u16*)d_in[7], (const u16*)d_in[8],
                                                  (const u16*)d_in[9], (u16*)d_out, (char*)d_ws);
}
```

```cpp
#include <hip/hip_runtime.h>
#include <hip/hip_bf16.h>

typedef unsigned short u16;
typedef unsigned long long u64;
typedef __attribute__((ext_vector_type(8))) short bf16x8;
typedef __attribute__((ext_vector_type(4))) short s16x4;
typedef __attribute__((ext_vector_type(2))) __bf16 bf16x2_t;
typedef __attribute__((ext_vector_type(2))) float f32x2;
typedef __attribute__((ext_vector_type(4))) float f32x4;
typedef __attribute__((ext_vector_type(16))) float f32x16;
typedef __attribute__((ext_vector_type(4))) unsigned u32x4;
typedef __attribute__((ext_vector_type(2))) unsigned u32x2;
#define LAS __attribute__((address_space(3)))
#define GAS __attribute__((address_space(1)))
typedef __amdgpu_buffer_rsrc_t rsrc_t;

constexpr int S_ = 2048, D_ = 512, DF = 1376;
constexpr unsigned WS_GEN = 0, WS_FA = 4096, WS_FH = WS_FA + 256 * 128, WS_FZ = WS_FH + 256 * 128;
constexpr unsigned WS_ST = WS_FZ + 256 * 128;
constexpr unsigned WS_KVT = 1u << 20;
constexpr unsigned WS_ZT = WS_KVT + (2u << 20);
constexpr unsigned WS_H = WS_ZT + (2u << 20);
constexpr unsigned WS_BYTES = WS_H + 64 * 86 * 1024;
constexpr int SLOT = 40960;
constexpr int L_CV = 3 * SLOT;
constexpr int L_KA = L_CV, L_VA = L_KA + 9216;
constexpr int L_B1 = 0, L_B2 = SLOT, L_B3 = 2 * SLOT;
constexpr int L_CS = 32768;
constexpr int L_ST = L_CS + 3072;
constexpr int L_SY = L_ST + 512;
constexpr int L_A = L_VA + 8192;
constexpr int L_U0 = 36864, L_U1 = L_CV + 33280, L_ZT = L_CV;
constexpr int LDS_BYTES = 163840;
constexpr unsigned OOB = 0x80000000u;
constexpr unsigned SPIN_MAX = 1u << 18;

#define LPRIO 2
#define XSLEEP 4
#define ROT1 ((rt + cg) & 7)
#define ROT2 ((ct * 11) >> 2)
#define HWIN 12
#define STEPMODE 1
#define NOSTATS 0
#define WS_BASE(sz) 0
#define MFMA32(a, b, c) __builtin_amdgcn_mfma_f32_32x32x16_bf16(a, b, c, 0, 0, 0)
#define MFMA16(a, b, c) __builtin_amdgcn_mfma_f32_16x16x32_bf16(a, b, c, 0, 0, 0)
#define WAIT_BAR(N) asm volatile("s_waitcnt vmcnt(" #N ") lgkmcnt(0)\n\ts_barrier" ::: "memory")
#define LBAR() asm volatile("s_waitcnt lgkmcnt(0)\n\ts_barrier" ::: "memory")
#define WAITV(N) asm volatile("s_waitcnt vmcnt(" #N ")" ::: "memory")
#define SB() __builtin_amdgcn_sched_barrier(0)

__device__ __forceinline__ rsrc_t mk_rsrc(const void* p, unsigned bytes) {
  return __builtin_amdgcn_make_buffer_rsrc((void*)p, (short)0, (int)bytes, 0x00020000);
}
__device__ __forceinline__ void dma16(rsrc_t rs, unsigned voff, unsigned soff, unsigned lds) {
  asm volatile("s_mov_b32 m0, %0\n\ts_nop 0\n\tbuffer_load_dwordx4 %1, %2, %3 offen lds" ::"s"(lds), "v"(voff), "s"(rs), "s"(soff) : "memory");
}
__device__ __forceinline__ u32x4 ld16(rsrc_t rs, unsigned voff) {
  u32x4 r; asm volatile("buffer_load_dwordx4 %0, %1, %2, 0 offen" : "=v"(r) : "v"(voff), "s"(rs) : "memory"); return r;
}
__device__ __forceinline__ u32x4 ld16_dev(rsrc_t rs, unsigned voff, unsigned soff) {
  u32x4 r; asm volatile("buffer_load_dwordx4 %0, %1, %2, %3 offen sc1" : "=v"(r) : "v"(voff), "s"(rs), "s"(soff) : "memory"); return r;
}
__device__ __forceinline__ u32x2 ld8(rsrc_t rs, unsigned voff) {
  u32x2 r; asm volatile("buffer_load_dwordx2 %0, %1, %2, 0 offen" : "=v"(r) : "v"(voff), "s"(rs) : "memory"); return r;
}
__device__ __forceinline__ u32x2 ld8_dev(rsrc_t rs, unsigned voff) {
  u32x2 r; asm volatile("buffer_load_dwordx2 %0, %1, %2, 0 offen sc1" : "=v"(r) : "v"(voff), "s"(rs) : "memory"); return r;
}
#define PIN(x) asm volatile("" : "+v"(x))
__device__ __forceinline__ void st16_dev(rsrc_t rs, unsigned voff, u32x4 v) { __builtin_amdgcn_raw_buffer_store_b128(v, rs, voff, 0, 16); }
__device__ __forceinline__ void st8_dev(rsrc_t rs, unsigned voff, u32x2 v) { __builtin_amdgcn_raw_buffer_store_b64(v, rs, voff, 0, 16); }

__device__ __forceinline__ unsigned cvtpk(float lo, float hi) {
  bf16x2_t r = {(__bf16)lo, (__bf16)hi};
  return __builtin_bit_cast(unsigned, r);
}
__device__ __forceinline__ float blo(unsigned w) { return __uint_as_float(w << 16); }
__device__ __forceinline__ float bhi(unsigned w) { return __uint_as_float(w & 0xffff0000u); }
__device__ __forceinline__ float rbf(float f) { return blo(cvtpk(f, f)); }
__device__ __forceinline__ s16x4 trd(unsigned a) { return __builtin_amdgcn_ds_read_tr16_b64_v4i16((LAS s16x4*)(size_t)a); }
__device__ __forceinline__ bf16x8 cat(s16x4 a, s16x4 b) { return __builtin_shufflevector(a, b, 0, 1, 2, 3, 4, 5, 6, 7); }
template <class T> __device__ __forceinline__ T lds_ld(unsigned a) { return *(const LAS T*)(size_t)a; }
template <class T> __device__ __forceinline__ void lds_st(unsigned a, T v) { *(LAS T*)(size_t)a = v; }
template <int CTRL> __device__ __forceinline__ float dpp_add(float x) {
  return x + __uint_as_float(__builtin_amdgcn_update_dpp(0u, __float_as_uint(x), CTRL, 0xf, 0xf, true));
}
__device__ __forceinline__ float swap_add(float x, bool half) {
  if (half) { auto r = __builtin_amdgcn_permlane32_swap(__float_as_uint(x), __float_as_uint(x), false, false); return __uint_as_float(r[0]) + __uint_as_float(r[1]); }
  auto r = __builtin_amdgcn_permlane16_swap(__float_as_uint(x), __float_as_uint(x), false, false); return __uint_as_float(r[0]) + __uint_as_float(r[1]);
}
__device__ __forceinline__ void wave_sum4(float& a, float& b, float& c, float& d) {
  a = dpp_add<0x128>(a); b = dpp_add<0x128>(b); c = dpp_add<0x128>(c); d = dpp_add<0x128>(d);
  a = dpp_add<0x124>(a); b = dpp_add<0x124>(b); c = dpp_add<0x124>(c); d = dpp_add<0x124>(d);
  a = dpp_add<0x122>(a); b = dpp_add<0x122>(b); c = dpp_add<0x122>(c); d = dpp_add<0x122>(d);
  a = dpp_add<0x121>(a); b = dpp_add<0x121>(b); c = dpp_add<0x121>(c); d = dpp_add<0x121>(d);
  a = swap_add(a, false); b = swap_add(b, false); c = swap_add(c, false); d = swap_add(d, false);
  a = swap_add(a, true); b = swap_add(b, true); c = swap_add(c, true); d = swap_add(d, true);
}
__device__ __forceinline__ void lds_wait_ge(unsigned a, unsigned v) {
  for (unsigned n = 0; *(volatile LAS unsigned*)(size_t)a < v && n < SPIN_MAX * 16; ++n) __builtin_amdgcn_s_sleep(1);
  asm volatile("" ::: "memory");
}
__device__ __forceinline__ void lds_spin_ge(unsigned a, unsigned v) {
  for (unsigned n = 0; *(volatile LAS unsigned*)(size_t)a < v && n < SPIN_MAX * 64; ++n) {}
  asm volatile("" ::: "memory");
}
__device__ __forceinline__ unsigned lds_inc(unsigned a) {
  return __hip_atomic_fetch_add((LAS unsigned*)(size_t)a, 1u, __ATOMIC_RELAXED, __HIP_MEMORY_SCOPE_WORKGROUP);
}
__device__ __forceinline__ int fsw(int r) { return ((r >> 1) & 1) | (((r >> 3) & 3) << 1); }
__device__ __forceinline__ void spin_flags(rsrc_t rs, unsigned voff, u32x2 f, unsigned lo, unsigned hi) {
  for (unsigned n = 0; !__all(f.x == lo && f.y == hi) && n < SPIN_MAX; ++n) {
    __builtin_amdgcn_s_sleep(1);
    f = ld8_dev(rs, voff); WAITV(0); PIN(f);
  }
}
__device__ __forceinline__ u32x2 spin_tag(rsrc_t rs, unsigned voff, u32x2 f, unsigned tag) {
  for (unsigned n = 0; !__all(f.y == tag) && n < SPIN_MAX; ++n) {
    __builtin_amdgcn_s_sleep(1);
    f = ld8_dev(rs, voff); WAITV(0); PIN(f);
  }
  return f;
}
#define STAMP(i)

__global__ __launch_bounds__(512, 2) void h3_geglu_fused(const u16* __restrict__ x, const u16* __restrict__ kin, const u16* __restrict__ vin,
                                                         const u16* __restrict__ shk, const u16* __restrict__ ssk, const u16* __restrict__ ssd,
                                                         const u16* __restrict__ wg, const u16* __restrict__ wu, const u16* __restrict__ wd,
                                                         u16* __restrict__ out, char* __restrict__ ws) {
  extern __shared__ __attribute__((aligned(16))) char smem[];
  const unsigned L0 = (unsigned)(size_t)(LAS char*)smem;
  const int tid = threadIdx.x, lane = tid & 63;
  const int w = __builtin_amdgcn_readfirstlane(tid >> 6), wj = w & 3;
  STAMP(0);
  const int b = blockIdx.x, xcd = b & 7, idx = b >> 3, rh = xcd & 1, q4 = xcd >> 1;
  const int pan = 4 * rh + q4, aa = 32 * pan + idx;
  const int l3 = lane >> 3, l7 = lane & 7, G = lane >> 4, qq = (lane >> 2) & 3, pp = lane & 3, hh = lane >> 5, l31 = lane & 31;
  const rsrc_t rX = mk_rsrc(x, S_ * D_ * 2), rW = mk_rsrc(ws, WS_BYTES), rD = mk_rsrc(wd, DF * D_ * 2);
  const int cg = idx < 24 ? ((q4 * 7 + 1) >> 1) + (idx >> 3) : idx < 28 ? (q4 < 2 ? 3 : 10) : 14 + ((idx >> 1) & 1);
  const int rt = 8 * rh + (idx < 24 ? (idx & 7) : idx < 28 ? (q4 & 1) * 4 + (idx & 3) : 2 * q4 + (idx & 1));
  const int r64 = 4 * pan + (idx >> 3), ct = idx & 7;
  const int ks0 = 22 * wj - (wj == 3 ? 1 : 0);
  u32x4 xo; u32x2 st2;
  unsigned elo, ehi;
#define VD() (unsigned)((16 * ks0 + l3) * 1024 + ct * 128 + ((l7 ^ (((l3 >> 1) & 1) << 2)) << 4))
  const int rot2 = ROT2;
#define PH(j) ((j) + rot2 >= 22 ? (j) + rot2 - 22 : (j) + rot2)
  const int klast = wj < 2 ? 21 : 20;
#define WDU(j, lds) do { const unsigned v_ = vd | (PH(j) > klast ? OOB : 0u);                                       \
    dma16(rD, v_, PH(j) * 16384, L0 + (lds)); dma16(rD, v_, PH(j) * 16384 + 8192, L0 + (lds) + 1024); } while (0)
#define VXO() (unsigned)((64 * r64 + 8 * w + l3) * 1024 + ct * 128 + l7 * 16)
#define VS2() (WS_ST + (unsigned)((64 * r64 + 8 * w + l3) * 8))

  if (w >= 4) {
    __builtin_amdgcn_s_setprio(2);
    u32x2 gen = ld8_dev(rW, WS_GEN);
    const unsigned vx = (cg < 15) ? (unsigned)((128 * rt + 8 * wj + l3) * 1024 + ((l7 ^ ((4 * wj + (l3 >> 1)) & 7)) << 4)) : OOB;
    unsigned vg[3], vu[3];
#pragma unroll
    for (int r = 0; r < 3; ++r) {
      const unsigned P = 64 * r + lane, k = (P * 43691u) >> 19, col = 96 * cg + 8 * (P - 12 * k);
      vg[r] = (col < DF) ? (16 * wj + k) * (DF * 2) + col * 2 : OOB; vu[r] = vg[r] ^ 32u;
    }
    const rsrc_t rG = mk_rsrc(wg, D_ * DF * 2), rU = mk_rsrc(wu, D_ * DF * 2);
    const int rot1 = ROT1;
    unsigned dpv;
    asm volatile("buffer_load_dword %0, %1, %2, 0 offen" : "=v"(dpv) : "v"(aa * 4), "s"(mk_rsrc(ssd, D_ * 2)) : "memory");
    if (tid == 256) lds_st<unsigned>(L0 + L_A, 0u);
    LBAR();
#pragma nounroll
    for (int c = 0; c < 8; ++c) {
      const int cc = (c + rot1) & 7;
      const unsigned sx = cc * 128, sw = cc * (64 * DF * 2), lx = L0 + (c & 3) * SLOT + wj * 1024, lw = L0 + (c & 3) * SLOT + 16384 + wj * 3072;
      dma16(rX, vx, sx, lx);                 dma16(rG, vg[0], sw, lw);        dma16(rU, vu[0], sw, lw + 12288);
      dma16(rX, vx, sx + 32768, lx + 4096);  dma16(rG, vg[1], sw, lw + 1024); dma16(rU, vu[1], sw, lw + 13312);
      if (c >= 2) asm volatile("s_waitcnt vmcnt(16)\n\ts_barrier" ::: "memory");
      if (c == 2) STAMP(16);
      if (c == 3) STAMP(18);
      if (c == 7) STAMP(23);
      if (c == 5 && tid == 256) {
        PIN(gen); const unsigned lo = gen.x + 1u;
        st8_dev(rW, WS_FA + (unsigned)(aa * 128), u32x2{lo, gen.y + (lo == 0u ? 1u : 0u)});
      }
      dma16(rX, vx, sx + 65536, lx + 8192);  dma16(rG, vg[2], sw, lw + 2048); dma16(rU, vu[2], sw, lw + 14336);
      dma16(rX, vx, sx + 98304, lx + 12288);
    }
    PIN(gen); PIN(dpv);
    elo = __builtin_amdgcn_readfirstlane(gen.x) + 1u;
    ehi = __builtin_amdgcn_readfirstlane(gen.y) + (elo == 0u ? 1u : 0u);
    const unsigned dpair = __builtin_amdgcn_readfirstlane(dpv);
    const int ce = tid & 255;
    const unsigned vd = VD();
    const unsigned vfa = WS_FA + (unsigned)((32 * pan + l31) * 128), vst = WS_ST + (unsigned)((128 * rt + 64 * (wj & 1) + lane) * 8);
    const u32x4 zero4 = {0u, 0u, 0u, 0u};
    u32x2 stg = ld8_dev(rW, vst), fa = ld8_dev(rW, vfa);
#pragma unroll
    for (int j = 0; j < 4; ++j) WDU(j, L_B1 + wj * 8192 + j * 2048);
    if (ce < 4) lds_st<u32x4>(L0 + L_SY + ce * 16, zero4);
    WAIT_BAR(20);
#pragma unroll
    for (int j = 0; j < 5; ++j) WDU(4 + j, L_B2 + wj * 10240 + j * 2048);
    asm volatile("s_waitcnt vmcnt(18)\n\ts_barrier" : "+v"(stg), "+v"(fa)::"memory");
    STAMP(4);
    stg = spin_tag(rW, vst, stg, elo);
    if (wj < 2) lds_st<unsigned>(L0 + L_ST + (64 * wj + lane) * 4, stg.x);
    spin_flags(rW, vfa, fa, elo, ehi);
    u32x4 hc[2], hp[2], ur[2];
    {
      const rsrc_t rHk = mk_rsrc(ssk, D_ * S_ * 2);
#pragma unroll
      for (int c = 0; c < 2; ++c) {
        hc[c] = ld16(rHk, (unsigned)((2 * aa + c) * 4096 + ce * 16));
        hp[c] = ld16(rHk, ce ? (unsigned)((2 * aa + c) * 4096 + ce * 16 - 16) : OOB);
        ur[c] = ld16_dev(rW, WS_KVT + (unsigned)(2 * aa * 4096 + ce * 16), c * 4096);
      }
    }
#pragma unroll
    for (int j = 0; j < 2; ++j) WDU(9 + j, L_B3 + wj * 10240 + j * 2048);
    WAIT_BAR(10);
    STAMP(5);
#pragma unroll
    for (int j = 2; j < 5; ++j) WDU(9 + j, L_B3 + wj * 10240 + j * 2048);
    {
      asm volatile("s_waitcnt vmcnt(10)" : "+v"(hc[0]), "+v"(hc[1]), "+v"(hp[0]), "+v"(hp[1]), "+v"(ur[0]), "+v"(ur[1])::"memory");
      auto put = [&](unsigned hb, u32x4 pv, u32x4 cu, int blk) {
        const unsigned a0 = __builtin_amdgcn_alignbit(pv[1], pv[0], 16), a1 = __builtin_amdgcn_alignbit(pv[2], pv[1], 16),
                       a2 = __builtin_amdgcn_alignbit(pv[3], pv[2], 16), a3 = __builtin_amdgcn_alignbit(cu[0], pv[3], 16),
                       a4 = __builtin_amdgcn_alignbit(cu[1], cu[0], 16);
        lds_st<u32x4>(hb + blk * 16, u32x4{a0, a1, a2, a3});
        lds_st<u32x4>(hb + 4160 + blk * 16, u32x4{pv[1], pv[2], pv[3], cu[0]});
        lds_st<u32x4>(hb + 8320 + blk * 16, u32x4{a1, a2, a3, a4});
        lds_st<u32x4>(hb + 12480 + blk * 16, u32x4{pv[2], pv[3], cu[0], cu[1]});
      };
#pragma unroll
      for (int c = 0; c < 2; ++c) {
        put(L0 + L_CV + c * 16640, hp[c], hc[c], ce + 1);
        if (ce == 0 || ce == 255) put(L0 + L_CV + c * 16640, ce ? hc[c] : zero4, zero4, ce ? 257 : 0);
        lds_st<u32x4>(L0 + (c ? L_U1 : L_U0) + ce * 16, ur[c]);
      }
      asm volatile("s_waitcnt lgkmcnt(0)" ::: "memory");
      if (lane == 0) lds_inc(L0 + L_SY + 24);
    }
    STAMP(7);
    const int vch = wj >> 1, vrh = wj & 1, c15 = lane & 15;
    f32x4 ya[8];
#pragma unroll
    for (int c = 0; c < 8; ++c) ya[c] = f32x4{0.f, 0.f, 0.f, 0.f};
    {
      unsigned cA = L0 + L_CV + vch * 16640 + (c15 & 3) * 4160 + (4 * (c15 >> 2) + 8 * G + 128 * vrh) * 2;
      const unsigned ub = L0 + (vch ? L_U1 : L_U0);
      unsigned cB = ub + (240 - 16 * c15 + 8 * G + 128 * vrh) * 2;
      int lim = ((16 * c15 - 8 * G + 47) >> 5) - 4 * vrh;
      PIN(cA); PIN(cB);
      bf16x8 bfr[2][8]; s16x4 ah[2][4];
#define VLD8(a) (*(const volatile LAS s16x4*)(size_t)(a))
#define CA(set, r, p, i) ah[set][i] = VLD8(cA + (r) * 64 + (((i) & 2) ? 7 - (p) : (p)) * 512 + ((i) & 1) * 8)
#define CB(r, mu) bfr[(r) & 1][mu] = lds_ld<bf16x8>((mu) == 0 ? ((r) >= lim ? L0 + L_SY + 32 : cB + (r) * 64 + 3584) : cB + (r) * 64 + (7 - (mu)) * 512)
#define CM(r, p, m) do { if ((m) + (p) < 8) { ya[(p) + (m)] = MFMA16(cat(ah[(p) & 1][0], ah[(p) & 1][1]), bfr[(r) & 1][(m) & 7], ya[((p) + (m)) & 7]); PIN(ya[((p) + (m)) & 7]); } \
        else { ya[(m) - 1] = MFMA16(cat(ah[(p) & 1][2], ah[(p) & 1][3]), bfr[(r) & 1][((m) + (p)) & 7], ya[(m) - 1]); PIN(ya[(m) - 1]); } } while (0)
#define CPAIR(r, p) do { _Pragma("unroll") for (int m = 0; m < 9; ++m) { CM(r, p, m);                                               \
          if (m < 4) CA(((p) + 1) & 1, (p) < 3 ? (r) : (r) + 1, ((p) + 1) & 3, m); else if (m < 6) CB((r) + 1, 2 * (p) + m - 4); SB(); } } while (0)
#define CONV(r) do { CPAIR(r, 0); CPAIR(r, 1); CPAIR(r, 2); CPAIR(r, 3); } while (0)
      lds_wait_ge(L0 + L_SY + 24, 8u); __builtin_amdgcn_s_setprio(2);
      STAMP(24);
#pragma unroll
      for (int i = 0; i < 4; ++i) CA(0, 0, 0, i);
#pragma unroll
      for (int mu = 0; mu < 8; ++mu) CB(0, mu);
#pragma nounroll
      for (int it = 0; it < 2; ++it) {
        CONV(0); CPAIR(1, 0); CPAIR(1, 1); CPAIR(1, 2);
        if (it) { asm volatile("s_waitcnt lgkmcnt(0)" ::: "memory"); *(volatile LAS unsigned*)(size_t)(L0 + L_SY + 48 + wj * 4) = 1u; }
        CPAIR(1, 3);
        cA += 128; cB += 128; lim -= 2; PIN(cA); PIN(cB);
      }
    }
    STAMP(27);
    WAITV(0);
    *(volatile LAS unsigned*)(size_t)(L0 + L_SY + wj * 4) = 1u;
    {
      const unsigned mine = L0 + L_SY + 48 + wj * 4, other = L0 + L_SY + 48 + (wj ^ 1) * 4;
      const unsigned xo_ = L0 + L_CV + vch * 16640 + (vrh ^ 1) * 4096 + lane * 16, xb = L0 + L_CV + vch * 16640 + vrh * 4096;
      const float dsc = vch ? bhi(dpair) : blo(dpair);
      u32x2 uq[4];
#pragma unroll
      for (int c = 0; c < 4; ++c) uq[c] = lds_ld<u32x2>(L0 + (vch ? L_U1 : L_U0) + (2044 - 256 * (4 * vrh + c) - 16 * c15 - 4 * G) * 2);
      lds_spin_ge(other, 1u);
      f32x4 y[4];
      if (vrh) {
        asm volatile("" ::: "memory");
#pragma unroll
        for (int c = 0; c < 4; ++c) { lds_st<f32x4>(xo_ + c * 1024, ya[c]); y[c] = ya[4 + c]; PIN(y[c]); }
      } else {
#pragma unroll
        for (int c = 0; c < 4; ++c) { lds_st<f32x4>(xo_ + c * 1024, ya[4 + c]); y[c] = ya[c]; PIN(y[c]); }
        asm volatile("" ::: "memory");
      }
      asm volatile("s_waitcnt lgkmcnt(0)" ::: "memory");
      *(volatile LAS unsigned*)(size_t)mine = 2u;
#pragma unroll
      for (int c = 0; c < 4; ++c)
#pragma unroll
        for (int e = 0; e < 4; ++e) {
          const unsigned uu = (e & 2) ? uq[c].x : uq[c].y;
          float s = __builtin_fmaf((e & 1) ? blo(uu) : bhi(uu), dsc, y[c][e]); PIN(s); y[c][e] = s;
        }
      lds_spin_ge(other, 2u);
      f32x4 o[4];
#pragma unroll
      for (int c = 0; c < 4; ++c) o[c] = lds_ld<f32x4>(xb + c * 1024 + lane * 16);
      SB();
      u32x2 zp[4];
#pragma unroll
      for (int c = 0; c < 4; ++c) {
        float z[4];
#pragma unroll
        for (int e = 0; e < 4; ++e) { z[e] = y[c][e] + o[c][e]; asm("" : "+v"(z[e])); }
        zp[c] = u32x2{cvtpk(z[0], z[1]), cvtpk(z[2], z[3])};
      }
#pragma unroll
      for (int c = 0; c < 4; ++c) lds_st<u32x2>(xb + c * 512 + c15 * 32 + G * 8, zp[c]);
#pragma unroll
      for (int i = 0; i < 2; ++i)
        st16_dev(rW, WS_ZT + (unsigned)((2 * aa + vch) * 4096 + vrh * 2048 + i * 1024 + lane * 16), lds_ld<u32x4>(xb + i * 1024 + lane * 16));
    }
    STAMP(26);
    xo = ld16(rX, VXO()); st2 = ld8_dev(rW, VS2());
    asm volatile("s_waitcnt vmcnt(2)" ::: "memory");
    if (lane == 0 && lds_inc(L0 + L_SY + 20) == 3u) { u32x2 e = {elo, ehi}; st8_dev(rW, WS_FZ + (unsigned)(aa * 128), e); }
    STAMP(9);
    if (b == 0 && wj == 0) {
#pragma nounroll
      for (int i = 0; i < 4; ++i) {
        const unsigned v = WS_FA + (unsigned)((64 * i + lane) * 128);
        spin_flags(rW, v, u32x2{~elo, 0u}, elo, ehi);
      }
      if (lane == 0) { u32x2 e = {elo, ehi}; st8_dev(rW, WS_GEN, e); }
    }
    {
      const unsigned vfz = WS_FZ + (unsigned)((32 * ct + l31) * 128);
      u32x2 fz = ld8_dev(rW, vfz);
      asm volatile("s_waitcnt vmcnt(0)" : "+v"(fz), "+v"(xo), "+v"(st2)::"memory");
      spin_flags(rW, vfz, fz, elo, ehi);
      STAMP(30);
      const unsigned vz = WS_ZT + (unsigned)((64 * ct + 16 * wj + l3) * 4096 + r64 * 128 + l7 * 16);
      u32x4 z0 = ld16_dev(rW, vz, 0), z1 = ld16_dev(rW, vz, 32768);
      lds_wait_ge(L0 + L_SY + 20, 4u);
      asm volatile("s_waitcnt vmcnt(0)" : "+v"(z0), "+v"(z1)::"memory");
      const int r = 16 * wj + l3;
      lds_st<u32x4>(L0 + L_ZT + r * 128 + ((l7 ^ fsw(r)) << 4), z0);
      lds_st<u32x4>(L0 + L_ZT + (r + 8) * 128 + ((l7 ^ fsw(r + 8)) << 4), z1);
    }
    STAMP(31);
  } else {
    __builtin_amdgcn_s_setprio(3);
    u32x2 sh[2]; u32x4 xr[2];
#pragma unroll
    for (int i = 0; i < 2; ++i) xr[i] = ld16(rX, (unsigned)((8 * aa + 2 * wj + i) * 1024 + lane * 16));
    {
      const rsrc_t rK = mk_rsrc(kin, S_ * D_ * 2), rV = mk_rsrc(vin, S_ * D_ * 2);
      const unsigned kb = (unsigned)((64 * idx + l3) * 1024 + pan * 128), fl = (unsigned)(l7 ^ ((l3 >> 1) & 1));
#pragma unroll
      for (int i = 0; i < 2; ++i) {
        const int g = 2 * wj + i;
        dma16(rK, kb + ((fl ^ (((g + 1) & 3) << 1)) << 4), g * 8192, L0 + L_KA + (g + 1) * 1024);
        dma16(rV, kb + ((fl ^ ((g & 3) << 1)) << 4), g * 8192, L0 + L_VA + g * 1024);
      }
      if (wj == 0) dma16(rK, idx == 0 ? OOB : kb - 8192 + (fl << 4), 0, L0 + L_KA);
    }
    u32x2 gen = ld8_dev(rW, WS_GEN);
    {
      const rsrc_t rSh = mk_rsrc(shk, D_ * 4 * 2);
#pragma unroll
      for (int i = 0; i < 2; ++i) sh[i] = ld8(rSh, (unsigned)((64 * pan + 16 * wj + 8 * i + 4 * (G & 1) + pp) * 8));
    }
    asm volatile("s_barrier" ::: "memory");
    STAMP(3);
    const int tp = wj & 1, ch = wj >> 1;
    f32x16 acc[2][3];
    f32x4 cs[3];
    unsigned ax[4], aw[2][3], ak[5];
    unsigned vkv = WS_KVT + (unsigned)((64 * pan + 16 * wj + l3) * 4096 + (1984 - 64 * idx + 8 * l7) * 2);
    unsigned vst0 = lane < 2 ? WS_ST + (unsigned)((8 * aa + 2 * wj + lane) * 8) : OOB;
    int src4 = 4 * (16 * ((((7 - l7) >> 2) << 1) | (l3 >> 2)) + 4 * ((7 - l7) & 3) + (l3 & 3));
    const unsigned so_ = ((lane & 15) == 4 * (G & 1)) ? 0x3f803f80u : 0u;
    bf16x8 sel = __builtin_bit_cast(bf16x8, u32x4{so_, so_, so_, so_});
    {
      const unsigned ax0 = L0 + (64 * tp + l31) * 128 + (((4 * tp + hh) ^ ((l31 >> 1) & 7)) << 4);
      const unsigned aw0 = L0 + 16384 + (G & 1) * 12288 + (8 * hh + qq) * 192 + ((pp & 1) << 4) + 8 * (pp >> 1);
#pragma unroll
      for (int s = 0; s < 4; ++s) { ax[s] = ax0 ^ (s << 5); PIN(ax[s]); }
#pragma unroll
      for (int i = 0; i < 3; ++i) {
        const unsigned a_ = aw0 + (((3 * ch + i) ^ (G & 1)) << 5);
        aw[0][i] = a_ + tp * 6144; aw[1][i] = a_ + 6144 - tp * 6144; PIN(aw[0][i]); PIN(aw[1][i]);
      }
#pragma unroll
      for (int j = 0; j < 5; ++j) { const int rr = 8 * (4 * (G >> 1) + pp) + qq + (j < 3 ? 4 + 4 * j : 4 * (j - 3));
        ak[j] = L0 + (j < 3 ? L_KA : L_VA) + rr * 128 + (((2 * wj) ^ fsw(rr)) << 4) + 8 * (G & 1); PIN(ak[j]); }
      PIN(vkv); PIN(vst0); PIN(src4); PIN(sel);
    }
    {
      asm volatile("s_waitcnt vmcnt(7)" : "+v"(xr[0]), "+v"(xr[1])::"memory");
      STAMP(14);
      const f32x2 x0 = {blo(__builtin_amdgcn_readfirstlane(xr[0][0])), blo(__builtin_amdgcn_readfirstlane(xr[1][0]))};
      f32x2 d[8];
#pragma unroll
      for (int e = 0; e < 4; ++e) { d[2 * e] = f32x2{blo(xr[0][e]), blo(xr[1][e])} - x0; d[2 * e + 1] = f32x2{bhi(xr[0][e]), bhi(xr[1][e])} - x0; }
      const f32x2 sm = ((d[0] + d[1]) + (d[2] + d[3])) + ((d[4] + d[5]) + (d[6] + d[7]));
      const f32x2 sq = ((d[0] * d[0] + d[1] * d[1]) + (d[2] * d[2] + d[3] * d[3])) + ((d[4] * d[4] + d[5] * d[5]) + (d[6] * d[6] + d[7] * d[7]));
      float s0 = sm[0], s1 = sm[1], q0 = sq[0], q1 = sq[1];
      wave_sum4(s0, s1, q0, q1);
      const float m = (lane ? s1 : s0) * (1.0f / D_), var = (lane ? q1 : q0) * (1.0f / D_) - m * m;
      unsigned ms0 = cvtpk(m + (lane ? x0[1] : x0[0]), __builtin_amdgcn_sqrtf(rbf(rbf(__builtin_fmaxf(var, 0.f)) + 1e-5f)));
      asm volatile("s_waitcnt vmcnt(0)" : "+v"(gen), "+v"(sh[0]), "+v"(sh[1]), "+v"(ms0)::"memory");
      STAMP(15);
      if (lane == 0) lds_inc(L0 + L_A);
      elo = __builtin_amdgcn_readfirstlane(gen.x) + 1u;
      ehi = __builtin_amdgcn_readfirstlane(gen.y) + (elo == 0u ? 1u : 0u);
      st8_dev(rW, vst0, u32x2{ms0, elo});
      f32x2 sk[4] = {{blo(sh[0].x), blo(sh[1].x)}, {bhi(sh[0].x), bhi(sh[1].x)}, {blo(sh[0].y), blo(sh[1].y)}, {bhi(sh[0].y), bhi(sh[1].y)}};
      PIN(sk[0]); PIN(sk[1]); PIN(sk[2]); PIN(sk[3]);
      lds_spin_ge(L0 + L_A, 4u);
      STAMP(1);
      u32x2 t[2][5];
#pragma unroll
      for (int j = 0; j < 5; ++j) { t[0][j] = __builtin_bit_cast(u32x2, trd(ak[j])); t[1][j] = __builtin_bit_cast(u32x2, trd(ak[j] ^ 16)); }
      SB();
      f32x2 kk[20], r[8];
#pragma unroll
      for (int j = 0; j < 5; ++j) {
        kk[4 * j] = f32x2{blo(t[0][j].x), blo(t[1][j].x)}; kk[4 * j + 1] = f32x2{bhi(t[0][j].x), bhi(t[1][j].x)};
        kk[4 * j + 2] = f32x2{blo(t[0][j].y), blo(t[1][j].y)}; kk[4 * j + 3] = f32x2{bhi(t[0][j].y), bhi(t[1][j].y)};
      }
#pragma unroll
      for (int e = 0; e < 8; ++e) r[e] = kk[e + 1] * sk[3];
#pragma unroll
      for (int j = 2; j >= 0; --j)
#pragma unroll
        for (int e = 0; e < 8; ++e) r[e] = __builtin_elementwise_fma(kk[e + 4 - j], sk[j], r[e]);
#pragma unroll
      for (int e = 0; e < 8; ++e) r[e] *= kk[12 + e];
      u32x4 po[2];
#pragma unroll
      for (int i = 0; i < 2; ++i)
#pragma unroll
        for (int e = 0; e < 4; ++e) po[i][e] = (unsigned)__builtin_amdgcn_ds_bpermute(src4, (int)cvtpk(r[7 - 2 * e][i], r[6 - 2 * e][i]));
      st16_dev(rW, vkv, po[0]); st16_dev(rW, vkv + 32768, po[1]);
      __builtin_amdgcn_s_setprio(1);
    }
    STAMP(2);
    {
      bf16x8 xf[2][2]; s16x4 wf[2][3][2];
#define PINA(t, i) PIN(acc[t][i])
#define M1(p, t, i) do { acc[t][i] = MFMA32(cat(wf[p][i][0], wf[p][i][1]), xf[p][t], acc[t][i]); PINA(t, i); } while (0)
#define MS(p, i, on) do { if (on) { cs[i] = MFMA16(sel, cat(wf[p][i][0], wf[p][i][1]), cs[i]); PIN(cs[i]); } } while (0)
#define RW(s, i) do { wf[(s) & 1][i][0] = trd(aw[(s) >> 1][i] + ((s) & 1) * 3072); wf[(s) & 1][i][1] = trd(aw[(s) >> 1][i] + ((s) & 1) * 3072 + 768); } while (0)
#define RX(s, t) xf[(s) & 1][t] = lds_ld<bf16x8>(ax[s] + (t) * 4096)
#define KEEP(p) do { asm volatile("" ::"v"(wf[p][0][0]), "v"(wf[p][0][1]), "v"(wf[p][1][0]), "v"(wf[p][1][1]), "v"(wf[p][2][0]), "v"(wf[p][2][1]), "v"(xf[p][0]), "v"(xf[p][1])); } while (0)
#define ADV(a) do { a += dl; PIN(a); } while (0)
#define STEP(p, s, on) do { M1(p, 0, 0); RW(s, 0); SB(); M1(p, 1, 0); RX(s, 0); RX(s, 1); SB(); M1(p, 0, 1); RW(s, 1); SB();  \
        M1(p, 1, 1); RW(s, 2); SB(); M1(p, 0, 2);                                                                             \
        if ((s) == 3) { ADV(ax[0]); ADV(ax[1]); ADV(ax[2]); ADV(ax[3]); ADV(aw[0][0]); } SB(); M1(p, 1, 2);                     \
        if ((s) == 3) { ADV(aw[0][1]); ADV(aw[0][2]); ADV(aw[1][0]); ADV(aw[1][1]); ADV(aw[1][2]); }                            \
        MS(p, 0, on); MS(p, 1, on); MS(p, 2, on); KEEP(p); SB(); } while (0)
      LBAR();
      STAMP(16);
      RW(0, 0); RX(0, 0); RX(0, 1); RW(0, 1); RW(0, 2); SB();
      unsigned dl = SLOT;
#pragma unroll
      for (int i = 0; i < 3; ++i) {
        cs[i] = f32x4{0.f, 0.f, 0.f, 0.f};
#pragma unroll
        for (int r = 0; r < 16; ++r) { acc[0][i][r] = 0.f; acc[1][i][r] = 0.f; }
      }
      STEP(0, 1, true);
#pragma nounroll
      for (int c = 0; c < 7; ++c) {
        dl = (c == 3) ? 0u - 3u * SLOT : (unsigned)SLOT;
        STEP(1, 2, true);
        if (c == 2) WAITV(0);
        STEP(0, 3, false);
        LBAR();
        STEP(1, 0, false); STEP(0, 1, true);
      }
      STAMP(4);
      STEP(1, 2, true);
#pragma unroll
      for (int i = 0; i < 3; ++i) lds_st<float>(L0 + L_CS + (wj * 3 + i) * 256 + lane * 4, cs[i][0]);
      STEP(0, 3, false);
      LBAR();
      float co[3]; unsigned ms[2];
#pragma unroll
      for (int i = 0; i < 3; ++i) co[i] = lds_ld<float>(L0 + L_CS + ((wj ^ 1) * 3 + i) * 256 + lane * 4);
#pragma unroll
      for (int t = 0; t < 2; ++t) ms[t] = lds_ld<unsigned>(L0 + L_ST + (64 * tp + 32 * t + l31) * 4);
      SB();
      M1(1, 0, 0); M1(1, 1, 0); M1(1, 0, 1); M1(1, 1, 1); M1(1, 0, 2); M1(1, 1, 2); SB();
      float k0[2], k1[2], s2[2];
      u32x4 bm[2];
#pragma unroll
      for (int t = 0; t < 2; ++t) {
        const unsigned mn = (ms[t] & 0xffffu) ^ 0x8000u;
        bm[t] = u32x4{mn | (mn << 16), mn, 0u, 0u};
        const float sd = bhi(ms[t]), rs = __builtin_amdgcn_rcpf(sd);
        k0[t] = rs * -2.3022081983f; k1[t] = k0[t] * rs * rs * 0.044715f; s2[t] = sd * sd;
      }
#pragma unroll
      for (int i = 0; i < 3; ++i) {
        const float c = cs[i][0] + co[i], c1 = c - rbf(c), c2 = c1 - rbf(c1);
        const u32x4 am = {cvtpk(c, c1), cvtpk(c2, 0.f), 0u, 0u};
#pragma unroll
        for (int t = 0; t < 2; ++t) { acc[t][i] = MFMA32(__builtin_bit_cast(bf16x8, am), __builtin_bit_cast(bf16x8, bm[t]), acc[t][i]); PINA(t, i); }
      }
      STAMP(5);
      const unsigned vhs = WS_H + (unsigned)(((4 * rt + 2 * tp) * 86 + 6 * cg + 3 * ch) * 1024 + lane * 16);
#pragma unroll
      for (int i = 0; i < 3; ++i) {
        const unsigned vo = (vhs + i * 1024) | (6 * cg + 3 * ch + i < 86 ? 0u : OOB);
#pragma unroll
        for (int t = 0; t < 2; ++t) {
          f32x2 g[4], zz[4], gu[4];
#pragma unroll
          for (int r = 0; r < 4; ++r) {
            g[r] = f32x2{acc[t][i][2 * r], acc[t][i][2 * r + 1]};
            zz[r] = g[r] * __builtin_elementwise_fma(g[r] * g[r], f32x2{k1[t], k1[t]}, f32x2{k0[t], k0[t]});
          }
#pragma unroll
          for (int r = 0; r < 4; ++r) {
            zz[r] = f32x2{__builtin_amdgcn_exp2f(zz[r][0]), __builtin_amdgcn_exp2f(zz[r][1])};
            gu[r] = g[r] * f32x2{acc[t][i][8 + 2 * r], acc[t][i][9 + 2 * r]};
          }
          u32x4 hq;
#pragma unroll
          for (int r = 0; r < 4; ++r) {
            const f32x2 dd = __builtin_elementwise_fma(zz[r], f32x2{s2[t], s2[t]}, f32x2{s2[t], s2[t]});
            const f32x2 hv = gu[r] * f32x2{__builtin_amdgcn_rcpf(dd[0]), __builtin_amdgcn_rcpf(dd[1])};
            hq[r] = cvtpk(hv[0], hv[1]);
          }
          st16_dev(rW, vo + t * (86 * 1024), hq);
        }
      }
    }
    if (lane == 0) lds_inc(L0 + L_SY + 24);
    STAMP(6);
    const unsigned ad = L0 + (8 * hh + qq) * 128 + ((4 * ((qq >> 1) & 1) + 2 * (G & 1) + (pp >> 1)) << 4) + 8 * (pp & 1);
    const unsigned a1 = ad + L_B1 + wj * 8192, a2 = ad + L_B2 + wj * 10240;
#define WDF(base, dt) cat(trd(((base) ^ ((dt) * 64))), trd(((base) ^ ((dt) * 64)) + 512))
#define UA(j) ((j) < 4 ? a1 + (j) * 2048 : (j) < 9 ? a2 + ((j) - 4) * 2048 : (j) < 14 ? a2 + SLOT + ((j) - 9) * 2048 : (j) < 18 ? a1 + ((j) - 14) * 2048 : a2 + ((j) - 18) * 2048)
    bf16x8 wpk[8][2];
    const unsigned vd = VD();
#pragma unroll
    for (int j = 0; j < 8; ++j) { wpk[j][0] = WDF(UA(j), 0); wpk[j][1] = WDF(UA(j), 1); }
#pragma unroll
    for (int j = 0; j < 8; ++j) { PIN(wpk[j][0]); PIN(wpk[j][1]); }
#pragma unroll
    for (int j = 0; j < 4; ++j) WDU(14 + j, L_B1 + wj * 8192 + j * 2048);
#pragma unroll
    for (int j = 0; j < 4; ++j) WDU(18 + j, L_B2 + wj * 10240 + j * 2048);
    xo = ld16(rX, VXO()); st2 = ld8_dev(rW, VS2());
    WAITV(18);
    if (lane == 0 && lds_inc(L0 + L_SY + 16) == 3u && cg < 15) { u32x2 e = {elo, ehi}; st8_dev(rW, WS_FH + (unsigned)((rt * 16 + cg) * 128), e); }
    STAMP(8);
    {
      const int gf = ks0 / 6, gl = (ks0 + 20 + (wj < 2)) / 6, gi = min(gf + l7, gl);
      const unsigned vfh = WS_FH + (unsigned)(((r64 >> 1) * 16 + gi) * 128);
      u32x2 fh = ld8_dev(rW, vfh);
      asm volatile("s_waitcnt vmcnt(0)" : "+v"(fh), "+v"(xo), "+v"(st2)::"memory");
      spin_flags(rW, vfh, fh, elo, ehi);
    }
    STAMP(10);
    u32x4 hf[22][2];
    const unsigned vh = WS_H + (unsigned)((2 * r64 * 86 + ks0) * 1024 + lane * 16);
#define HL(j, t) hf[j][t] = ld16_dev(rW, vh, (min(PH(j), klast) + 86 * (t)) * 1024)
#pragma unroll
    for (int j = 0; j < HWIN; ++j) { HL(j, 0); HL(j, 1); }
    STAMP(28);
    lds_wait_ge(L0 + L_SY + wj * 4, 1u);
    f32x16 fo[2][2];
#pragma unroll
    for (int r = 0; r < 16; ++r) { fo[0][0][r] = 0.f; fo[0][1][r] = 0.f; fo[1][0][r] = 0.f; fo[1][1][r] = 0.f; }
#define G2(j) do { if ((j) + 2 >= 8 && (j) + 2 < 22) { wpk[((j) + 2) & 7][0] = WDF(UA((j) + 2), 0); wpk[((j) + 2) & 7][1] = WDF(UA((j) + 2), 1); } \
      asm volatile("s_waitcnt vmcnt(%2)" : "+v"(hf[j][0]), "+v"(hf[j][1]) : "i"(2 * ((j) + HWIN - 1 < 21 ? HWIN - 1 : 21 - (j))) : "memory");                              \
      fo[0][0] = MFMA32(wpk[(j) & 7][0], __builtin_bit_cast(bf16x8, hf[j][0]), fo[0][0]);                             \
      fo[0][1] = MFMA32(wpk[(j) & 7][1], __builtin_bit_cast(bf16x8, hf[j][0]), fo[0][1]); PIN(fo[0][0]); PIN(fo[0][1]); SB(); \
      if ((j) + HWIN < 22) HL(((j) + HWIN) % 22, 0);                                                                         \
      fo[1][0] = MFMA32(wpk[(j) & 7][0], __builtin_bit_cast(bf16x8, hf[j][1]), fo[1][0]);                             \
      fo[1][1] = MFMA32(wpk[(j) & 7][1], __builtin_bit_cast(bf16x8, hf[j][1]), fo[1][1]); PIN(fo[1][0]); PIN(fo[1][1]); SB(); \
      if ((j) + HWIN < 22) HL(((j) + HWIN) % 22, 1); } while (0)
    G2(0); STAMP(29); G2(1); G2(2); G2(3); G2(4); G2(5); G2(6); G2(7); G2(8); G2(9); G2(10);
    G2(11); G2(12); G2(13); G2(14); G2(15); G2(16); G2(17); G2(18); G2(19); G2(20); G2(21);
    STAMP(11);
#pragma unroll
    for (int t = 0; t < 2; ++t)
#pragma unroll
      for (int dt = 0; dt < 2; ++dt)
#pragma unroll
        for (int i = 0; i < 4; ++i)
          lds_st<f32x4>(L0 + L_B2 + t * SLOT + wj * 10240 + l31 * 256 + (((8 * dt + 2 * i + hh) ^ l7) << 4),
                        f32x4{fo[t][dt][4 * i], fo[t][dt][4 * i + 1], fo[t][dt][4 * i + 2], fo[t][dt][4 * i + 3]});
  }
  st2 = spin_tag(rW, VS2(), st2, elo);
  LBAR();
  STAMP(12);
  {
    u32x2 zt[2];
    const int o = 4 * (G >> 1) + pp;
#pragma unroll
    for (int j = 0; j < 2; ++j) { const int r = 8 * o + 4 * j + qq;
      zt[j] = __builtin_bit_cast(u32x2, trd(L0 + L_ZT + r * 128 + ((w ^ fsw(r)) << 4) + 8 * (G & 1))); }
    const unsigned pb = L0 + L_B2 + (w >> 2) * SLOT + (8 * wj + l3) * 256;
    f32x4 p0 = {0.f, 0.f, 0.f, 0.f}, p1 = p0;
#pragma unroll
    for (int k = 0; k < 4; ++k) {
      p0 += lds_ld<f32x4>(pb + k * 10240 + (((2 * l7) ^ l3) << 4));
      p1 += lds_ld<f32x4>(pb + k * 10240 + (((2 * l7 + 1) ^ l3) << 4));
    }
    const int src = 16 * (((l7 >> 2) << 1) | (l3 >> 2)) + 4 * (l7 & 3) + (l3 & 3);
    u32x4 zq;
    zq[0] = (unsigned)__builtin_amdgcn_ds_bpermute(src * 4, (int)zt[0].x); zq[1] = (unsigned)__builtin_amdgcn_ds_bpermute(src * 4, (int)zt[0].y);
    zq[2] = (unsigned)__builtin_amdgcn_ds_bpermute(src * 4, (int)zt[1].x); zq[3] = (unsigned)__builtin_amdgcn_ds_bpermute(src * 4, (int)zt[1].y);
    const float mean = blo(st2.x), rs = __builtin_amdgcn_rcpf(bhi(st2.x));
    float o8[8];
#pragma unroll
    for (int i = 0; i < 8; ++i) {
      const float xv = (i & 1) ? bhi(xo[i >> 1]) : blo(xo[i >> 1]), zv = (i & 1) ? bhi(zq[i >> 1]) : blo(zq[i >> 1]);
      const float F = i < 4 ? p0[i & 3] : p1[i & 3];
      const float gs = F * __builtin_amdgcn_rcpf(1.0f + __builtin_amdgcn_exp2f(F * -1.4426950408889634f));
      o8[i] = __builtin_fmaf((xv - mean) * rs * zv, gs, xv);
    }
    const u32x4 ov = {cvtpk(o8[0], o8[1]), cvtpk(o8[2], o8[3]), cvtpk(o8[4], o8[5]), cvtpk(o8[6], o8[7])};
    *(GAS u32x4*)((GAS char*)out + VXO()) = ov;
  }
}

extern "C" void kernel_launch(void* const* d_in, const int* in_sizes, int n_in, void* d_out, int out_size, void* d_ws, size_t ws_size,
                              hipStream_t stream) {
  (void)in_sizes; (void)n_in; (void)out_size;
  (void)hipFuncSetAttribute((const void*)h3_geglu_fused, hipFuncAttributeMaxDynamicSharedMemorySize, LDS_BYTES);
  h3_geglu_fused<<<256, 512, LDS_BYTES, stream>>>((const u16*)d_in[0], (const u16*)d_in[1], (const u16*)d_in[2], (const u16*)d_in[3],
                                                  (const u16*)d_in[4], (const u16*)d_in[5], (const u16*)d_in[7], (const u16*)d_in[8],
                                                  (const u16*)d_in[9], (u16*)d_out, (char*)d_ws + WS_BASE(ws_size));
}
```

```cpp
#include <hip/hip_runtime.h>
#include <hip/hip_bf16.h>

typedef unsigned short u16;
typedef unsigned long long u64;
typedef __attribute__((ext_vector_type(8))) short bf16x8;
typedef __attribute__((ext_vector_type(4))) short s16x4;
typedef __attribute__((ext_vector_type(2))) __bf16 bf16x2_t;
typedef __attribute__((ext_vector_type(2))) float f32x2;
typedef __attribute__((ext_vector_type(4))) float f32x4;
typedef __attribute__((ext_vector_type(16))) float f32x16;
typedef __attribute__((ext_vector_type(4))) unsigned u32x4;
typedef __attribute__((ext_vector_type(2))) unsigned u32x2;
#define LAS __attribute__((address_space(3)))
#define GAS __attribute__((address_space(1)))
typedef __amdgpu_buffer_rsrc_t rsrc_t;

constexpr int S_ = 2048, D_ = 512, DF = 1376;
constexpr unsigned WS_GEN = 0, WS_FA = 4096, WS_FH = WS_FA + 256 * 128, WS_FZ = WS_FH + 256 * 128;
constexpr unsigned WS_ST = WS_FZ + 256 * 128;
constexpr unsigned WS_KVT = 1u << 20;
constexpr unsigned WS_ZT = WS_KVT + (2u << 20);
constexpr unsigned WS_H = WS_ZT + (2u << 20);
constexpr unsigned WS_BYTES = WS_H + 64 * 86 * 1024;
constexpr int SLOT = 40960;
constexpr int L_CV = 3 * SLOT;
constexpr int L_KA = L_CV, L_VA = L_KA + 9216;
constexpr int L_B1 = 0, L_B2 = SLOT, L_B3 = 2 * SLOT;
constexpr int L_CS = 32768;
constexpr int L_ST = L_CS + 3072;
constexpr int L_SY = L_ST + 512;
constexpr int L_A = L_VA + 8192;
constexpr int L_P0 = L_CV + 8192;
constexpr int L_U0 = 36864, L_U1 = L_CV + 33280, L_ZT = L_CV;
constexpr int LDS_BYTES = 163840;
constexpr unsigned OOB = 0x80000000u;
constexpr unsigned POISON = 0xffffffffu;
constexpr unsigned SPIN_MAX = 1u << 18;

#define LPRIO 2
#define XSLEEP 4
#define ROT1 ((rt + cg) & 7)
#define ROT2 0
#define HWIN 12
#define STEPMODE 1
#define NOSTATS 0
#define WS_BASE(sz) 0
#define ZSLEEP 4
#define ABL_PLAIN 0
#define MFMA32(a, b, c) __builtin_amdgcn_mfma_f32_32x32x16_bf16(a, b, c, 0, 0, 0)
#define MFMA16(a, b, c) __builtin_amdgcn_mfma_f32_16x16x32_bf16(a, b, c, 0, 0, 0)
#define WAIT_BAR(N) asm volatile("s_waitcnt vmcnt(" #N ") lgkmcnt(0)\n\ts_barrier" ::: "memory")
#define LBAR() asm volatile("s_waitcnt lgkmcnt(0)\n\ts_barrier" ::: "memory")
#define WAITV(N) asm volatile("s_waitcnt vmcnt(" #N ")" ::: "memory")
#define SB() __builtin_amdgcn_sched_barrier(0)

__device__ __forceinline__ rsrc_t mk_rsrc(const void* p, unsigned bytes) {
  return __builtin_amdgcn_make_buffer_rsrc((void*)p, (short)0, (int)bytes, 0x00020000);
}
__device__ __forceinline__ void dma16(rsrc_t rs, unsigned voff, unsigned soff, unsigned lds) {
  asm volatile("s_mov_b32 m0, %0\n\ts_nop 0\n\tbuffer_load_dwordx4 %1, %2, %3 offen lds" ::"s"(lds), "v"(voff), "s"(rs), "s"(soff) : "memory");
}
__device__ __forceinline__ u32x4 ld16(rsrc_t rs, unsigned voff) {
  u32x4 r; asm volatile("buffer_load_dwordx4 %0, %1, %2, 0 offen" : "=v"(r) : "v"(voff), "s"(rs) : "memory"); return r;
}
__device__ __forceinline__ u32x4 ld16_dev(rsrc_t rs, unsigned voff, unsigned soff) {
  u32x4 r; asm volatile("buffer_load_dwordx4 %0, %1, %2, %3 offen sc1" : "=v"(r) : "v"(voff), "s"(rs), "s"(soff) : "memory"); return r;
}
__device__ __forceinline__ u32x2 ld8(rsrc_t rs, unsigned voff) {
  u32x2 r; asm volatile("buffer_load_dwordx2 %0, %1, %2, 0 offen" : "=v"(r) : "v"(voff), "s"(rs) : "memory"); return r;
}
__device__ __forceinline__ u32x2 ld8_dev(rsrc_t rs, unsigned voff) {
  u32x2 r; asm volatile("buffer_load_dwordx2 %0, %1, %2, 0 offen sc1" : "=v"(r) : "v"(voff), "s"(rs) : "memory"); return r;
}
#define PIN(x) asm volatile("" : "+v"(x))
__device__ __forceinline__ void st16_dev(rsrc_t rs, unsigned voff, u32x4 v) { __builtin_amdgcn_raw_buffer_store_b128(v, rs, voff, 0, 16); }
__device__ __forceinline__ void st8_dev(rsrc_t rs, unsigned voff, u32x2 v) { __builtin_amdgcn_raw_buffer_store_b64(v, rs, voff, 0, 16); }

__device__ __forceinline__ unsigned cvtpk(float lo, float hi) {
  bf16x2_t r = {(__bf16)lo, (__bf16)hi};
  return __builtin_bit_cast(unsigned, r);
}
__device__ __forceinline__ float blo(unsigned w) { return __uint_as_float(w << 16); }
__device__ __forceinline__ float bhi(unsigned w) { return __uint_as_float(w & 0xffff0000u); }
__device__ __forceinline__ float rbf(float f) { return blo(cvtpk(f, f)); }
__device__ __forceinline__ s16x4 trd(unsigned a) { return __builtin_amdgcn_ds_read_tr16_b64_v4i16((LAS s16x4*)(size_t)a); }
__device__ __forceinline__ bf16x8 cat(s16x4 a, s16x4 b) { return __builtin_shufflevector(a, b, 0, 1, 2, 3, 4, 5, 6, 7); }
template <class T> __device__ __forceinline__ T lds_ld(unsigned a) { return *(const LAS T*)(size_t)a; }
template <class T> __device__ __forceinline__ void lds_st(unsigned a, T v) { *(LAS T*)(size_t)a = v; }
template <int CTRL> __device__ __forceinline__ float dpp_add(float x) {
  return x + __uint_as_float(__builtin_amdgcn_update_dpp(0u, __float_as_uint(x), CTRL, 0xf, 0xf, true));
}
__device__ __forceinline__ float swap_add(float x, bool half) {
  if (half) { auto r = __builtin_amdgcn_permlane32_swap(__float_as_uint(x), __float_as_uint(x), false, false); return __uint_as_float(r[0]) + __uint_as_float(r[1]); }
  auto r = __builtin_amdgcn_permlane16_swap(__float_as_uint(x), __float_as_uint(x), false, false); return __uint_as_float(r[0]) + __uint_as_float(r[1]);
}
__device__ __forceinline__ void wave_sum4(float& a, float& b, float& c, float& d) {
  a = dpp_add<0x128>(a); b = dpp_add<0x128>(b); c = dpp_add<0x128>(c); d = dpp_add<0x128>(d);
  a = dpp_add<0x124>(a); b = dpp_add<0x124>(b); c = dpp_add<0x124>(c); d = dpp_add<0x124>(d);
  a = dpp_add<0x122>(a); b = dpp_add<0x122>(b); c = dpp_add<0x122>(c); d = dpp_add<0x122>(d);
  a = dpp_add<0x121>(a); b = dpp_add<0x121>(b); c = dpp_add<0x121>(c); d = dpp_add<0x121>(d);
  a = swap_add(a, false); b = swap_add(b, false); c = swap_add(c, false); d = swap_add(d, false);
  a = swap_add(a, true); b = swap_add(b, true); c = swap_add(c, true); d = swap_add(d, true);
}
__device__ __forceinline__ void lds_wait_ge(unsigned a, unsigned v) {
  for (unsigned n = 0; *(volatile LAS unsigned*)(size_t)a < v && n < SPIN_MAX * 16; ++n) __builtin_amdgcn_s_sleep(1);
  asm volatile("" ::: "memory");
}
__device__ __forceinline__ void lds_spin_ge(unsigned a, unsigned v) {
  for (unsigned n = 0; *(volatile LAS unsigned*)(size_t)a < v && n < SPIN_MAX * 64; ++n) {}
  asm volatile("" ::: "memory");
}
__device__ __forceinline__ unsigned lds_inc(unsigned a) {
  return __hip_atomic_fetch_add((LAS unsigned*)(size_t)a, 1u, __ATOMIC_RELAXED, __HIP_MEMORY_SCOPE_WORKGROUP);
}
__device__ __forceinline__ int fsw(int r) { return ((r >> 1) & 1) | (((r >> 3) & 3) << 1); }
__device__ __forceinline__ void spin_flags(rsrc_t rs, unsigned voff, u32x2 f, unsigned lo, unsigned hi) {
  for (unsigned n = 0; !__all(f.x == lo && f.y == hi) && n < SPIN_MAX; ++n) {
    __builtin_amdgcn_s_sleep(1);
    f = ld8_dev(rs, voff); WAITV(0); PIN(f);
  }
}
__device__ __forceinline__ u32x2 spin_tag(rsrc_t rs, unsigned voff, u32x2 f, unsigned tag) {
  for (unsigned n = 0; !__all(f.y == tag) && n < SPIN_MAX; ++n) {
    __builtin_amdgcn_s_sleep(1);
    f = ld8_dev(rs, voff); WAITV(0); PIN(f);
  }
  return f;
}
#define STAMP(i)

__global__ __launch_bounds__(512, 2) void h3_geglu_fused(const u16* __restrict__ x, const u16* __restrict__ kin, const u16* __restrict__ vin,
                                                         const u16* __restrict__ shk, const u16* __restrict__ ssk, const u16* __restrict__ ssd,
                                                         const u16* __restrict__ wg, const u16* __restrict__ wu, const u16* __restrict__ wd,
                                                         u16* __restrict__ out, char* __restrict__ ws) {
  extern __shared__ __attribute__((aligned(16))) char smem[];
  const unsigned L0 = (unsigned)(size_t)(LAS char*)smem;
  const int tid = threadIdx.x, lane = tid & 63;
  const int w = __builtin_amdgcn_readfirstlane(tid >> 6), wj = w & 3;
  STAMP(0);
  const int b = blockIdx.x, xcd = b & 7, idx = b >> 3, rh = xcd & 1, q4 = xcd >> 1;
  const int pan = 4 * rh + q4, aa = 32 * pan + idx;
  const int l3 = lane >> 3, l7 = lane & 7, G = lane >> 4, qq = (lane >> 2) & 3, pp = lane & 3, hh = lane >> 5, l31 = lane & 31;
  const rsrc_t rX = mk_rsrc(x, S_ * D_ * 2), rW = mk_rsrc(ws, WS_BYTES), rD = mk_rsrc(wd, DF * D_ * 2);
  const int cg = idx < 24 ? ((q4 * 7 + 1) >> 1) + (idx >> 3) : idx < 28 ? (q4 < 2 ? 3 : 10) : 14 + ((idx >> 1) & 1);
  const int rt = 8 * rh + (idx < 24 ? (idx & 7) : idx < 28 ? (q4 & 1) * 4 + (idx & 3) : 2 * q4 + (idx & 1));
  const int r64 = 4 * pan + (idx >> 3), ct = idx & 7;
  const int ks0 = 22 * wj - (wj == 3 ? 1 : 0);
  u32x4 xo; u32x2 st2;
  unsigned elo, ehi;
#define VD() (unsigned)((16 * ks0 + l3) * 1024 + ct * 128 + ((l7 ^ (((l3 >> 1) & 1) << 2)) << 4))
  const int rot2 = ROT2;
#define PH(j) ((j) + rot2 >= 22 ? (j) + rot2 - 22 : (j) + rot2)
  const int klast = wj < 2 ? 21 : 20;
#define WDU(j, lds) do { const unsigned v_ = vd | (PH(j) > klast ? OOB : 0u);                                       \
    dma16(rD, v_, PH(j) * 16384, L0 + (lds)); dma16(rD, v_, PH(j) * 16384 + 8192, L0 + (lds) + 1024); } while (0)
#define VXO() (unsigned)((64 * r64 + 8 * w + l3) * 1024 + ct * 128 + l7 * 16)
#define VS2() (WS_ST + (unsigned)((64 * r64 + 8 * w + l3) * 8))

  bf16x8 wpk[8][2];
#define AD() (L0 + (8 * hh + qq) * 128 + ((4 * ((qq >> 1) & 1) + 2 * (G & 1) + (pp >> 1)) << 4) + 8 * (pp & 1))
#define WDF(base, dt) cat(trd(((base) ^ ((dt) * 64))), trd(((base) ^ ((dt) * 64)) + 512))
#define UA(j) ((j) < 4 ? a1 + (j) * 2048 : (j) < 9 ? a2 + ((j) - 4) * 2048 : (j) < 14 ? a2 + SLOT + ((j) - 9) * 2048 : (j) < 18 ? a1 + ((j) - 14) * 2048 : a2 + ((j) - 18) * 2048)
#define PARK() do { const unsigned a1 = AD() + L_B1 + wj * 8192, a2 = AD() + L_B2 + wj * 10240;                         \
    _Pragma("unroll") for (int j = 0; j < 8; ++j) { wpk[j][0] = WDF(UA(j), 0); wpk[j][1] = WDF(UA(j), 1); }              \
    _Pragma("unroll") for (int j = 0; j < 8; ++j) { PIN(wpk[j][0]); PIN(wpk[j][1]); } } while (0)
#define LDS_SET(a, v) do { asm volatile("" ::: "memory"); *(volatile LAS unsigned*)(size_t)(a) = (v); } while (0)

  if (w >= 4) {
    __builtin_amdgcn_s_setprio(2);
    u32x2 gen = ld8_dev(rW, WS_GEN);
    const unsigned vx = (cg < 15) ? (unsigned)((128 * rt + 8 * wj + l3) * 1024 + ((l7 ^ ((4 * wj + (l3 >> 1)) & 7)) << 4)) : OOB;
    unsigned vg[3], vu[3];
#pragma unroll
    for (int r = 0; r < 3; ++r) {
      const unsigned P = 64 * r + lane, k = (P * 43691u) >> 19, col = 96 * cg + 8 * (P - 12 * k);
      vg[r] = (col < DF) ? (16 * wj + k) * (DF * 2) + col * 2 : OOB; vu[r] = vg[r] ^ 32u;
    }
    const rsrc_t rG = mk_rsrc(wg, D_ * DF * 2), rU = mk_rsrc(wu, D_ * DF * 2);
    const int rot1 = ROT1;
    unsigned dpv;
    asm volatile("buffer_load_dword %0, %1, %2, 0 offen" : "=v"(dpv) : "v"(aa * 4), "s"(mk_rsrc(ssd, D_ * 2)) : "memory");
#pragma unroll
    for (int i = 0; i < 2; ++i) st16_dev(rW, WS_ZT + (unsigned)(2 * aa * 4096 + (2 * wj + i) * 1024 + lane * 16), u32x4{POISON, POISON, POISON, POISON});
    if (tid == 256) lds_st<unsigned>(L0 + L_A, 0u);
    LBAR();
#pragma nounroll
    for (int c = 0; c < 8; ++c) {
      const int cc = (c + rot1) & 7;
      const unsigned sx = cc * 128, sw = cc * (64 * DF * 2), lx = L0 + (c & 3) * SLOT + wj * 1024, lw = L0 + (c & 3) * SLOT + 16384 + wj * 3072;
      dma16(rX, vx, sx, lx);                 dma16(rG, vg[0], sw, lw);        dma16(rU, vu[0], sw, lw + 12288);
      dma16(rX, vx, sx + 32768, lx + 4096);  dma16(rG, vg[1], sw, lw + 1024); dma16(rU, vu[1], sw, lw + 13312);
      if (c >= 2) asm volatile("s_waitcnt vmcnt(16)\n\ts_barrier" ::: "memory");
      if (c == 2) STAMP(16);
      if (c == 3) STAMP(18);
      if (c == 7) STAMP(23);
      if (c == 5 && tid == 256) {
        PIN(gen); const unsigned lo = gen.x + 1u;
        st8_dev(rW, WS_FA + (unsigned)(aa * 128), u32x2{lo, gen.y + (lo == 0u ? 1u : 0u)});
      }
      dma16(rX, vx, sx + 65536, lx + 8192);  dma16(rG, vg[2], sw, lw + 2048); dma16(rU, vu[2], sw, lw + 14336);
      dma16(rX, vx, sx + 98304, lx + 12288);
    }
    PIN(gen); PIN(dpv);
    elo = __builtin_amdgcn_readfirstlane(gen.x) + 1u;
    ehi = __builtin_amdgcn_readfirstlane(gen.y) + (elo == 0u ? 1u : 0u);
    const unsigned dpair = __builtin_amdgcn_readfirstlane(dpv);
    const int ce = tid & 255;
    const unsigned vd = VD();
    const unsigned vfa = WS_FA + (unsigned)((32 * (hh ? ct : pan) + l31) * 128), vst = WS_ST + (unsigned)((128 * rt + 64 * (wj & 1) + lane) * 8);
    const u32x4 zero4 = {0u, 0u, 0u, 0u};
    u32x2 stg = ld8_dev(rW, vst), fa = ld8_dev(rW, vfa);
#pragma unroll
    for (int j = 0; j < 4; ++j) WDU(j, L_B1 + wj * 8192 + j * 2048);
    if (ce < 7) lds_st<u32x4>(L0 + L_SY + ce * 16, zero4);
    WAIT_BAR(20);
#pragma unroll
    for (int j = 0; j < 5; ++j) WDU(4 + j, L_B2 + wj * 10240 + j * 2048);
    asm volatile("s_waitcnt vmcnt(18)\n\ts_barrier" : "+v"(stg), "+v"(fa)::"memory");
    STAMP(4);
    stg = spin_tag(rW, vst, stg, elo);
    if (wj < 2) lds_st<unsigned>(L0 + L_ST + (64 * wj + lane) * 4, stg.x);
    spin_flags(rW, vfa, fa, elo, ehi);
    u32x4 hc[2], hp[2], ur[2];
    {
      const rsrc_t rHk = mk_rsrc(ssk, D_ * S_ * 2);
#pragma unroll
      for (int c = 0; c < 2; ++c) {
        hc[c] = ld16(rHk, (unsigned)((2 * aa + c) * 4096 + ce * 16));
        hp[c] = ld16(rHk, ce ? (unsigned)((2 * aa + c) * 4096 + ce * 16 - 16) : OOB);
        ur[c] = ld16_dev(rW, WS_KVT + (unsigned)(2 * aa * 4096 + ce * 16), c * 4096);
      }
    }
#pragma unroll
    for (int j = 0; j < 2; ++j) WDU(9 + j, L_B3 + wj * 10240 + j * 2048);
    WAIT_BAR(10);
    STAMP(5);
#pragma unroll
    for (int j = 2; j < 5; ++j) WDU(9 + j, L_B3 + wj * 10240 + j * 2048);
    {
      asm volatile("s_waitcnt vmcnt(10)" : "+v"(hc[0]), "+v"(hc[1]), "+v"(hp[0]), "+v"(hp[1]), "+v"(ur[0]), "+v"(ur[1])::"memory");
      auto put = [&](unsigned hb, u32x4 pv, u32x4 cu, int blk) {
        const unsigned a0 = __builtin_amdgcn_alignbit(pv[1], pv[0], 16), a1 = __builtin_amdgcn_alignbit(pv[2], pv[1], 16),
                       a2 = __builtin_amdgcn_alignbit(pv[3], pv[2], 16), a3 = __builtin_amdgcn_alignbit(cu[0], pv[3], 16),
                       a4 = __builtin_amdgcn_alignbit(cu[1], cu[0], 16);
        lds_st<u32x4>(hb + blk * 16, u32x4{a0, a1, a2, a3});
        lds_st<u32x4>(hb + 4160 + blk * 16, u32x4{pv[1], pv[2], pv[3], cu[0]});
        lds_st<u32x4>(hb + 8320 + blk * 16, u32x4{a1, a2, a3, a4});
        lds_st<u32x4>(hb + 12480 + blk * 16, u32x4{pv[2], pv[3], cu[0], cu[1]});
      };
#pragma unroll
      for (int c = 0; c < 2; ++c) {
        put(L0 + L_CV + c * 16640, hp[c], hc[c], ce + 1);
        if (ce == 0 || ce == 255) put(L0 + L_CV + c * 16640, ce ? hc[c] : zero4, zero4, ce ? 257 : 0);
        lds_st<u32x4>(L0 + (c ? L_U1 : L_U0) + ce * 16, ur[c]);
      }
      asm volatile("s_waitcnt lgkmcnt(0)" ::: "memory");
      if (lane == 0) lds_inc(L0 + L_SY + 24);
    }
    PARK();
    LDS_SET(L0 + L_SY + 64 + wj * 4, 1u);
    if (b == 0 && wj == 0) {
      u32x2 f[4];
#pragma unroll
      for (int i = 0; i < 4; ++i) f[i] = ld8_dev(rW, WS_FA + (unsigned)((64 * i + lane) * 128));
      asm volatile("s_waitcnt vmcnt(0)" : "+v"(f[0]), "+v"(f[1]), "+v"(f[2]), "+v"(f[3])::"memory");
#pragma unroll
      for (int i = 0; i < 4; ++i) spin_flags(rW, WS_FA + (unsigned)((64 * i + lane) * 128), f[i], elo, ehi);
      if (lane == 0) { u32x2 e = {elo, ehi}; st8_dev(rW, WS_GEN, e); }
    }
    STAMP(7);
    const int vch = wj >> 1, vrh = wj & 1, c15 = lane & 15;
    f32x4 ya[8];
#pragma unroll
    for (int c = 0; c < 8; ++c) ya[c] = f32x4{0.f, 0.f, 0.f, 0.f};
    {
      unsigned cA = L0 + L_CV + vch * 16640 + (c15 & 3) * 4160 + (4 * (c15 >> 2) + 8 * G + 128 * vrh) * 2;
      const unsigned ub = L0 + (vch ? L_U1 : L_U0);
      unsigned cB = ub + (240 - 16 * c15 + 8 * G + 128 * vrh) * 2;
      int lim = ((16 * c15 - 8 * G + 47) >> 5) - 4 * vrh;
      PIN(cA); PIN(cB);
      bf16x8 bfr[2][8]; s16x4 ah[2][4];
#define VLD8(a) (*(const volatile LAS s16x4*)(size_t)(a))
#define CA(set, r, p, i) ah[set][i] = VLD8(cA + (r) * 64 + (((i) & 2) ? 7 - (p) : (p)) * 512 + ((i) & 1) * 8)
#define CB(r, mu) bfr[(r) & 1][mu] = lds_ld<bf16x8>((mu) == 0 ? ((r) >= lim ? L0 + L_SY + 32 : cB + (r) * 64 + 3584) : cB + (r) * 64 + (7 - (mu)) * 512)
#define CM(r, p, m) do { if ((m) + (p) < 8) { ya[(p) + (m)] = MFMA16(cat(ah[(p) & 1][0], ah[(p) & 1][1]), bfr[(r) & 1][(m) & 7], ya[((p) + (m)) & 7]); PIN(ya[((p) + (m)) & 7]); } \
        else { ya[(m) - 1] = MFMA16(cat(ah[(p) & 1][2], ah[(p) & 1][3]), bfr[(r) & 1][((m) + (p)) & 7], ya[(m) - 1]); PIN(ya[(m) - 1]); } } while (0)
#define CPAIR(r, p) do { _Pragma("unroll") for (int m = 0; m < 9; ++m) { CM(r, p, m);                                               \
          if (m < 4) CA(((p) + 1) & 1, (p) < 3 ? (r) : (r) + 1, ((p) + 1) & 3, m); else if (m < 6) CB((r) + 1, 2 * (p) + m - 4); SB(); } } while (0)
#define CONV(r) do { CPAIR(r, 0); CPAIR(r, 1); CPAIR(r, 2); CPAIR(r, 3); } while (0)
      lds_wait_ge(L0 + L_SY + 24, 8u); __builtin_amdgcn_s_setprio(2);
      STAMP(24);
#pragma unroll
      for (int i = 0; i < 4; ++i) CA(0, 0, 0, i);
#pragma unroll
      for (int mu = 0; mu < 8; ++mu) CB(0, mu);
#pragma nounroll
      for (int it = 0; it < 2; ++it) {
        CONV(0); CPAIR(1, 0); CPAIR(1, 1); CPAIR(1, 2);
        if (it) { asm volatile("s_waitcnt lgkmcnt(0)" ::: "memory"); *(volatile LAS unsigned*)(size_t)(L0 + L_SY + 48 + wj * 4) = 1u; }
        CPAIR(1, 3);
        cA += 128; cB += 128; lim -= 2; PIN(cA); PIN(cB);
      }
    }
    STAMP(27);
    WAITV(0);
    *(volatile LAS unsigned*)(size_t)(L0 + L_SY + wj * 4) = 1u;
    {
      const unsigned mine = L0 + L_SY + 48 + wj * 4, other = L0 + L_SY + 48 + (wj ^ 1) * 4;
      const unsigned xo_ = L0 + L_CV + vch * 16640 + (vrh ^ 1) * 4096 + lane * 16, xb = L0 + L_CV + vch * 16640 + vrh * 4096;
      const float dsc = vch ? bhi(dpair) : blo(dpair);
      u32x2 uq[4];
#pragma unroll
      for (int c = 0; c < 4; ++c) uq[c] = lds_ld<u32x2>(L0 + (vch ? L_U1 : L_U0) + (2044 - 256 * (4 * vrh + c) - 16 * c15 - 4 * G) * 2);
      lds_spin_ge(other, 1u);
      f32x4 y[4];
      if (vrh) {
        asm volatile("" ::: "memory");
#pragma unroll
        for (int c = 0; c < 4; ++c) { lds_st<f32x4>(xo_ + c * 1024, ya[c]); y[c] = ya[4 + c]; PIN(y[c]); }
      } else {
#pragma unroll
        for (int c = 0; c < 4; ++c) { lds_st<f32x4>(xo_ + c * 1024, ya[4 + c]); y[c] = ya[c]; PIN(y[c]); }
        asm volatile("" ::: "memory");
      }
      asm volatile("s_waitcnt lgkmcnt(0)" ::: "memory");
      *(volatile LAS unsigned*)(size_t)mine = 2u;
#pragma unroll
      for (int c = 0; c < 4; ++c)
#pragma unroll
        for (int e = 0; e < 4; ++e) {
          const unsigned uu = (e & 2) ? uq[c].x : uq[c].y;
          float s = __builtin_fmaf((e & 1) ? blo(uu) : bhi(uu), dsc, y[c][e]); PIN(s); y[c][e] = s;
        }
      lds_spin_ge(other, 2u);
      f32x4 o[4];
#pragma unroll
      for (int c = 0; c < 4; ++c) o[c] = lds_ld<f32x4>(xb + c * 1024 + lane * 16);
      SB();
      u32x2 zp[4];
#pragma unroll
      for (int c = 0; c < 4; ++c) {
        float z[4];
#pragma unroll
        for (int e = 0; e < 4; ++e) { z[e] = y[c][e] + o[c][e]; asm("" : "+v"(z[e])); }
        zp[c] = u32x2{min(cvtpk(z[0], z[1]), POISON - 1u), min(cvtpk(z[2], z[3]), POISON - 1u)};
      }
#pragma unroll
      for (int c = 0; c < 4; ++c) lds_st<u32x2>(xb + c * 512 + c15 * 32 + G * 8, zp[c]);
#pragma unroll
      for (int i = 0; i < 2; ++i)
        st16_dev(rW, WS_ZT + (unsigned)((2 * aa + vch) * 4096 + vrh * 2048 + i * 1024 + lane * 16), lds_ld<u32x4>(xb + i * 1024 + lane * 16));
    }
    STAMP(26);
    xo = ld16(rX, VXO()); st2 = ld8_dev(rW, VS2());
    if (lane == 0) lds_inc(L0 + L_SY + 20);
    STAMP(9);
    lds_spin_ge(L0 + L_SY + 80 + wj * 4, 1u);
    STAMP(31);
  } else {
    __builtin_amdgcn_s_setprio(3);
    u32x2 sh[2]; u32x4 xr[2];
#pragma unroll
    for (int i = 0; i < 2; ++i) xr[i] = ld16(rX, (unsigned)((8 * aa + 2 * wj + i) * 1024 + lane * 16));
    {
      const rsrc_t rK = mk_rsrc(kin, S_ * D_ * 2), rV = mk_rsrc(vin, S_ * D_ * 2);
      const unsigned kb = (unsigned)((64 * idx + l3) * 1024 + pan * 128), fl = (unsigned)(l7 ^ ((l3 >> 1) & 1));
#pragma unroll
      for (int i = 0; i < 2; ++i) {
        const int g = 2 * wj + i;
        dma16(rK, kb + ((fl ^ (((g + 1) & 3) << 1)) << 4), g * 8192, L0 + L_KA + (g + 1) * 1024);
        dma16(rV, kb + ((fl ^ ((g & 3) << 1)) << 4), g * 8192, L0 + L_VA + g * 1024);
      }
      if (wj == 0) dma16(rK, idx == 0 ? OOB : kb - 8192 + (fl << 4), 0, L0 + L_KA);
    }
    u32x2 gen = ld8_dev(rW, WS_GEN);
    {
      const rsrc_t rSh = mk_rsrc(shk, D_ * 4 * 2);
#pragma unroll
      for (int i = 0; i < 2; ++i) sh[i] = ld8(rSh, (unsigned)((64 * pan + 16 * wj + 8 * i + 4 * (G & 1) + pp) * 8));
    }
    asm volatile("s_barrier" ::: "memory");
    STAMP(3);
    const int tp = wj & 1, ch = wj >> 1;
    f32x16 acc[2][3];
    f32x4 cs[3];
    unsigned ax[4], aw[2][3], ak[5];
    unsigned vkv = WS_KVT + (unsigned)((64 * pan + 16 * wj + l3) * 4096 + (1984 - 64 * idx + 8 * l7) * 2);
    unsigned vst0 = lane < 2 ? WS_ST + (unsigned)((8 * aa + 2 * wj + lane) * 8) : OOB;
    int src4 = 4 * (16 * ((((7 - l7) >> 2) << 1) | (l3 >> 2)) + 4 * ((7 - l7) & 3) + (l3 & 3));
    const unsigned so_ = ((lane & 15) == 4 * (G & 1)) ? 0x3f803f80u : 0u;
    bf16x8 sel = __builtin_bit_cast(bf16x8, u32x4{so_, so_, so_, so_});
    {
      const unsigned ax0 = L0 + (64 * tp + l31) * 128 + (((4 * tp + hh) ^ ((l31 >> 1) & 7)) << 4);
      const unsigned aw0 = L0 + 16384 + (G & 1) * 12288 + (8 * hh + qq) * 192 + ((pp & 1) << 4) + 8 * (pp >> 1);
#pragma unroll
      for (int s = 0; s < 4; ++s) { ax[s] = ax0 ^ (s << 5); PIN(ax[s]); }
#pragma unroll
      for (int i = 0; i < 3; ++i) {
        const unsigned a_ = aw0 + (((3 * ch + i) ^ (G & 1)) << 5);
        aw[0][i] = a_ + tp * 6144; aw[1][i] = a_ + 6144 - tp * 6144; PIN(aw[0][i]); PIN(aw[1][i]);
      }
#pragma unroll
      for (int j = 0; j < 5; ++j) { const int rr = 8 * (4 * (G >> 1) + pp) + qq + (j < 3 ? 4 + 4 * j : 4 * (j - 3));
        ak[j] = L0 + (j < 3 ? L_KA : L_VA) + rr * 128 + (((2 * wj) ^ fsw(rr)) << 4) + 8 * (G & 1); PIN(ak[j]); }
      PIN(vkv); PIN(vst0); PIN(src4); PIN(sel);
    }
    {
      asm volatile("s_waitcnt vmcnt(7)" : "+v"(xr[0]), "+v"(xr[1])::"memory");
      STAMP(14);
      const f32x2 x0 = {blo(__builtin_amdgcn_readfirstlane(xr[0][0])), blo(__builtin_amdgcn_readfirstlane(xr[1][0]))};
      f32x2 d[8];
#pragma unroll
      for (int e = 0; e < 4; ++e) { d[2 * e] = f32x2{blo(xr[0][e]), blo(xr[1][e])} - x0; d[2 * e + 1] = f32x2{bhi(xr[0][e]), bhi(xr[1][e])} - x0; }
      const f32x2 sm = ((d[0] + d[1]) + (d[2] + d[3])) + ((d[4] + d[5]) + (d[6] + d[7]));
      const f32x2 sq = ((d[0] * d[0] + d[1] * d[1]) + (d[2] * d[2] + d[3] * d[3])) + ((d[4] * d[4] + d[5] * d[5]) + (d[6] * d[6] + d[7] * d[7]));
      float s0 = sm[0], s1 = sm[1], q0 = sq[0], q1 = sq[1];
      wave_sum4(s0, s1, q0, q1);
      const float m = (lane ? s1 : s0) * (1.0f / D_), var = (lane ? q1 : q0) * (1.0f / D_) - m * m;
      unsigned ms0 = cvtpk(m + (lane ? x0[1] : x0[0]), __builtin_amdgcn_sqrtf(rbf(rbf(__builtin_fmaxf(var, 0.f)) + 1e-5f)));
      asm volatile("s_waitcnt vmcnt(0)" : "+v"(gen), "+v"(sh[0]), "+v"(sh[1]), "+v"(ms0)::"memory");
      STAMP(15);
      if (lane == 0) lds_inc(L0 + L_A);
      elo = __builtin_amdgcn_readfirstlane(gen.x) + 1u;
      ehi = __builtin_amdgcn_readfirstlane(gen.y) + (elo == 0u ? 1u : 0u);
      st8_dev(rW, vst0, u32x2{ms0, elo});
      f32x2 sk[4] = {{blo(sh[0].x), blo(sh[1].x)}, {bhi(sh[0].x), bhi(sh[1].x)}, {blo(sh[0].y), blo(sh[1].y)}, {bhi(sh[0].y), bhi(sh[1].y)}};
      PIN(sk[0]); PIN(sk[1]); PIN(sk[2]); PIN(sk[3]);
      lds_spin_ge(L0 + L_A, 4u);
      STAMP(1);
      u32x2 t[2][5];
#pragma unroll
      for (int j = 0; j < 5; ++j) { t[0][j] = __builtin_bit_cast(u32x2, trd(ak[j])); t[1][j] = __builtin_bit_cast(u32x2, trd(ak[j] ^ 16)); }
      SB();
      f32x2 kk[20], r[8];
#pragma unroll
      for (int j = 0; j < 5; ++j) {
        kk[4 * j] = f32x2{blo(t[0][j].x), blo(t[1][j].x)}; kk[4 * j + 1] = f32x2{bhi(t[0][j].x), bhi(t[1][j].x)};
        kk[4 * j + 2] = f32x2{blo(t[0][j].y), blo(t[1][j].y)}; kk[4 * j + 3] = f32x2{bhi(t[0][j].y), bhi(t[1][j].y)};
      }
#pragma unroll
      for (int e = 0; e < 8; ++e) r[e] = kk[e + 1] * sk[3];
#pragma unroll
      for (int j = 2; j >= 0; --j)
#pragma unroll
        for (int e = 0; e < 8; ++e) r[e] = __builtin_elementwise_fma(kk[e + 4 - j], sk[j], r[e]);
#pragma unroll
      for (int e = 0; e < 8; ++e) r[e] *= kk[12 + e];
      u32x4 po[2];
#pragma unroll
      for (int i = 0; i < 2; ++i)
#pragma unroll
        for (int e = 0; e < 4; ++e) po[i][e] = (unsigned)__builtin_amdgcn_ds_bpermute(src4, (int)cvtpk(r[7 - 2 * e][i], r[6 - 2 * e][i]));
      st16_dev(rW, vkv, po[0]); st16_dev(rW, vkv + 32768, po[1]);
      __builtin_amdgcn_s_setprio(1);
    }
    STAMP(2);
    {
      bf16x8 xf[2][2]; s16x4 wf[2][3][2];
#define PINA(t, i) PIN(acc[t][i])
#define M1(p, t, i) do { acc[t][i] = MFMA32(cat(wf[p][i][0], wf[p][i][1]), xf[p][t], acc[t][i]); PINA(t, i); } while (0)
#define MS(p, i, on) do { if (on) { cs[i] = MFMA16(sel, cat(wf[p][i][0], wf[p][i][1]), cs[i]); PIN(cs[i]); } } while (0)
#define RW(s, i) do { wf[(s) & 1][i][0] = trd(aw[(s) >> 1][i] + ((s) & 1) * 3072); wf[(s) & 1][i][1] = trd(aw[(s) >> 1][i] + ((s) & 1) * 3072 + 768); } while (0)
#define RX(s, t) xf[(s) & 1][t] = lds_ld<bf16x8>(ax[s] + (t) * 4096)
#define KEEP(p) do { asm volatile("" ::"v"(wf[p][0][0]), "v"(wf[p][0][1]), "v"(wf[p][1][0]), "v"(wf[p][1][1]), "v"(wf[p][2][0]), "v"(wf[p][2][1]), "v"(xf[p][0]), "v"(xf[p][1])); } while (0)
#define ADV(a) do { a += dl; PIN(a); } while (0)
#define STEP(p, s, on) do { M1(p, 0, 0); RW(s, 0); SB(); M1(p, 1, 0); RX(s, 0); RX(s, 1); SB(); M1(p, 0, 1); RW(s, 1); SB();  \
        M1(p, 1, 1); RW(s, 2); SB(); M1(p, 0, 2);                                                                             \
        if ((s) == 3) { ADV(ax[0]); ADV(ax[1]); ADV(ax[2]); ADV(ax[3]); ADV(aw[0][0]); } SB(); M1(p, 1, 2);                     \
        if ((s) == 3) { ADV(aw[0][1]); ADV(aw[0][2]); ADV(aw[1][0]); ADV(aw[1][1]); ADV(aw[1][2]); }                            \
        MS(p, 0, on); MS(p, 1, on); MS(p, 2, on); KEEP(p); SB(); } while (0)
      LBAR();
      STAMP(16);
      RW(0, 0); RX(0, 0); RX(0, 1); RW(0, 1); RW(0, 2); SB();
      unsigned dl = SLOT;
#pragma unroll
      for (int i = 0; i < 3; ++i) {
        cs[i] = f32x4{0.f, 0.f, 0.f, 0.f};
#pragma unroll
        for (int r = 0; r < 16; ++r) { acc[0][i][r] = 0.f; acc[1][i][r] = 0.f; }
      }
      STEP(0, 1, true);
#pragma nounroll
      for (int c = 0; c < 7; ++c) {
        dl = (c == 3) ? 0u - 3u * SLOT : (unsigned)SLOT;
        STEP(1, 2, true);
        if (c == 2) WAITV(0);
        STEP(0, 3, false);
        LBAR();
        STEP(1, 0, false); STEP(0, 1, true);
      }
      STAMP(4);
      STEP(1, 2, true);
#pragma unroll
      for (int i = 0; i < 3; ++i) lds_st<float>(L0 + L_CS + (wj * 3 + i) * 256 + lane * 4, cs[i][0]);
      STEP(0, 3, false);
      LBAR();
      float co[3]; unsigned ms[2];
#pragma unroll
      for (int i = 0; i < 3; ++i) co[i] = lds_ld<float>(L0 + L_CS + ((wj ^ 1) * 3 + i) * 256 + lane * 4);
#pragma unroll
      for (int t = 0; t < 2; ++t) ms[t] = lds_ld<unsigned>(L0 + L_ST + (64 * tp + 32 * t + l31) * 4);
      SB();
      M1(1, 0, 0); M1(1, 1, 0); M1(1, 0, 1); M1(1, 1, 1); M1(1, 0, 2); M1(1, 1, 2); SB();
      float k0[2], k1[2], s2[2];
      u32x4 bm[2];
#pragma unroll
      for (int t = 0; t < 2; ++t) {
        const unsigned mn = (ms[t] & 0xffffu) ^ 0x8000u;
        bm[t] = u32x4{mn | (mn << 16), mn, 0u, 0u};
        const float sd = bhi(ms[t]), rs = __builtin_amdgcn_rcpf(sd);
        k0[t] = rs * -2.3022081983f; k1[t] = k0[t] * rs * rs * 0.044715f; s2[t] = sd * sd;
      }
#pragma unroll
      for (int i = 0; i < 3; ++i) {
        const float c = cs[i][0] + co[i], c1 = c - rbf(c), c2 = c1 - rbf(c1);
        const u32x4 am = {cvtpk(c, c1), cvtpk(c2, 0.f), 0u, 0u};
#pragma unroll
        for (int t = 0; t < 2; ++t) { acc[t][i] = MFMA32(__builtin_bit_cast(bf16x8, am), __builtin_bit_cast(bf16x8, bm[t]), acc[t][i]); PINA(t, i); }
      }
      STAMP(5);
      const unsigned vhs = WS_H + (unsigned)(((4 * rt + 2 * tp) * 86 + 6 * cg + 3 * ch) * 1024 + lane * 16);
#pragma unroll
      for (int i = 0; i < 3; ++i) {
        const unsigned vo = (vhs + i * 1024) | (6 * cg + 3 * ch + i < 86 ? 0u : OOB);
#pragma unroll
        for (int t = 0; t < 2; ++t) {
          f32x2 g[4], zz[4], gu[4];
#pragma unroll
          for (int r = 0; r < 4; ++r) {
            g[r] = f32x2{acc[t][i][2 * r], acc[t][i][2 * r + 1]};
            zz[r] = g[r] * __builtin_elementwise_fma(g[r] * g[r], f32x2{k1[t], k1[t]}, f32x2{k0[t], k0[t]});
          }
#pragma unroll
          for (int r = 0; r < 4; ++r) {
            zz[r] = f32x2{__builtin_amdgcn_exp2f(zz[r][0]), __builtin_amdgcn_exp2f(zz[r][1])};
            gu[r] = g[r] * f32x2{acc[t][i][8 + 2 * r], acc[t][i][9 + 2 * r]};
          }
          u32x4 hq;
#pragma unroll
          for (int r = 0; r < 4; ++r) {
            const f32x2 dd = __builtin_elementwise_fma(zz[r], f32x2{s2[t], s2[t]}, f32x2{s2[t], s2[t]});
            const f32x2 hv = gu[r] * f32x2{__builtin_amdgcn_rcpf(dd[0]), __builtin_amdgcn_rcpf(dd[1])};
            hq[r] = cvtpk(hv[0], hv[1]);
          }
          st16_dev(rW, vo + t * (86 * 1024), hq);
        }
      }
    }
    if (lane == 0) lds_inc(L0 + L_SY + 24);
    STAMP(6);
    const unsigned vd = VD();
    PARK();
    lds_wait_ge(L0 + L_SY + 64 + wj * 4, 1u);
#pragma unroll
    for (int j = 0; j < 4; ++j) WDU(14 + j, L_B1 + wj * 8192 + j * 2048);
#pragma unroll
    for (int j = 0; j < 4; ++j) WDU(18 + j, L_B2 + wj * 10240 + j * 2048);
    xo = ld16(rX, VXO()); st2 = ld8_dev(rW, VS2());
    WAITV(18);
    if (lane == 0 && lds_inc(L0 + L_SY + 16) == 3u && cg < 15) { u32x2 e = {elo, ehi}; st8_dev(rW, WS_FH + (unsigned)((rt * 16 + cg) * 128), e); }
    STAMP(8);
    {
      const int gf = ks0 / 6, gl = (ks0 + 20 + (wj < 2)) / 6, gi = min(gf + l7, gl);
      const unsigned vfh = WS_FH + (unsigned)(((r64 >> 1) * 16 + gi) * 128);
      u32x2 fh = ld8_dev(rW, vfh);
      asm volatile("s_waitcnt vmcnt(0)" : "+v"(fh)::"memory");
      spin_flags(rW, vfh, fh, elo, ehi);
    }
    LDS_SET(L0 + L_SY + 80 + wj * 4, 1u);
  }
  STAMP(10);
  {
    u32x4 hf[22];
    const unsigned vh = WS_H + (unsigned)(((2 * r64 + (w >> 2)) * 86 + ks0) * 1024 + lane * 16);
#pragma unroll
    for (int j = 0; j < 22; ++j) hf[j] = ld16_dev(rW, vh, min(PH(j), klast) * 1024);
    const unsigned vz = WS_ZT + (unsigned)((64 * ct + 8 * w + l3) * 4096 + r64 * 128 + l7 * 16);
    u32x4 zr = ld16_dev(rW, vz, 0);
    STAMP(28);
    lds_wait_ge(L0 + L_SY + wj * 4, 1u);
    const unsigned a1 = AD() + L_B1 + wj * 8192, a2 = AD() + L_B2 + wj * 10240;
    f32x16 fo[2];
#pragma unroll
    for (int r = 0; r < 16; ++r) { fo[0][r] = 0.f; fo[1][r] = 0.f; }
#define G2(j) do { if ((j) + 2 >= 8 && (j) + 2 < 22) { wpk[((j) + 2) & 7][0] = WDF(UA((j) + 2), 0); wpk[((j) + 2) & 7][1] = WDF(UA((j) + 2), 1); } \
      asm volatile("s_waitcnt vmcnt(%1)" : "+v"(hf[j]) : "i"(22 - (j)) : "memory");                                    \
      fo[0] = MFMA32(wpk[(j) & 7][0], __builtin_bit_cast(bf16x8, hf[j]), fo[0]);                                      \
      fo[1] = MFMA32(wpk[(j) & 7][1], __builtin_bit_cast(bf16x8, hf[j]), fo[1]); PIN(fo[0]); PIN(fo[1]); SB(); } while (0)
    G2(0); STAMP(29); G2(1); G2(2); G2(3); G2(4); G2(5); G2(6); G2(7); G2(8); G2(9); G2(10);
    G2(11); G2(12); G2(13); G2(14); G2(15); G2(16); G2(17); G2(18); G2(19); G2(20); G2(21);
    STAMP(11);
    unsigned pb;
    if (w < 4) { LDS_SET(L0 + L_SY + 96 + wj * 4, 1u); pb = L0 + L_P0 + wj * 8192; }
    else { lds_spin_ge(L0 + L_SY + 96 + wj * 4, 1u); pb = L0 + L_B2 + wj * 10240; }
    lds_spin_ge(L0 + L_SY + 20, 4u);
#pragma unroll
    for (int dt = 0; dt < 2; ++dt)
#pragma unroll
      for (int i = 0; i < 4; ++i)
        lds_st<f32x4>(pb + l31 * 256 + (((8 * dt + 2 * i + hh) ^ l7) << 4), f32x4{fo[dt][4 * i], fo[dt][4 * i + 1], fo[dt][4 * i + 2], fo[dt][4 * i + 3]});
    asm volatile("s_waitcnt vmcnt(0)" : "+v"(zr), "+v"(xo), "+v"(st2)::"memory");
    for (unsigned n = 0; __any(max(max(zr[0], zr[1]), max(zr[2], zr[3])) == POISON) && n < SPIN_MAX; ++n) {
      zr = ld16_dev(rW, vz, 0);
      asm volatile("s_waitcnt vmcnt(0)" : "+v"(zr)::"memory");
    }
    STAMP(30);
    const int r = 8 * w + l3;
    lds_st<u32x4>(L0 + L_ZT + r * 128 + ((l7 ^ fsw(r)) << 4), zr);
  }
  st2 = spin_tag(rW, VS2(), st2, elo);
  LBAR();
  STAMP(12);
  {
    u32x2 zt[2];
    const int o = 4 * (G >> 1) + pp;
#pragma unroll
    for (int j = 0; j < 2; ++j) { const int r = 8 * o + 4 * j + qq;
      zt[j] = __builtin_bit_cast(u32x2, trd(L0 + L_ZT + r * 128 + ((w ^ fsw(r)) << 4) + 8 * (G & 1))); }
    const unsigned pb = L0 + (w < 4 ? L_P0 : L_B2) + (8 * wj + l3) * 256, ps = w < 4 ? 8192 : 10240;
    f32x4 p0 = {0.f, 0.f, 0.f, 0.f}, p1 = p0;
#pragma unroll
    for (int k = 0; k < 4; ++k) {
      p0 += lds_ld<f32x4>(pb + k * ps + (((2 * l7) ^ l3) << 4));
      p1 += lds_ld<f32x4>(pb + k * ps + (((2 * l7 + 1) ^ l3) << 4));
    }
    const int src = 16 * (((l7 >> 2) << 1) | (l3 >> 2)) + 4 * (l7 & 3) + (l3 & 3);
    u32x4 zq;
    zq[0] = (unsigned)__builtin_amdgcn_ds_bpermute(src * 4, (int)zt[0].x); zq[1] = (unsigned)__builtin_amdgcn_ds_bpermute(src * 4, (int)zt[0].y);
    zq[2] = (unsigned)__builtin_amdgcn_ds_bpermute(src * 4, (int)zt[1].x); zq[3] = (unsigned)__builtin_amdgcn_ds_bpermute(src * 4, (int)zt[1].y);
    const float mean = blo(st2.x), rs = __builtin_amdgcn_rcpf(bhi(st2.x));
    float o8[8];
#pragma unroll
    for (int i = 0; i < 8; ++i) {
      const float xv = (i & 1) ? bhi(xo[i >> 1]) : blo(xo[i >> 1]), zv = (i & 1) ? bhi(zq[i >> 1]) : blo(zq[i >> 1]);
      const float F = i < 4 ? p0[i & 3] : p1[i & 3];
      const float gs = F * __builtin_amdgcn_rcpf(1.0f + __builtin_amdgcn_exp2f(F * -1.4426950408889634f));
      o8[i] = __builtin_fmaf((xv - mean) * rs * zv, gs, xv);
    }
    const u32x4 ov = {cvtpk(o8[0], o8[1]), cvtpk(o8[2], o8[3]), cvtpk(o8[4], o8[5]), cvtpk(o8[6], o8[7])};
    *(GAS u32x4*)((GAS char*)out + VXO()) = ov;
  }
}

extern "C" void kernel_launch(void* const* d_in, const int* in_sizes, int n_in, void* d_out, int out_size, void* d_ws, size_t ws_size,
                              hipStream_t stream) {
  (void)in_sizes; (void)n_in; (void)out_size;
  (void)hipFuncSetAttribute((const void*)h3_geglu_fused, hipFuncAttributeMaxDynamicSharedMemorySize, LDS_BYTES);
  h3_geglu_fused<<<256, 512, LDS_BYTES, stream>>>((const u16*)d_in[0], (const u16*)d_in[1], (const u16*)d_in[2], (const u16*)d_in[3],
                                                  (const u16*)d_in[4], (const u16*)d_in[5], (const u16*)d_in[7], (const u16*)d_in[8],
                                                  (const u16*)d_in[9], (u16*)d_out, (char*)d_ws + WS_BASE(ws_size));
}
```

```cpp
#define ROT1 ((rt + cg) & 7)
#include <hip/hip_runtime.h>
#include <hip/hip_bf16.h>

typedef unsigned short u16;
typedef unsigned long long u64;
typedef __attribute__((ext_vector_type(8))) short bf16x8;
typedef __attribute__((ext_vector_type(4))) short s16x4;
typedef __attribute__((ext_vector_type(2))) __bf16 bf16x2_t;
typedef __attribute__((ext_vector_type(2))) float f32x2;
typedef __attribute__((ext_vector_type(4))) float f32x4;
typedef __attribute__((ext_vector_type(16))) float f32x16;
typedef __attribute__((ext_vector_type(4))) unsigned u32x4;
typedef __attribute__((ext_vector_type(2))) unsigned u32x2;
#define LAS __attribute__((address_space(3)))
#define GAS __attribute__((address_space(1)))
typedef __amdgpu_buffer_rsrc_t rsrc_t;

constexpr int S_ = 2048, D_ = 512, DF = 1376;
constexpr unsigned WS_GEN = 0, WS_FA = 4096, WS_FH = WS_FA + 256 * 128, WS_FZ = WS_FH + 256 * 128;
constexpr unsigned WS_ST = WS_FZ + 256 * 128;
constexpr unsigned WS_KVT = 1u << 20;
constexpr unsigned WS_ZT = WS_KVT + (2u << 20);
constexpr unsigned WS_H = WS_ZT + (2u << 20);
constexpr unsigned WS_BYTES = WS_H + 64 * 86 * 1024;
constexpr int SLOT = 40960;
constexpr int L_CV = 3 * SLOT;
constexpr int L_KA = L_CV, L_VA = L_KA + 9216;
constexpr int L_B1 = 0, L_B2 = SLOT, L_B3 = 2 * SLOT;
constexpr int L_CS = 32768;
constexpr int L_ST = L_CS + 3072;
constexpr int L_SY = L_ST + 512;
constexpr int L_A = L_VA + 8192;
constexpr int L_P0 = L_CV + 8192;
constexpr int L_U0 = 36864, L_U1 = L_CV + 33280, L_ZT = L_CV;
constexpr int LDS_BYTES = 163840;
constexpr unsigned OOB = 0x80000000u;
constexpr unsigned POISON = 0xffffffffu;
constexpr unsigned SPIN_MAX = 1u << 18;

#define LPRIO 2
#define XSLEEP 4
#define ROT2 (((xcd * 4 + (idx >> 3)) * 11) >> 4)
#define HWIN 12
#define STEPMODE 1
#define NOSTATS 0
#define WS_BASE(sz) 0
#define ZSLEEP 4
#define ABL_PLAIN 0
#define MFMA32(a, b, c) __builtin_amdgcn_mfma_f32_32x32x16_bf16(a, b, c, 0, 0, 0)
#define MFMA16(a, b, c) __builtin_amdgcn_mfma_f32_16x16x32_bf16(a, b, c, 0, 0, 0)
#define WAIT_BAR(N) asm volatile("s_waitcnt vmcnt(" #N ") lgkmcnt(0)\n\ts_barrier" ::: "memory")
#define LBAR() asm volatile("s_waitcnt lgkmcnt(0)\n\ts_barrier" ::: "memory")
#define WAITV(N) asm volatile("s_waitcnt vmcnt(" #N ")" ::: "memory")
#define SB() __builtin_amdgcn_sched_barrier(0)

__device__ __forceinline__ rsrc_t mk_rsrc(const void* p, unsigned bytes) {
  return __builtin_amdgcn_make_buffer_rsrc((void*)p, (short)0, (int)bytes, 0x00020000);
}
__device__ __forceinline__ void dma16(rsrc_t rs, unsigned voff, unsigned soff, unsigned lds) {
  asm volatile("s_mov_b32 m0, %0\n\ts_nop 0\n\tbuffer_load_dwordx4 %1, %2, %3 offen lds" ::"s"(lds), "v"(voff), "s"(rs), "s"(soff) : "memory");
}
__device__ __forceinline__ u32x4 ld16(rsrc_t rs, unsigned voff) {
  u32x4 r; asm volatile("buffer_load_dwordx4 %0, %1, %2, 0 offen" : "=v"(r) : "v"(voff), "s"(rs) : "memory"); return r;
}
__device__ __forceinline__ u32x4 ld16_dev(rsrc_t rs, unsigned voff, unsigned soff) {
  u32x4 r; asm volatile("buffer_load_dwordx4 %0, %1, %2, %3 offen sc1" : "=v"(r) : "v"(voff), "s"(rs), "s"(soff) : "memory"); return r;
}
__device__ __forceinline__ u32x2 ld8(rsrc_t rs, unsigned voff) {
  u32x2 r; asm volatile("buffer_load_dwordx2 %0, %1, %2, 0 offen" : "=v"(r) : "v"(voff), "s"(rs) : "memory"); return r;
}
__device__ __forceinline__ u32x2 ld8_dev(rsrc_t rs, unsigned voff) {
  u32x2 r; asm volatile("buffer_load_dwordx2 %0, %1, %2, 0 offen sc1" : "=v"(r) : "v"(voff), "s"(rs) : "memory"); return r;
}
#define PIN(x) asm volatile("" : "+v"(x))
__device__ __forceinline__ void st16_dev(rsrc_t rs, unsigned voff, u32x4 v) { __builtin_amdgcn_raw_buffer_store_b128(v, rs, voff, 0, 16); }
__device__ __forceinline__ void st8_dev(rsrc_t rs, unsigned voff, u32x2 v) { __builtin_amdgcn_raw_buffer_store_b64(v, rs, voff, 0, 16); }

__device__ __forceinline__ unsigned cvtpk(float lo, float hi) {
  bf16x2_t r = {(__bf16)lo, (__bf16)hi};
  return __builtin_bit_cast(unsigned, r);
}
__device__ __forceinline__ float blo(unsigned w) { return __uint_as_float(w << 16); }
__device__ __forceinline__ float bhi(unsigned w) { return __uint_as_float(w & 0xffff0000u); }
__device__ __forceinline__ float rbf(float f) { return blo(cvtpk(f, f)); }
__device__ __forceinline__ s16x4 trd(unsigned a) { return __builtin_amdgcn_ds_read_tr16_b64_v4i16((LAS s16x4*)(size_t)a); }
__device__ __forceinline__ bf16x8 cat(s16x4 a, s16x4 b) { return __builtin_shufflevector(a, b, 0, 1, 2, 3, 4, 5, 6, 7); }
template <class T> __device__ __forceinline__ T lds_ld(unsigned a) { return *(const LAS T*)(size_t)a; }
template <class T> __device__ __forceinline__ void lds_st(unsigned a, T v) { *(LAS T*)(size_t)a = v; }
template <int CTRL> __device__ __forceinline__ float dpp_add(float x) {
  return x + __uint_as_float(__builtin_amdgcn_update_dpp(0u, __float_as_uint(x), CTRL, 0xf, 0xf, true));
}
__device__ __forceinline__ float swap_add(float x, bool half) {
  if (half) { auto r = __builtin_amdgcn_permlane32_swap(__float_as_uint(x), __float_as_uint(x), false, false); return __uint_as_float(r[0]) + __uint_as_float(r[1]); }
  auto r = __builtin_amdgcn_permlane16_swap(__float_as_uint(x), __float_as_uint(x), false, false); return __uint_as_float(r[0]) + __uint_as_float(r[1]);
}
__device__ __forceinline__ void wave_sum4(float& a, float& b, float& c, float& d) {
  a = dpp_add<0x128>(a); b = dpp_add<0x128>(b); c = dpp_add<0x128>(c); d = dpp_add<0x128>(d);
  a = dpp_add<0x124>(a); b = dpp_add<0x124>(b); c = dpp_add<0x124>(c); d = dpp_add<0x124>(d);
  a = dpp_add<0x122>(a); b = dpp_add<0x122>(b); c = dpp_add<0x122>(c); d = dpp_add<0x122>(d);
  a = dpp_add<0x121>(a); b = dpp_add<0x121>(b); c = dpp_add<0x121>(c); d = dpp_add<0x121>(d);
  a = swap_add(a, false); b = swap_add(b, false); c = swap_add(c, false); d = swap_add(d, false);
  a = swap_add(a, true); b = swap_add(b, true); c = swap_add(c, true); d = swap_add(d, true);
}
__device__ __forceinline__ void lds_wait_ge(unsigned a, unsigned v) {
  for (unsigned n = 0; *(volatile LAS unsigned*)(size_t)a < v && n < SPIN_MAX * 16; ++n) __builtin_amdgcn_s_sleep(1);
  asm volatile("" ::: "memory");
}
__device__ __forceinline__ void lds_spin_ge(unsigned a, unsigned v) {
  for (unsigned n = 0; *(volatile LAS unsigned*)(size_t)a < v && n < SPIN_MAX * 64; ++n) {}
  asm volatile("" ::: "memory");
}
__device__ __forceinline__ unsigned lds_inc(unsigned a) {
  return __hip_atomic_fetch_add((LAS unsigned*)(size_t)a, 1u, __ATOMIC_RELAXED, __HIP_MEMORY_SCOPE_WORKGROUP);
}
__device__ __forceinline__ int fsw(int r) { return ((r >> 1) & 1) | (((r >> 3) & 3) << 1); }
__device__ __forceinline__ void spin_flags(rsrc_t rs, unsigned voff, u32x2 f, unsigned lo, unsigned hi) {
  for (unsigned n = 0; !__all(f.x == lo && f.y == hi) && n < SPIN_MAX; ++n) {
    __builtin_amdgcn_s_sleep(1);
    f = ld8_dev(rs, voff); WAITV(0); PIN(f);
  }
}
__device__ __forceinline__ u32x2 spin_tag(rsrc_t rs, unsigned voff, u32x2 f, unsigned tag) {
  for (unsigned n = 0; !__all(f.y == tag) && n < SPIN_MAX; ++n) {
    __builtin_amdgcn_s_sleep(1);
    f = ld8_dev(rs, voff); WAITV(0); PIN(f);
  }
  return f;
}
#define STAMP(i)

__global__ __launch_bounds__(512, 2) void h3_geglu_fused(const u16* __restrict__ x, const u16* __restrict__ kin, const u16* __restrict__ vin,
                                                         const u16* __restrict__ shk, const u16* __restrict__ ssk, const u16* __restrict__ ssd,
                                                         const u16* __restrict__ wg, const u16* __restrict__ wu, const u16* __restrict__ wd,
                                                         u16* __restrict__ out, char* __restrict__ ws) {
  extern __shared__ __attribute__((aligned(16))) char smem[];
  const unsigned L0 = (unsigned)(size_t)(LAS char*)smem;
  const int tid = threadIdx.x, lane = tid & 63;
  const int w = __builtin_amdgcn_readfirstlane(tid >> 6), wj = w & 3;
  STAMP(0);
  const int b = blockIdx.x, xcd = b & 7, idx = b >> 3, rh = xcd & 1, q4 = xcd >> 1;
  const int pan = 4 * rh + q4, aa = 32 * pan + idx;
  const int l3 = lane >> 3, l7 = lane & 7, G = lane >> 4, qq = (lane >> 2) & 3, pp = lane & 3, hh = lane >> 5, l31 = lane & 31;
  const rsrc_t rX = mk_rsrc(x, S_ * D_ * 2), rW = mk_rsrc(ws, WS_BYTES), rD = mk_rsrc(wd, DF * D_ * 2);
  const int cg = idx < 24 ? ((q4 * 7 + 1) >> 1) + (idx >> 3) : idx < 28 ? (q4 < 2 ? 3 : 10) : 14 + ((idx >> 1) & 1);
  const int rt = 8 * rh + (idx < 24 ? (idx & 7) : idx < 28 ? (q4 & 1) * 4 + (idx & 3) : 2 * q4 + (idx & 1));
  const int r64 = 4 * pan + (idx >> 3), ct = idx & 7;
  const int ks0 = 22 * wj - (wj == 3 ? 1 : 0);
  u32x4 xo; u32x2 st2;
  unsigned elo, ehi;
#define VD() (unsigned)((16 * ks0 + l3) * 1024 + ct * 128 + ((l7 ^ (((l3 >> 1) & 1) << 2)) << 4))
  const int rot2 = ROT2;
#define PH(j) ((j) + rot2 >= 22 ? (j) + rot2 - 22 : (j) + rot2)
  const int klast = wj < 2 ? 21 : 20;
#define WDU(j, lds) do { const unsigned v_ = vd | (PH(j) > klast ? OOB : 0u);                                       \
    dma16(rD, v_, PH(j) * 16384, L0 + (lds)); dma16(rD, v_, PH(j) * 16384 + 8192, L0 + (lds) + 1024); } while (0)
#define VXO() (unsigned)((64 * r64 + 8 * w + l3) * 1024 + ct * 128 + l7 * 16)
#define VS2() (WS_ST + (unsigned)((64 * r64 + 8 * w + l3) * 8))

  bf16x8 wpk[8][2];
#define AD() (L0 + (8 * hh + qq) * 128 + ((4 * ((qq >> 1) & 1) + 2 * (G & 1) + (pp >> 1)) << 4) + 8 * (pp & 1))
#define WDF(base, dt) cat(trd(((base) ^ ((dt) * 64))), trd(((base) ^ ((dt) * 64)) + 512))
#define UA(j) ((j) < 4 ? a1 + (j) * 2048 : (j) < 9 ? a2 + ((j) - 4) * 2048 : (j) < 14 ? a2 + SLOT + ((j) - 9) * 2048 : (j) < 18 ? a1 + ((j) - 14) * 2048 : a2 + ((j) - 18) * 2048)
#define PARK() do { const unsigned a1 = AD() + L_B1 + wj * 8192, a2 = AD() + L_B2 + wj * 10240;                         \
    _Pragma("unroll") for (int j = 0; j < 8; ++j) { wpk[j][0] = WDF(UA(j), 0); wpk[j][1] = WDF(UA(j), 1); }              \
    _Pragma("unroll") for (int j = 0; j < 8; ++j) { PIN(wpk[j][0]); PIN(wpk[j][1]); } } while (0)
#define LDS_SET(a, v) do { asm volatile("" ::: "memory"); *(volatile LAS unsigned*)(size_t)(a) = (v); } while (0)

  if (w >= 4) {
    __builtin_amdgcn_s_setprio(2);
    u32x2 gen = ld8_dev(rW, WS_GEN);
    const unsigned vx = (cg < 15) ? (unsigned)((128 * rt + 8 * wj + l3) * 1024 + ((l7 ^ ((4 * wj + (l3 >> 1)) & 7)) << 4)) : OOB;
    unsigned vg[3], vu[3];
#pragma unroll
    for (int r = 0; r < 3; ++r) {
      const unsigned P = 64 * r + lane, k = (P * 43691u) >> 19, col = 96 * cg + 8 * (P - 12 * k);
      vg[r] = (col < DF) ? (16 * wj + k) * (DF * 2) + col * 2 : OOB; vu[r] = vg[r] ^ 32u;
    }
    const rsrc_t rG = mk_rsrc(wg, D_ * DF * 2), rU = mk_rsrc(wu, D_ * DF * 2);
    const int rot1 = ROT1;
    unsigned dpv;
    asm volatile("buffer_load_dword %0, %1, %2, 0 offen" : "=v"(dpv) : "v"(aa * 4), "s"(mk_rsrc(ssd, D_ * 2)) : "memory");
#pragma unroll
    for (int i = 0; i < 2; ++i) st16_dev(rW, WS_ZT + (unsigned)(2 * aa * 4096 + (2 * wj + i) * 1024 + lane * 16), u32x4{POISON, POISON, POISON, POISON});
    if (tid == 256) lds_st<unsigned>(L0 + L_A, 0u);
    LBAR();
#pragma nounroll
    for (int c = 0; c < 8; ++c) {
      const int cc = (c + rot1) & 7;
      const unsigned sx = cc * 128, sw = cc * (64 * DF * 2), lx = L0 + (c & 3) * SLOT + wj * 1024, lw = L0 + (c & 3) * SLOT + 16384 + wj * 3072;
      dma16(rX, vx, sx, lx);                 dma16(rG, vg[0], sw, lw);        dma16(rU, vu[0], sw, lw + 12288);
      dma16(rX, vx, sx + 32768, lx + 4096);  dma16(rG, vg[1], sw, lw + 1024); dma16(rU, vu[1], sw, lw + 13312);
      if (c >= 2) asm volatile("s_waitcnt vmcnt(16)\n\ts_barrier" ::: "memory");
      if (c == 2) STAMP(16);
      if (c == 3) STAMP(18);
      if (c == 7) STAMP(23);
      if (c == 5 && tid == 256) {
        PIN(gen); const unsigned lo = gen.x + 1u;
        st8_dev(rW, WS_FA + (unsigned)(aa * 128), u32x2{lo, gen.y + (lo == 0u ? 1u : 0u)});
      }
      dma16(rX, vx, sx + 65536, lx + 8192);  dma16(rG, vg[2], sw, lw + 2048); dma16(rU, vu[2], sw, lw + 14336);
      dma16(rX, vx, sx + 98304, lx + 12288);
    }
    PIN(gen); PIN(dpv);
    elo = __builtin_amdgcn_readfirstlane(gen.x) + 1u;
    ehi = __builtin_amdgcn_readfirstlane(gen.y) + (elo == 0u ? 1u : 0u);
    const unsigned dpair = __builtin_amdgcn_readfirstlane(dpv);
    const int ce = tid & 255;
    const unsigned vd = VD();
    const unsigned vfa = WS_FA + (unsigned)((32 * (hh ? ct : pan) + l31) * 128), vst = WS_ST + (unsigned)((128 * rt + 64 * (wj & 1) + lane) * 8);
    const u32x4 zero4 = {0u, 0u, 0u, 0u};
    u32x2 stg = ld8_dev(rW, vst), fa = ld8_dev(rW, vfa);
#pragma unroll
    for (int j = 0; j < 4; ++j) WDU(j, L_B1 + wj * 8192 + j * 2048);
    if (ce < 7) lds_st<u32x4>(L0 + L_SY + ce * 16, zero4);
    WAIT_BAR(20);
#pragma unroll
    for (int j = 0; j < 5; ++j) WDU(4 + j, L_B2 + wj * 10240 + j * 2048);
    asm volatile("s_waitcnt vmcnt(18)\n\ts_barrier" : "+v"(stg), "+v"(fa)::"memory");
    STAMP(4);
    stg = spin_tag(rW, vst, stg, elo);
    if (wj < 2) lds_st<unsigned>(L0 + L_ST + (64 * wj + lane) * 4, stg.x);
    spin_flags(rW, vfa, fa, elo, ehi);
    u32x4 hc[2], hp[2], ur[2];
    {
      const rsrc_t rHk = mk_rsrc(ssk, D_ * S_ * 2);
#pragma unroll
      for (int c = 0; c < 2; ++c) {
        hc[c] = ld16(rHk, (unsigned)((2 * aa + c) * 4096 + ce * 16));
        hp[c] = ld16(rHk, ce ? (unsigned)((2 * aa + c) * 4096 + ce * 16 - 16) : OOB);
        ur[c] = ld16_dev(rW, WS_KVT + (unsigned)(2 * aa * 4096 + ce * 16), c * 4096);
      }
    }
#pragma unroll
    for (int j = 0; j < 2; ++j) WDU(9 + j, L_B3 + wj * 10240 + j * 2048);
    WAIT_BAR(10);
    STAMP(5);
#pragma unroll
    for (int j = 2; j < 5; ++j) WDU(9 + j, L_B3 + wj * 10240 + j * 2048);
    {
      asm volatile("s_waitcnt vmcnt(10)" : "+v"(hc[0]), "+v"(hc[1]), "+v"(hp[0]), "+v"(hp[1]), "+v"(ur[0]), "+v"(ur[1])::"memory");
      auto put = [&](unsigned hb, u32x4 pv, u32x4 cu, int blk) {
        const unsigned a0 = __builtin_amdgcn_alignbit(pv[1], pv[0], 16), a1 = __builtin_amdgcn_alignbit(pv[2], pv[1], 16),
                       a2 = __builtin_amdgcn_alignbit(pv[3], pv[2], 16), a3 = __builtin_amdgcn_alignbit(cu[0], pv[3], 16),
                       a4 = __builtin_amdgcn_alignbit(cu[1], cu[0], 16);
        lds_st<u32x4>(hb + blk * 16, u32x4{a0, a1, a2, a3});
        lds_st<u32x4>(hb + 4160 + blk * 16, u32x4{pv[1], pv[2], pv[3], cu[0]});
        lds_st<u32x4>(hb + 8320 + blk * 16, u32x4{a1, a2, a3, a4});
        lds_st<u32x4>(hb + 12480 + blk * 16, u32x4{pv[2], pv[3], cu[0], cu[1]});
      };
#pragma unroll
      for (int c = 0; c < 2; ++c) {
        put(L0 + L_CV + c * 16640, hp[c], hc[c], ce + 1);
        if (ce == 0 || ce == 255) put(L0 + L_CV + c * 16640, ce ? hc[c] : zero4, zero4, ce ? 257 : 0);
        lds_st<u32x4>(L0 + (c ? L_U1 : L_U0) + ce * 16, ur[c]);
      }
      asm volatile("s_waitcnt lgkmcnt(0)" ::: "memory");
      if (lane == 0) lds_inc(L0 + L_SY + 24);
    }
    PARK();
    LDS_SET(L0 + L_SY + 64 + wj * 4, 1u);
    xo = ld16(rX, VXO()); st2 = ld8_dev(rW, VS2());
    if (b == 0 && wj == 0) {
      u32x2 f[4];
#pragma unroll
      for (int i = 0; i < 4; ++i) f[i] = ld8_dev(rW, WS_FA + (unsigned)((64 * i + lane) * 128));
      asm volatile("s_waitcnt vmcnt(0)" : "+v"(f[0]), "+v"(f[1]), "+v"(f[2]), "+v"(f[3])::"memory");
#pragma unroll
      for (int i = 0; i < 4; ++i) spin_flags(rW, WS_FA + (unsigned)((64 * i + lane) * 128), f[i], elo, ehi);
      if (lane == 0) { u32x2 e = {elo, ehi}; st8_dev(rW, WS_GEN, e); }
    }
    STAMP(7);
    const int vch = wj >> 1, vrh = wj & 1, c15 = lane & 15;
    f32x4 ya[8];
#pragma unroll
    for (int c = 0; c < 8; ++c) { ya[c] = f32x4{0.f, 0.f, 0.f, 0.f}; PIN(ya[c]); }
    unsigned xo_ = L0 + L_CV + vch * 16640 + (vrh ^ 1) * 4096 + lane * 16, xb = L0 + L_CV + vch * 16640 + vrh * 4096 + lane * 16;
    unsigned zs_ = L0 + L_CV + vch * 16640 + vrh * 4096 + c15 * 32 + G * 8, uq_ = L0 + (vch ? L_U1 : L_U0) + (2044 - 768 - 1024 * vrh - 16 * c15 - 4 * G) * 2;
    unsigned vzs = WS_ZT + (unsigned)((2 * aa + vch) * 4096 + vrh * 2048 + lane * 16);
    PIN(xo_); PIN(xb); PIN(zs_); PIN(uq_); PIN(vzs);
    {
      unsigned cA = L0 + L_CV + vch * 16640 + (c15 & 3) * 4160 + (4 * (c15 >> 2) + 8 * G + 128 * vrh) * 2;
      const unsigned ub = L0 + (vch ? L_U1 : L_U0);
      unsigned cB = ub + (240 - 16 * c15 + 8 * G + 128 * vrh) * 2;
      int lim = ((16 * c15 - 8 * G + 47) >> 5) - 4 * vrh;
      PIN(cA); PIN(cB);
      bf16x8 bfr[2][8]; s16x4 ah[2][4];
#define VLD8(a) (*(const volatile LAS s16x4*)(size_t)(a))
#define CA(set, r, p, i) ah[set][i] = VLD8(cA + (r) * 64 + (((i) & 2) ? 7 - (p) : (p)) * 512 + ((i) & 1) * 8)
#define CB(r, mu) bfr[(r) & 1][mu] = lds_ld<bf16x8>((mu) == 0 ? ((r) >= lim ? L0 + L_SY + 32 : cB + (r) * 64 + 3584) : cB + (r) * 64 + (7 - (mu)) * 512)
#define MF16(c, a, b) asm volatile("v_mfma_f32_16x16x32_bf16 %0, %1, %2, %0" : "+v"(c) : "v"(a), "v"(b))
#define CM(r, p, m) do { if ((m) + (p) < 8) MF16(ya[((p) + (m)) & 7], cat(ah[(p) & 1][0], ah[(p) & 1][1]), bfr[(r) & 1][(m) & 7]);  \
        else MF16(ya[((m) - 1) & 7], cat(ah[(p) & 1][2], ah[(p) & 1][3]), bfr[(r) & 1][((m) + (p)) & 7]); } while (0)
#define CKEEP(r, p) asm volatile("" ::"v"(ah[(p) & 1][0]), "v"(ah[(p) & 1][1]), "v"(ah[(p) & 1][2]), "v"(ah[(p) & 1][3]), "v"(bfr[(r) & 1][0]), "v"(bfr[(r) & 1][1]), \
        "v"(bfr[(r) & 1][2]), "v"(bfr[(r) & 1][3]), "v"(bfr[(r) & 1][4]), "v"(bfr[(r) & 1][5]), "v"(bfr[(r) & 1][6]), "v"(bfr[(r) & 1][7]))
#define CPAIR(r, p) do { _Pragma("unroll") for (int m = 0; m < 9; ++m) { CM(r, p, m);                                               \
          if (m < 4) CA(((p) + 1) & 1, (p) < 3 ? (r) : (r) + 1, ((p) + 1) & 3, m); else if (m < 6) CB((r) + 1, 2 * (p) + m - 4); SB(); } CKEEP(r, p); } while (0)
#define CONV(r) do { CPAIR(r, 0); CPAIR(r, 1); CPAIR(r, 2); CPAIR(r, 3); } while (0)
      lds_wait_ge(L0 + L_SY + 24, 8u); __builtin_amdgcn_s_setprio(2);
      STAMP(24);
#pragma unroll
      for (int i = 0; i < 4; ++i) CA(0, 0, 0, i);
#pragma unroll
      for (int mu = 0; mu < 8; ++mu) CB(0, mu);
#pragma nounroll
      for (int it = 0; it < 2; ++it) {
        CONV(0); CPAIR(1, 0); CPAIR(1, 1); CPAIR(1, 2);
        if (it) {
          asm volatile("s_waitcnt vmcnt(0) lgkmcnt(0)" ::: "memory"); LDS_SET(L0 + L_SY + 48 + wj * 4, 1u); LDS_SET(L0 + L_SY + wj * 4, 1u); }
        CPAIR(1, 3);
        cA += 128; cB += 128; lim -= 2; PIN(cA); PIN(cB);
      }
    }
    asm volatile("s_nop 7" ::: "memory");
    STAMP(27);
    {
      const unsigned mine = L0 + L_SY + 48 + wj * 4, other = L0 + L_SY + 48 + (wj ^ 1) * 4;
      const float dsc = vch ? bhi(dpair) : blo(dpair);
      u32x2 uq[4];
#pragma unroll
      for (int c = 0; c < 4; ++c) uq[c] = lds_ld<u32x2>(uq_ + (768 - 256 * c) * 2);
      lds_spin_ge(other, 1u);
      f32x4 y[4];
      if (vrh) {
        asm volatile("" ::: "memory");
#pragma unroll
        for (int c = 0; c < 4; ++c) { lds_st<f32x4>(xo_ + c * 1024, ya[c]); y[c] = ya[4 + c]; PIN(y[c]); }
      } else {
#pragma unroll
        for (int c = 0; c < 4; ++c) { lds_st<f32x4>(xo_ + c * 1024, ya[4 + c]); y[c] = ya[c]; PIN(y[c]); }
        asm volatile("" ::: "memory");
      }
      asm volatile("s_waitcnt lgkmcnt(0)" ::: "memory");
      *(volatile LAS unsigned*)(size_t)mine = 2u;
#pragma unroll
      for (int c = 0; c < 4; ++c)
#pragma unroll
        for (int e = 0; e < 4; ++e) {
          const unsigned uu = (e & 2) ? uq[c].x : uq[c].y;
          float s = __builtin_fmaf((e & 1) ? blo(uu) : bhi(uu), dsc, y[c][e]); PIN(s); y[c][e] = s;
        }
      lds_spin_ge(other, 2u);
      f32x4 o[4];
#pragma unroll
      for (int c = 0; c < 4; ++c) o[c] = lds_ld<f32x4>(xb + c * 1024);
      SB();
      u32x2 zp[4];
#pragma unroll
      for (int c = 0; c < 4; ++c) {
        float z[4];
#pragma unroll
        for (int e = 0; e < 4; ++e) { z[e] = y[c][e] + o[c][e]; asm("" : "+v"(z[e])); }
        zp[c] = u32x2{min(cvtpk(z[0], z[1]), POISON - 1u), min(cvtpk(z[2], z[3]), POISON - 1u)};
      }
#pragma unroll
      for (int c = 0; c < 4; ++c) lds_st<u32x2>(zs_ + c * 512, zp[c]);
#pragma unroll
      for (int i = 0; i < 2; ++i)
        st16_dev(rW, vzs + i * 1024, lds_ld<u32x4>(xb + i * 1024));
    }
    STAMP(26);
    if (lane == 0) lds_inc(L0 + L_SY + 20);
    STAMP(9);
    lds_spin_ge(L0 + L_SY + 80 + wj * 4, 1u);
    STAMP(31);
  } else {
    __builtin_amdgcn_s_setprio(3);
    u32x2 sh[2]; u32x4 xr[2];
#pragma unroll
    for (int i = 0; i < 2; ++i) xr[i] = ld16(rX, (unsigned)((8 * aa + 2 * wj + i) * 1024 + lane * 16));
    {
      const rsrc_t rK = mk_rsrc(kin, S_ * D_ * 2), rV = mk_rsrc(vin, S_ * D_ * 2);
      const unsigned kb = (unsigned)((64 * idx + l3) * 1024 + pan * 128), fl = (unsigned)(l7 ^ ((l3 >> 1) & 1));
#pragma unroll
      for (int i = 0; i < 2; ++i) {
        const int g = 2 * wj + i;
        dma16(rK, kb + ((fl ^ (((g + 1) & 3) << 1)) << 4), g * 8192, L0 + L_KA + (g + 1) * 1024);
        dma16(rV, kb + ((fl ^ ((g & 3) << 1)) << 4), g * 8192, L0 + L_VA + g * 1024);
      }
      if (wj == 0) dma16(rK, idx == 0 ? OOB : kb - 8192 + (fl << 4), 0, L0 + L_KA);
    }
    u32x2 gen = ld8_dev(rW, WS_GEN);
    {
      const rsrc_t rSh = mk_rsrc(shk, D_ * 4 * 2);
#pragma unroll
      for (int i = 0; i < 2; ++i) sh[i] = ld8(rSh, (unsigned)((64 * pan + 16 * wj + 8 * i + 4 * (G & 1) + pp) * 8));
    }
    asm volatile("s_barrier" ::: "memory");
    STAMP(3);
    const int tp = wj & 1, ch = wj >> 1;
    f32x16 acc[2][3];
    f32x4 cs[3];
    unsigned ax[4], aw[2][3], ak[5];
    unsigned vkv = WS_KVT + (unsigned)((64 * pan + 16 * wj + l3) * 4096 + (1984 - 64 * idx + 8 * l7) * 2);
    unsigned vst0 = lane < 2 ? WS_ST + (unsigned)((8 * aa + 2 * wj + lane) * 8) : OOB;
    int src4 = 4 * (16 * ((((7 - l7) >> 2) << 1) | (l3 >> 2)) + 4 * ((7 - l7) & 3) + (l3 & 3));
    const unsigned so_ = ((lane & 15) == 4 * (G & 1)) ? 0x3f803f80u : 0u;
    bf16x8 sel = __builtin_bit_cast(bf16x8, u32x4{so_, so_, so_, so_});
    {
      const unsigned ax0 = L0 + (64 * tp + l31) * 128 + (((4 * tp + hh) ^ ((l31 >> 1) & 7)) << 4);
      const unsigned aw0 = L0 + 16384 + (G & 1) * 12288 + (8 * hh + qq) * 192 + ((pp & 1) << 4) + 8 * (pp >> 1);
#pragma unroll
      for (int s = 0; s < 4; ++s) { ax[s] = ax0 ^ (s << 5); PIN(ax[s]); }
#pragma unroll
      for (int i = 0; i < 3; ++i) {
        const unsigned a_ = aw0 + (((3 * ch + i) ^ (G & 1)) << 5);
        aw[0][i] = a_ + tp * 6144; aw[1][i] = a_ + 6144 - tp * 6144; PIN(aw[0][i]); PIN(aw[1][i]);
      }
#pragma unroll
      for (int j = 0; j < 5; ++j) { const int rr = 8 * (4 * (G >> 1) + pp) + qq + (j < 3 ? 4 + 4 * j : 4 * (j - 3));
        ak[j] = L0 + (j < 3 ? L_KA : L_VA) + rr * 128 + (((2 * wj) ^ fsw(rr)) << 4) + 8 * (G & 1); PIN(ak[j]); }
      PIN(vkv); PIN(vst0); PIN(src4); PIN(sel);
    }
    {
      asm volatile("s_waitcnt vmcnt(7)" : "+v"(xr[0]), "+v"(xr[1])::"memory");
      STAMP(14);
      const f32x2 x0 = {blo(__builtin_amdgcn_readfirstlane(xr[0][0])), blo(__builtin_amdgcn_readfirstlane(xr[1][0]))};
      f32x2 d[8];
#pragma unroll
      for (int e = 0; e < 4; ++e) { d[2 * e] = f32x2{blo(xr[0][e]), blo(xr[1][e])} - x0; d[2 * e + 1] = f32x2{bhi(xr[0][e]), bhi(xr[1][e])} - x0; }
      const f32x2 sm = ((d[0] + d[1]) + (d[2] + d[3])) + ((d[4] + d[5]) + (d[6] + d[7]));
      const f32x2 sq = ((d[0] * d[0] + d[1] * d[1]) + (d[2] * d[2] + d[3] * d[3])) + ((d[4] * d[4] + d[5] * d[5]) + (d[6] * d[6] + d[7] * d[7]));
      float s0 = sm[0], s1 = sm[1], q0 = sq[0], q1 = sq[1];
      wave_sum4(s0, s1, q0, q1);
      const float m = (lane ? s1 : s0) * (1.0f / D_), var = (lane ? q1 : q0) * (1.0f / D_) - m * m;
      unsigned ms0 = cvtpk(m + (lane ? x0[1] : x0[0]), __builtin_amdgcn_sqrtf(rbf(rbf(__builtin_fmaxf(var, 0.f)) + 1e-5f)));
      asm volatile("s_waitcnt vmcnt(0)" : "+v"(gen), "+v"(sh[0]), "+v"(sh[1]), "+v"(ms0)::"memory");
      STAMP(15);
      if (lane == 0) lds_inc(L0 + L_A);
      elo = __builtin_amdgcn_readfirstlane(gen.x) + 1u;
      ehi = __builtin_amdgcn_readfirstlane(gen.y) + (elo == 0u ? 1u : 0u);
      st8_dev(rW, vst0, u32x2{ms0, elo});
      f32x2 sk[4] = {{blo(sh[0].x), blo(sh[1].x)}, {bhi(sh[0].x), bhi(sh[1].x)}, {blo(sh[0].y), blo(sh[1].y)}, {bhi(sh[0].y), bhi(sh[1].y)}};
      PIN(sk[0]); PIN(sk[1]); PIN(sk[2]); PIN(sk[3]);
      lds_spin_ge(L0 + L_A, 4u);
      STAMP(1);
      u32x2 t[2][5];
#pragma unroll
      for (int j = 0; j < 5; ++j) { t[0][j] = __builtin_bit_cast(u32x2, trd(ak[j])); t[1][j] = __builtin_bit_cast(u32x2, trd(ak[j] ^ 16)); }
      SB();
      f32x2 kk[20], r[8];
#pragma unroll
      for (int j = 0; j < 5; ++j) {
        kk[4 * j] = f32x2{blo(t[0][j].x), blo(t[1][j].x)}; kk[4 * j + 1] = f32x2{bhi(t[0][j].x), bhi(t[1][j].x)};
        kk[4 * j + 2] = f32x2{blo(t[0][j].y), blo(t[1][j].y)}; kk[4 * j + 3] = f32x2{bhi(t[0][j].y), bhi(t[1][j].y)};
      }
#pragma unroll
      for (int e = 0; e < 8; ++e) r[e] = kk[e + 1] * sk[3];
#pragma unroll
      for (int j = 2; j >= 0; --j)
#pragma unroll
        for (int e = 0; e < 8; ++e) r[e] = __builtin_elementwise_fma(kk[e + 4 - j], sk[j], r[e]);
#pragma unroll
      for (int e = 0; e < 8; ++e) r[e] *= kk[12 + e];
      u32x4 po[2];
#pragma unroll
      for (int i = 0; i < 2; ++i)
#pragma unroll
        for (int e = 0; e < 4; ++e) po[i][e] = (unsigned)__builtin_amdgcn_ds_bpermute(src4, (int)cvtpk(r[7 - 2 * e][i], r[6 - 2 * e][i]));
      st16_dev(rW, vkv, po[0]); st16_dev(rW, vkv + 32768, po[1]);
      __builtin_amdgcn_s_setprio(1);
    }
    STAMP(2);
    {
      bf16x8 xf[2][2]; s16x4 wf[2][3][2];
#define PINA(t, i) PIN(acc[t][i])
#define M1(p, t, i) do { acc[t][i] = MFMA32(cat(wf[p][i][0], wf[p][i][1]), xf[p][t], acc[t][i]); PINA(t, i); } while (0)
#define MS(p, i, on) do { if (on) { cs[i] = MFMA16(sel, cat(wf[p][i][0], wf[p][i][1]), cs[i]); PIN(cs[i]); } } while (0)
#define RW(s, i) do { wf[(s) & 1][i][0] = trd(aw[(s) >> 1][i] + ((s) & 1) * 3072); wf[(s) & 1][i][1] = trd(aw[(s) >> 1][i] + ((s) & 1) * 3072 + 768); } while (0)
#define RX(s, t) xf[(s) & 1][t] = lds_ld<bf16x8>(ax[s] + (t) * 4096)
#define KEEP(p) do { asm volatile("" ::"v"(wf[p][0][0]), "v"(wf[p][0][1]), "v"(wf[p][1][0]), "v"(wf[p][1][1]), "v"(wf[p][2][0]), "v"(wf[p][2][1]), "v"(xf[p][0]), "v"(xf[p][1])); } while (0)
#define ADV(a) do { a += dl; PIN(a); } while (0)
#define STEP(p, s, on) do { M1(p, 0, 0); RW(s, 0); SB(); M1(p, 1, 0); RX(s, 0); RX(s, 1); SB(); M1(p, 0, 1); RW(s, 1); SB();  \
        M1(p, 1, 1); RW(s, 2); SB(); M1(p, 0, 2);                                                                             \
        if ((s) == 3) { ADV(ax[0]); ADV(ax[1]); ADV(ax[2]); ADV(ax[3]); ADV(aw[0][0]); } SB(); M1(p, 1, 2);                     \
        if ((s) == 3) { ADV(aw[0][1]); ADV(aw[0][2]); ADV(aw[1][0]); ADV(aw[1][1]); ADV(aw[1][2]); }                            \
        MS(p, 0, on); MS(p, 1, on); MS(p, 2, on); KEEP(p); SB(); } while (0)
      LBAR();
      STAMP(16);
      RW(0, 0); RX(0, 0); RX(0, 1); RW(0, 1); RW(0, 2); SB();
      unsigned dl = SLOT;
#pragma unroll
      for (int i = 0; i < 3; ++i) {
        cs[i] = f32x4{0.f, 0.f, 0.f, 0.f};
#pragma unroll
        for (int r = 0; r < 16; ++r) { acc[0][i][r] = 0.f; acc[1][i][r] = 0.f; }
      }
      STEP(0, 1, true);
#pragma nounroll
      for (int c = 0; c < 7; ++c) {
        dl = (c == 3) ? 0u - 3u * SLOT : (unsigned)SLOT;
        STEP(1, 2, true);
        if (c == 2) WAITV(0);
        STEP(0, 3, false);
        LBAR();
        STEP(1, 0, false); STEP(0, 1, true);
      }
      STAMP(4);
      STEP(1, 2, true);
#pragma unroll
      for (int i = 0; i < 3; ++i) lds_st<float>(L0 + L_CS + (wj * 3 + i) * 256 + lane * 4, cs[i][0]);
      STEP(0, 3, false);
      LBAR();
      float co[3]; unsigned ms[2];
#pragma unroll
      for (int i = 0; i < 3; ++i) co[i] = lds_ld<float>(L0 + L_CS + ((wj ^ 1) * 3 + i) * 256 + lane * 4);
#pragma unroll
      for (int t = 0; t < 2; ++t) ms[t] = lds_ld<unsigned>(L0 + L_ST + (64 * tp + 32 * t + l31) * 4);
      SB();
      M1(1, 0, 0); M1(1, 1, 0); M1(1, 0, 1); M1(1, 1, 1); M1(1, 0, 2); M1(1, 1, 2); SB();
      float k0[2], k1[2], s2[2];
      u32x4 bm[2];
#pragma unroll
      for (int t = 0; t < 2; ++t) {
        const unsigned mn = (ms[t] & 0xffffu) ^ 0x8000u;
        bm[t] = u32x4{mn | (mn << 16), mn, 0u, 0u};
        const float sd = bhi(ms[t]), rs = __builtin_amdgcn_rcpf(sd);
        k0[t] = rs * -2.3022081983f; k1[t] = k0[t] * rs * rs * 0.044715f; s2[t] = sd * sd;
      }
#pragma unroll
      for (int i = 0; i < 3; ++i) {
        const float c = cs[i][0] + co[i], c1 = c - rbf(c), c2 = c1 - rbf(c1);
        const u32x4 am = {cvtpk(c, c1), cvtpk(c2, 0.f), 0u, 0u};
#pragma unroll
        for (int t = 0; t < 2; ++t) { acc[t][i] = MFMA32(__builtin_bit_cast(bf16x8, am), __builtin_bit_cast(bf16x8, bm[t]), acc[t][i]); PINA(t, i); }
      }
      STAMP(5);
      const unsigned vhs = WS_H + (unsigned)(((4 * rt + 2 * tp) * 86 + 6 * cg + 3 * ch) * 1024 + lane * 16);
#pragma unroll
      for (int i = 0; i < 3; ++i) {
        const unsigned vo = (vhs + i * 1024) | (6 * cg + 3 * ch + i < 86 ? 0u : OOB);
#pragma unroll
        for (int t = 0; t < 2; ++t) {
          f32x2 g[4], zz[4], gu[4];
#pragma unroll
          for (int r = 0; r < 4; ++r) {
            g[r] = f32x2{acc[t][i][2 * r], acc[t][i][2 * r + 1]};
            zz[r] = g[r] * __builtin_elementwise_fma(g[r] * g[r], f32x2{k1[t], k1[t]}, f32x2{k0[t], k0[t]});
          }
#pragma unroll
          for (int r = 0; r < 4; ++r) {
            zz[r] = f32x2{__builtin_amdgcn_exp2f(zz[r][0]), __builtin_amdgcn_exp2f(zz[r][1])};
            gu[r] = g[r] * f32x2{acc[t][i][8 + 2 * r], acc[t][i][9 + 2 * r]};
          }
          u32x4 hq;
#pragma unroll
          for (int r = 0; r < 4; ++r) {
            const f32x2 dd = __builtin_elementwise_fma(zz[r], f32x2{s2[t], s2[t]}, f32x2{s2[t], s2[t]});
            const f32x2 hv = gu[r] * f32x2{__builtin_amdgcn_rcpf(dd[0]), __builtin_amdgcn_rcpf(dd[1])};
            hq[r] = cvtpk(hv[0], hv[1]);
          }
          st16_dev(rW, vo + t * (86 * 1024), hq);
        }
      }
    }
    if (lane == 0) lds_inc(L0 + L_SY + 24);
    STAMP(6);
    const unsigned vd = VD();
    PARK();
    WAITV(0);
    if (lane == 0 && lds_inc(L0 + L_SY + 16) == 3u && cg < 15) { u32x2 e = {elo, ehi}; st8_dev(rW, WS_FH + (unsigned)((rt * 16 + cg) * 128), e); }
    STAMP(8);
    lds_wait_ge(L0 + L_SY + 64 + wj * 4, 1u);
#pragma unroll
    for (int j = 0; j < 4; ++j) WDU(14 + j, L_B1 + wj * 8192 + j * 2048);
#pragma unroll
    for (int j = 0; j < 4; ++j) WDU(18 + j, L_B2 + wj * 10240 + j * 2048);
    xo = ld16(rX, VXO()); st2 = ld8_dev(rW, VS2());
    {
      const int gf = ks0 / 6, gl = (ks0 + 20 + (wj < 2)) / 6, gi = min(gf + l7, gl);
      const unsigned vfh = WS_FH + (unsigned)(((r64 >> 1) * 16 + gi) * 128);
      u32x2 fh = ld8_dev(rW, vfh);
      asm volatile("s_waitcnt vmcnt(0)" : "+v"(fh)::"memory");
      spin_flags(rW, vfh, fh, elo, ehi);
    }
    LDS_SET(L0 + L_SY + 80 + wj * 4, 1u);
  }
  STAMP(10);
  {
    u32x4 hf[22];
    const unsigned vh = WS_H + (unsigned)(((2 * r64 + (w >> 2)) * 86 + ks0) * 1024 + lane * 16);
#pragma unroll
    for (int j = 0; j < 22; ++j) hf[j] = ld16_dev(rW, vh, min(PH(j), klast) * 1024);
    const unsigned vz = WS_ZT + (unsigned)((64 * ct + 8 * w + l3) * 4096 + r64 * 128 + l7 * 16);
    u32x4 zr = ld16_dev(rW, vz, 0);
    STAMP(28);
    lds_wait_ge(L0 + L_SY + wj * 4, 1u);
    const unsigned a1 = AD() + L_B1 + wj * 8192, a2 = AD() + L_B2 + wj * 10240;
    f32x16 fo[2];
#pragma unroll
    for (int r = 0; r < 16; ++r) { fo[0][r] = 0.f; fo[1][r] = 0.f; }
#define G2(j) do { if ((j) + 2 >= 8 && (j) + 2 < 22) { wpk[((j) + 2) & 7][0] = WDF(UA((j) + 2), 0); wpk[((j) + 2) & 7][1] = WDF(UA((j) + 2), 1); } \
      asm volatile("s_waitcnt vmcnt(%1)" : "+v"(hf[j]) : "i"(22 - (j)) : "memory");                                    \
      fo[0] = MFMA32(wpk[(j) & 7][0], __builtin_bit_cast(bf16x8, hf[j]), fo[0]);                                      \
      fo[1] = MFMA32(wpk[(j) & 7][1], __builtin_bit_cast(bf16x8, hf[j]), fo[1]); PIN(fo[0]); PIN(fo[1]); SB(); } while (0)
    G2(0); STAMP(29); G2(1); G2(2); G2(3); G2(4); G2(5); G2(6); G2(7); G2(8); G2(9); G2(10);
    G2(11); G2(12); G2(13); G2(14); G2(15); G2(16); G2(17); G2(18); G2(19); G2(20); G2(21);
    STAMP(11);
    unsigned pb;
    if (w < 4) { LDS_SET(L0 + L_SY + 96 + wj * 4, 1u); pb = L0 + L_P0 + wj * 8192; }
    else { lds_spin_ge(L0 + L_SY + 96 + wj * 4, 1u); pb = L0 + L_B2 + wj * 10240; }
    lds_spin_ge(L0 + L_SY + 20, 4u);
#pragma unroll
    for (int dt = 0; dt < 2; ++dt)
#pragma unroll
      for (int i = 0; i < 4; ++i)
        lds_st<f32x4>(pb + l31 * 256 + (((8 * dt + 2 * i + hh) ^ l7) << 4), f32x4{fo[dt][4 * i], fo[dt][4 * i + 1], fo[dt][4 * i + 2], fo[dt][4 * i + 3]});
    asm volatile("s_waitcnt vmcnt(0)" : "+v"(zr), "+v"(xo), "+v"(st2)::"memory");
    for (unsigned n = 0; __any(max(max(zr[0], zr[1]), max(zr[2], zr[3])) == POISON) && n < SPIN_MAX; ++n) {
      zr = ld16_dev(rW, vz, 0);
      asm volatile("s_waitcnt vmcnt(0)" : "+v"(zr)::"memory");
    }
    STAMP(30);
    const int r = 8 * w + l3;
    lds_st<u32x4>(L0 + L_ZT + r * 128 + ((l7 ^ fsw(r)) << 4), zr);
  }
  st2 = spin_tag(rW, VS2(), st2, elo);
  LBAR();
  STAMP(12);
  {
    u32x2 zt[2];
    const int o = 4 * (G >> 1) + pp;
#pragma unroll
    for (int j = 0; j < 2; ++j) { const int r = 8 * o + 4 * j + qq;
      zt[j] = __builtin_bit_cast(u32x2, trd(L0 + L_ZT + r * 128 + ((w ^ fsw(r)) << 4) + 8 * (G & 1))); }
    const unsigned pb = L0 + (w < 4 ? L_P0 : L_B2) + (8 * wj + l3) * 256, ps = w < 4 ? 8192 : 10240;
    f32x4 p0 = {0.f, 0.f, 0.f, 0.f}, p1 = p0;
#pragma unroll
    for (int k = 0; k < 4; ++k) {
      p0 += lds_ld<f32x4>(pb + k * ps + (((2 * l7) ^ l3) << 4));
      p1 += lds_ld<f32x4>(pb + k * ps + (((2 * l7 + 1) ^ l3) << 4));
    }
    const int src = 16 * (((l7 >> 2) << 1) | (l3 >> 2)) + 4 * (l7 & 3) + (l3 & 3);
    u32x4 zq;
    zq[0] = (unsigned)__builtin_amdgcn_ds_bpermute(src * 4, (int)zt[0].x); zq[1] = (unsigned)__builtin_amdgcn_ds_bpermute(src * 4, (int)zt[0].y);
    zq[2] = (unsigned)__builtin_amdgcn_ds_bpermute(src * 4, (int)zt[1].x); zq[3] = (unsigned)__builtin_amdgcn_ds_bpermute(src * 4, (int)zt[1].y);
    const float mean = blo(st2.x), rs = __builtin_amdgcn_rcpf(bhi(st2.x));
    float o8[8];
#pragma unroll
    for (int i = 0; i < 8; ++i) {
      const float xv = (i & 1) ? bhi(xo[i >> 1]) : blo(xo[i >> 1]), zv = (i & 1) ? bhi(zq[i >> 1]) : blo(zq[i >> 1]);
      const float F = i < 4 ? p0[i & 3] : p1[i & 3];
      const float gs = F * __builtin_amdgcn_rcpf(1.0f + __builtin_amdgcn_exp2f(F * -1.4426950408889634f));
      o8[i] = __builtin_fmaf((xv - mean) * rs * zv, gs, xv);
    }
    const u32x4 ov = {cvtpk(o8[0], o8[1]), cvtpk(o8[2], o8[3]), cvtpk(o8[4], o8[5]), cvtpk(o8[6], o8[7])};
    *(GAS u32x4*)((GAS char*)out + VXO()) = ov;
  }
}

extern "C" void kernel_launch(void* const* d_in, const int* in_sizes, int n_in, void* d_out, int out_size, void* d_ws, size_t ws_size,
                              hipStream_t stream) {
  (void)in_sizes; (void)n_in; (void)out_size;
  (void)hipFuncSetAttribute((const void*)h3_geglu_fused, hipFuncAttributeMaxDynamicSharedMemorySize, LDS_BYTES);
  h3_geglu_fused<<<256, 512, LDS_BYTES, stream>>>((const u16*)d_in[0], (const u16*)d_in[1], (const u16*)d_in[2], (const u16*)d_in[3],
                                                  (const u16*)d_in[4], (const u16*)d_in[5], (const u16*)d_in[7], (const u16*)d_in[8],
                                                  (const u16*)d_in[9], (u16*)d_out, (char*)d_ws + WS_BASE(ws_size));
}
```

```cpp
#define ROT1 ((rt + cg) & 7)
#include <hip/hip_runtime.h>
#include <hip/hip_bf16.h>

typedef unsigned short u16;
typedef unsigned long long u64;
typedef __attribute__((ext_vector_type(8))) short bf16x8;
typedef __attribute__((ext_vector_type(4))) short s16x4;
typedef __attribute__((ext_vector_type(2))) __bf16 bf16x2_t;
typedef __attribute__((ext_vector_type(2))) float f32x2;
typedef __attribute__((ext_vector_type(4))) float f32x4;
typedef __attribute__((ext_vector_type(16))) float f32x16;
typedef __attribute__((ext_vector_type(4))) unsigned u32x4;
typedef __attribute__((ext_vector_type(2))) unsigned u32x2;
#define LAS __attribute__((address_space(3)))
#define GAS __attribute__((address_space(1)))
typedef __amdgpu_buffer_rsrc_t rsrc_t;

constexpr int S_ = 2048, D_ = 512, DF = 1376;
constexpr unsigned WS_GEN = 0, WS_FA = 4096, WS_FH = WS_FA + 256 * 128, WS_FZ = WS_FH + 256 * 128;
constexpr unsigned WS_ST = WS_FZ + 256 * 128;
constexpr unsigned WS_KVT = 1u << 20;
constexpr unsigned WS_ZT = WS_KVT + (2u << 20);
constexpr unsigned WS_H = WS_ZT + (2u << 20);
constexpr unsigned WS_BYTES = WS_H + 64 * 86 * 1024;
constexpr int SLOT = 40960;
constexpr int L_CV = 3 * SLOT;
constexpr int L_KA = L_CV, L_VA = L_KA + 9216;
constexpr int L_B1 = 0, L_B2 = SLOT, L_B3 = 2 * SLOT;
constexpr int L_CS = 32768;
constexpr int L_ST = L_CS + 3072;
constexpr int L_SY = L_ST + 512;
constexpr int L_A = L_VA + 8192;
constexpr int L_P0 = L_CV + 8192;
constexpr int L_U0 = 36864, L_U1 = L_CV + 33280, L_ZT = L_CV;
constexpr int LDS_BYTES = 163840;
constexpr unsigned OOB = 0x80000000u;
constexpr unsigned POISON = 0xffffffffu;
constexpr unsigned SPIN_MAX = 1u << 18;

#define LPRIO 2
#define XSLEEP 4
#define ROT2 (((xcd * 4 + (idx >> 3)) * 11) >> 4)
#define HWIN 12
#define STEPMODE 1
#define NOSTATS 0
#define WS_BASE(sz) 0
#define ZSLEEP 4
#define ABL_PLAIN 0
#define MFMA32(a, b, c) __builtin_amdgcn_mfma_f32_32x32x16_bf16(a, b, c, 0, 0, 0)
#define MFMA16(a, b, c) __builtin_amdgcn_mfma_f32_16x16x32_bf16(a, b, c, 0, 0, 0)
#define WAIT_BAR(N) asm volatile("s_waitcnt vmcnt(" #N ") lgkmcnt(0)\n\ts_barrier" ::: "memory")
#define LBAR() asm volatile("s_waitcnt lgkmcnt(0)\n\ts_barrier" ::: "memory")
#define WAITV(N) asm volatile("s_waitcnt vmcnt(" #N ")" ::: "memory")
#define SB() __builtin_amdgcn_sched_barrier(0)

__device__ __forceinline__ rsrc_t mk_rsrc(const void* p, unsigned bytes) {
  return __builtin_amdgcn_make_buffer_rsrc((void*)p, (short)0, (int)bytes, 0x00020000);
}
__device__ __forceinline__ void dma16(rsrc_t rs, unsigned voff, unsigned soff, unsigned lds) {
  asm volatile("s_mov_b32 m0, %0\n\ts_nop 0\n\tbuffer_load_dwordx4 %1, %2, %3 offen lds" ::"s"(lds), "v"(voff), "s"(rs), "s"(soff) : "memory");
}
__device__ __forceinline__ u32x4 ld16(rsrc_t rs, unsigned voff) {
  u32x4 r; asm volatile("buffer_load_dwordx4 %0, %1, %2, 0 offen" : "=v"(r) : "v"(voff), "s"(rs) : "memory"); return r;
}
__device__ __forceinline__ u32x4 ld16_dev(rsrc_t rs, unsigned voff, unsigned soff) {
  u32x4 r; asm volatile("buffer_load_dwordx4 %0, %1, %2, %3 offen sc1" : "=v"(r) : "v"(voff), "s"(rs), "s"(soff) : "memory"); return r;
}
__device__ __forceinline__ u32x2 ld8(rsrc_t rs, unsigned voff) {
  u32x2 r; asm volatile("buffer_load_dwordx2 %0, %1, %2, 0 offen" : "=v"(r) : "v"(voff), "s"(rs) : "memory"); return r;
}
__device__ __forceinline__ u32x2 ld8_dev(rsrc_t rs, unsigned voff) {
  u32x2 r; asm volatile("buffer_load_dwordx2 %0, %1, %2, 0 offen sc1" : "=v"(r) : "v"(voff), "s"(rs) : "memory"); return r;
}
#define PIN(x) asm volatile("" : "+v"(x))
__device__ __forceinline__ void st16_dev(rsrc_t rs, unsigned voff, u32x4 v) { __builtin_amdgcn_raw_buffer_store_b128(v, rs, voff, 0, 16); }
__device__ __forceinline__ void st8_dev(rsrc_t rs, unsigned voff, u32x2 v) { __builtin_amdgcn_raw_buffer_store_b64(v, rs, voff, 0, 16); }

__device__ __forceinline__ unsigned cvtpk(float lo, float hi) {
  bf16x2_t r = {(__bf16)lo, (__bf16)hi};
  return __builtin_bit_cast(unsigned, r);
}
__device__ __forceinline__ float blo(unsigned w) { return __uint_as_float(w << 16); }
__device__ __forceinline__ float bhi(unsigned w) { return __uint_as_float(w & 0xffff0000u); }
__device__ __forceinline__ float rbf(float f) { return blo(cvtpk(f, f)); }
__device__ __forceinline__ s16x4 trd(unsigned a) { return __builtin_amdgcn_ds_read_tr16_b64_v4i16((LAS s16x4*)(size_t)a); }
__device__ __forceinline__ bf16x8 cat(s16x4 a, s16x4 b) { return __builtin_shufflevector(a, b, 0, 1, 2, 3, 4, 5, 6, 7); }
template <class T> __device__ __forceinline__ T lds_ld(unsigned a) { return *(const LAS T*)(size_t)a; }
template <class T> __device__ __forceinline__ void lds_st(unsigned a, T v) { *(LAS T*)(size_t)a = v; }
template <int CTRL> __device__ __forceinline__ float dpp_add(float x) {
  return x + __uint_as_float(__builtin_amdgcn_update_dpp(0u, __float_as_uint(x), CTRL, 0xf, 0xf, true));
}
__device__ __forceinline__ float swap_add(float x, bool half) {
  if (half) { auto r = __builtin_amdgcn_permlane32_swap(__float_as_uint(x), __float_as_uint(x), false, false); return __uint_as_float(r[0]) + __uint_as_float(r[1]); }
  auto r = __builtin_amdgcn_permlane16_swap(__float_as_uint(x), __float_as_uint(x), false, false); return __uint_as_float(r[0]) + __uint_as_float(r[1]);
}
__device__ __forceinline__ void wave_sum4(float& a, float& b, float& c, float& d) {
  a = dpp_add<0x128>(a); b = dpp_add<0x128>(b); c = dpp_add<0x128>(c); d = dpp_add<0x128>(d);
  a = dpp_add<0x124>(a); b = dpp_add<0x124>(b); c = dpp_add<0x124>(c); d = dpp_add<0x124>(d);
  a = dpp_add<0x122>(a); b = dpp_add<0x122>(b); c = dpp_add<0x122>(c); d = dpp_add<0x122>(d);
  a = dpp_add<0x121>(a); b = dpp_add<0x121>(b); c = dpp_add<0x121>(c); d = dpp_add<0x121>(d);
  a = swap_add(a, false); b = swap_add(b, false); c = swap_add(c, false); d = swap_add(d, false);
  a = swap_add(a, true); b = swap_add(b, true); c = swap_add(c, true); d = swap_add(d, true);
}
__device__ __forceinline__ void lds_wait_ge(unsigned a, unsigned v) {
  for (unsigned n = 0; *(volatile LAS unsigned*)(size_t)a < v && n < SPIN_MAX * 16; ++n) __builtin_amdgcn_s_sleep(1);
  asm volatile("" ::: "memory");
}
__device__ __forceinline__ void lds_spin_ge(unsigned a, unsigned v) {
  for (unsigned n = 0; *(volatile LAS unsigned*)(size_t)a < v && n < SPIN_MAX * 64; ++n) {}
  asm volatile("" ::: "memory");
}
__device__ __forceinline__ unsigned lds_inc(unsigned a) {
  return __hip_atomic_fetch_add((LAS unsigned*)(size_t)a, 1u, __ATOMIC_RELAXED, __HIP_MEMORY_SCOPE_WORKGROUP);
}
__device__ __forceinline__ int fsw(int r) { return ((r >> 1) & 1) | (((r >> 3) & 3) << 1); }
__device__ __forceinline__ void spin_flags(rsrc_t rs, unsigned voff, u32x2 f, unsigned lo, unsigned hi) {
  for (unsigned n = 0; !__all(f.x == lo && f.y == hi) && n < SPIN_MAX; ++n) {
    __builtin_amdgcn_s_sleep(1);
    f = ld8_dev(rs, voff); WAITV(0); PIN(f);
  }
}
__device__ __forceinline__ u32x2 spin_tag(rsrc_t rs, unsigned voff, u32x2 f, unsigned tag) {
  for (unsigned n = 0; !__all(f.y == tag) && n < SPIN_MAX; ++n) {
    __builtin_amdgcn_s_sleep(1);
    f = ld8_dev(rs, voff); WAITV(0); PIN(f);
  }
  return f;
}
#define STAMP(i) do { if ((i) == 0) { prb0 = __builtin_amdgcn_s_memrealtime(); prb1 = prb0; } if ((i) == 0) { SB(); prb1 = __builtin_amdgcn_s_memrealtime(); SB(); } } while (0)

__global__ __launch_bounds__(512, 2) void h3_geglu_fused(const u16* __restrict__ x, const u16* __restrict__ kin, const u16* __restrict__ vin,
                                                         const u16* __restrict__ shk, const u16* __restrict__ ssk, const u16* __restrict__ ssd,
                                                         const u16* __restrict__ wg, const u16* __restrict__ wu, const u16* __restrict__ wd,
                                                         u16* __restrict__ out, char* __restrict__ ws) {
  extern __shared__ __attribute__((aligned(16))) char smem[];
  const unsigned L0 = (unsigned)(size_t)(LAS char*)smem;
  const int tid = threadIdx.x, lane = tid & 63;
  const int w = __builtin_amdgcn_readfirstlane(tid >> 6), wj = w & 3;
  u64 prb0, prb1;
  STAMP(0);
  const int b = blockIdx.x, xcd = b & 7, idx = b >> 3, rh = xcd & 1, q4 = xcd >> 1;
  const int pan = 4 * rh + q4, aa = 32 * pan + idx;
  const int l3 = lane >> 3, l7 = lane & 7, G = lane >> 4, qq = (lane >> 2) & 3, pp = lane & 3, hh = lane >> 5, l31 = lane & 31;
  const rsrc_t rX = mk_rsrc(x, S_ * D_ * 2), rW = mk_rsrc(ws, WS_BYTES), rD = mk_rsrc(wd, DF * D_ * 2);
  const int cg = idx < 24 ? ((q4 * 7 + 1) >> 1) + (idx >> 3) : idx < 28 ? (q4 < 2 ? 3 : 10) : 14 + ((idx >> 1) & 1);
  const int rt = 8 * rh + (idx < 24 ? (idx & 7) : idx < 28 ? (q4 & 1) * 4 + (idx & 3) : 2 * q4 + (idx & 1));
  const int r64 = 4 * pan + (idx >> 3), ct = idx & 7;
  const int ks0 = 22 * wj - (wj == 3 ? 1 : 0);
  u32x4 xo; u32x2 st2;
  unsigned elo, ehi;
#define VD() (unsigned)((16 * ks0 + l3) * 1024 + ct * 128 + ((l7 ^ (((l3 >> 1) & 1) << 2)) << 4))
  const int rot2 = ROT2;
#define PH(j) ((j) + rot2 >= 22 ? (j) + rot2 - 22 : (j) + rot2)
  const int klast = wj < 2 ? 21 : 20;
#define WDU(j, lds) do { const unsigned v_ = vd | (PH(j) > klast ? OOB : 0u);                                       \
    dma16(rD, v_, PH(j) * 16384, L0 + (lds)); dma16(rD, v_, PH(j) * 16384 + 8192, L0 + (lds) + 1024); } while (0)
#define VXO() (unsigned)((64 * r64 + 8 * w + l3) * 1024 + ct * 128 + l7 * 16)
#define VS2() (WS_ST + (unsigned)((64 * r64 + 8 * w + l3) * 8))

  bf16x8 wpk[8][2];
#define AD() (L0 + (8 * hh + qq) * 128 + ((4 * ((qq >> 1) & 1) + 2 * (G & 1) + (pp >> 1)) << 4) + 8 * (pp & 1))
#define WDF(base, dt) cat(trd(((base) ^ ((dt) * 64))), trd(((base) ^ ((dt) * 64)) + 512))
#define UA(j) ((j) < 4 ? a1 + (j) * 2048 : (j) < 9 ? a2 + ((j) - 4) * 2048 : (j) < 14 ? a2 + SLOT + ((j) - 9) * 2048 : (j) < 18 ? a1 + ((j) - 14) * 2048 : a2 + ((j) - 18) * 2048)
#define PARK() do { const unsigned a1 = AD() + L_B1 + wj * 8192, a2 = AD() + L_B2 + wj * 10240;                         \
    _Pragma("unroll") for (int j = 0; j < 8; ++j) { wpk[j][0] = WDF(UA(j), 0); wpk[j][1] = WDF(UA(j), 1); }              \
    _Pragma("unroll") for (int j = 0; j < 8; ++j) { PIN(wpk[j][0]); PIN(wpk[j][1]); } } while (0)
#define LDS_SET(a, v) do { asm volatile("" ::: "memory"); *(volatile LAS unsigned*)(size_t)(a) = (v); } while (0)

  if (w >= 4) {
    __builtin_amdgcn_s_setprio(2);
    u32x2 gen = ld8_dev(rW, WS_GEN);
    const unsigned vx = (cg < 15) ? (unsigned)((128 * rt + 8 * wj + l3) * 1024 + ((l7 ^ ((4 * wj + (l3 >> 1)) & 7)) << 4)) : OOB;
    unsigned vg[3], vu[3];
#pragma unroll
    for (int r = 0; r < 3; ++r) {
      const unsigned P = 64 * r + lane, k = (P * 43691u) >> 19, col = 96 * cg + 8 * (P - 12 * k);
      vg[r] = (col < DF) ? (16 * wj + k) * (DF * 2) + col * 2 : OOB; vu[r] = vg[r] ^ 32u;
    }
    const rsrc_t rG = mk_rsrc(wg, D_ * DF * 2), rU = mk_rsrc(wu, D_ * DF * 2);
    const int rot1 = ROT1;
    unsigned dpv;
    asm volatile("buffer_load_dword %0, %1, %2, 0 offen" : "=v"(dpv) : "v"(aa * 4), "s"(mk_rsrc(ssd, D_ * 2)) : "memory");
#pragma unroll
    for (int i = 0; i < 2; ++i) st16_dev(rW, WS_ZT + (unsigned)(2 * aa * 4096 + (2 * wj + i) * 1024 + lane * 16), u32x4{POISON, POISON, POISON, POISON});
    if (tid == 256) lds_st<unsigned>(L0 + L_A, 0u);
    LBAR();
#pragma nounroll
    for (int c = 0; c < 8; ++c) {
      const int cc = (c + rot1) & 7;
      const unsigned sx = cc * 128, sw = cc * (64 * DF * 2), lx = L0 + (c & 3) * SLOT + wj * 1024, lw = L0 + (c & 3) * SLOT + 16384 + wj * 3072;
      dma16(rX, vx, sx, lx);                 dma16(rG, vg[0], sw, lw);        dma16(rU, vu[0], sw, lw + 12288);
      dma16(rX, vx, sx + 32768, lx + 4096);  dma16(rG, vg[1], sw, lw + 1024); dma16(rU, vu[1], sw, lw + 13312);
      if (c >= 2) asm volatile("s_waitcnt vmcnt(16)\n\ts_barrier" ::: "memory");
      if (c == 2) STAMP(16);
      if (c == 3) STAMP(18);
      if (c == 7) STAMP(23);
      if (c == 5 && tid == 256) {
        PIN(gen); const unsigned lo = gen.x + 1u;
        st8_dev(rW, WS_FA + (unsigned)(aa * 128), u32x2{lo, gen.y + (lo == 0u ? 1u : 0u)});
      }
      dma16(rX, vx, sx + 65536, lx + 8192);  dma16(rG, vg[2], sw, lw + 2048); dma16(rU, vu[2], sw, lw + 14336);
      dma16(rX, vx, sx + 98304, lx + 12288);
    }
    PIN(gen); PIN(dpv);
    elo = __builtin_amdgcn_readfirstlane(gen.x) + 1u;
    ehi = __builtin_amdgcn_readfirstlane(gen.y) + (elo == 0u ? 1u : 0u);
    const unsigned dpair = __builtin_amdgcn_readfirstlane(dpv);
    const int ce = tid & 255;
    const unsigned vd = VD();
    const unsigned vfa = WS_FA + (unsigned)((32 * (hh ? ct : pan) + l31) * 128), vst = WS_ST + (unsigned)((128 * rt + 64 * (wj & 1) + lane) * 8);
    const u32x4 zero4 = {0u, 0u, 0u, 0u};
    u32x2 stg = ld8_dev(rW, vst), fa = ld8_dev(rW, vfa);
#pragma unroll
    for (int j = 0; j < 4; ++j) WDU(j, L_B1 + wj * 8192 + j * 2048);
    if (ce < 7) lds_st<u32x4>(L0 + L_SY + ce * 16, zero4);
    WAIT_BAR(20);
#pragma unroll
    for (int j = 0; j < 5; ++j) WDU(4 + j, L_B2 + wj * 10240 + j * 2048);
    asm volatile("s_waitcnt vmcnt(18)\n\ts_barrier" : "+v"(stg), "+v"(fa)::"memory");
    STAMP(4);
    stg = spin_tag(rW, vst, stg, elo);
    if (wj < 2) lds_st<unsigned>(L0 + L_ST + (64 * wj + lane) * 4, stg.x);
    spin_flags(rW, vfa, fa, elo, ehi);
    u32x4 hc[2], hp[2], ur[2];
    {
      const rsrc_t rHk = mk_rsrc(ssk, D_ * S_ * 2);
#pragma unroll
      for (int c = 0; c < 2; ++c) {
        hc[c] = ld16(rHk, (unsigned)((2 * aa + c) * 4096 + ce * 16));
        hp[c] = ld16(rHk, ce ? (unsigned)((2 * aa + c) * 4096 + ce * 16 - 16) : OOB);
        ur[c] = ld16_dev(rW, WS_KVT + (unsigned)(2 * aa * 4096 + ce * 16), c * 4096);
      }
    }
#pragma unroll
    for (int j = 0; j < 2; ++j) WDU(9 + j, L_B3 + wj * 10240 + j * 2048);
    WAIT_BAR(10);
    STAMP(5);
#pragma unroll
    for (int j = 2; j < 5; ++j) WDU(9 + j, L_B3 + wj * 10240 + j * 2048);
    {
      asm volatile("s_waitcnt vmcnt(10)" : "+v"(hc[0]), "+v"(hc[1]), "+v"(hp[0]), "+v"(hp[1]), "+v"(ur[0]), "+v"(ur[1])::"memory");
      auto put = [&](unsigned hb, u32x4 pv, u32x4 cu, int blk) {
        const unsigned a0 = __builtin_amdgcn_alignbit(pv[1], pv[0], 16), a1 = __builtin_amdgcn_alignbit(pv[2], pv[1], 16),
                       a2 = __builtin_amdgcn_alignbit(pv[3], pv[2], 16), a3 = __builtin_amdgcn_alignbit(cu[0], pv[3], 16),
                       a4 = __builtin_amdgcn_alignbit(cu[1], cu[0], 16);
        lds_st<u32x4>(hb + blk * 16, u32x4{a0, a1, a2, a3});
        lds_st<u32x4>(hb + 4160 + blk * 16, u32x4{pv[1], pv[2], pv[3], cu[0]});
        lds_st<u32x4>(hb + 8320 + blk * 16, u32x4{a1, a2, a3, a4});
        lds_st<u32x4>(hb + 12480 + blk * 16, u32x4{pv[2], pv[3], cu[0], cu[1]});
      };
#pragma unroll
      for (int c = 0; c < 2; ++c) {
        put(L0 + L_CV + c * 16640, hp[c], hc[c], ce + 1);
        if (ce == 0 || ce == 255) put(L0 + L_CV + c * 16640, ce ? hc[c] : zero4, zero4, ce ? 257 : 0);
        lds_st<u32x4>(L0 + (c ? L_U1 : L_U0) + ce * 16, ur[c]);
      }
      asm volatile("s_waitcnt lgkmcnt(0)" ::: "memory");
      if (lane == 0) lds_inc(L0 + L_SY + 24);
    }
    PARK();
    LDS_SET(L0 + L_SY + 64 + wj * 4, 1u);
    xo = ld16(rX, VXO()); st2 = ld8_dev(rW, VS2());
    if (b == 0 && wj == 0) {
      u32x2 f[4];
#pragma unroll
      for (int i = 0; i < 4; ++i) f[i] = ld8_dev(rW, WS_FA + (unsigned)((64 * i + lane) * 128));
      asm volatile("s_waitcnt vmcnt(0)" : "+v"(f[0]), "+v"(f[1]), "+v"(f[2]), "+v"(f[3])::"memory");
#pragma unroll
      for (int i = 0; i < 4; ++i) spin_flags(rW, WS_FA + (unsigned)((64 * i + lane) * 128), f[i], elo, ehi);
      if (lane == 0) { u32x2 e = {elo, ehi}; st8_dev(rW, WS_GEN, e); }
    }
    STAMP(7);
    const int vch = wj >> 1, vrh = wj & 1, c15 = lane & 15;
    f32x4 ya[8];
#pragma unroll
    for (int c = 0; c < 8; ++c) { ya[c] = f32x4{0.f, 0.f, 0.f, 0.f}; PIN(ya[c]); }
    unsigned xo_ = L0 + L_CV + vch * 16640 + (vrh ^ 1) * 4096 + lane * 16, xb = L0 + L_CV + vch * 16640 + vrh * 4096 + lane * 16;
    unsigned zs_ = L0 + L_CV + vch * 16640 + vrh * 4096 + c15 * 32 + G * 8, uq_ = L0 + (vch ? L_U1 : L_U0) + (2044 - 768 - 1024 * vrh - 16 * c15 - 4 * G) * 2;
    unsigned vzs = WS_ZT + (unsigned)((2 * aa + vch) * 4096 + vrh * 2048 + lane * 16);
    PIN(xo_); PIN(xb); PIN(zs_); PIN(uq_); PIN(vzs);
    {
      unsigned cA = L0 + L_CV + vch * 16640 + (c15 & 3) * 4160 + (4 * (c15 >> 2) + 8 * G + 128 * vrh) * 2;
      const unsigned ub = L0 + (vch ? L_U1 : L_U0);
      unsigned cB = ub + (240 - 16 * c15 + 8 * G + 128 * vrh) * 2;
      int lim = ((16 * c15 - 8 * G + 47) >> 5) - 4 * vrh;
      PIN(cA); PIN(cB);
      bf16x8 bfr[2][8]; s16x4 ah[2][4];
#define VLD8(a) (*(const volatile LAS s16x4*)(size_t)(a))
#define CA(set, r, p, i) ah[set][i] = VLD8(cA + (r) * 64 + (((i) & 2) ? 7 - (p) : (p)) * 512 + ((i) & 1) * 8)
#define CB(r, mu) bfr[(r) & 1][mu] = lds_ld<bf16x8>((mu) == 0 ? ((r) >= lim ? L0 + L_SY + 32 : cB + (r) * 64 + 3584) : cB + (r) * 64 + (7 - (mu)) * 512)
#define MF16(c, a, b) asm volatile("v_mfma_f32_16x16x32_bf16 %0, %1, %2, %0" : "+v"(c) : "v"(a), "v"(b))
#define CM(r, p, m) do { if ((m) + (p) < 8) MF16(ya[((p) + (m)) & 7], cat(ah[(p) & 1][0], ah[(p) & 1][1]), bfr[(r) & 1][(m) & 7]);  \
        else MF16(ya[((m) - 1) & 7], cat(ah[(p) & 1][2], ah[(p) & 1][3]), bfr[(r) & 1][((m) + (p)) & 7]); } while (0)
#define CKEEP(r, p) asm volatile("" ::"v"(ah[(p) & 1][0]), "v"(ah[(p) & 1][1]), "v"(ah[(p) & 1][2]), "v"(ah[(p) & 1][3]), "v"(bfr[(r) & 1][0]), "v"(bfr[(r) & 1][1]), \
        "v"(bfr[(r) & 1][2]), "v"(bfr[(r) & 1][3]), "v"(bfr[(r) & 1][4]), "v"(bfr[(r) & 1][5]), "v"(bfr[(r) & 1][6]), "v"(bfr[(r) & 1][7]))
#define CPAIR(r, p) do { _Pragma("unroll") for (int m = 0; m < 9; ++m) { CM(r, p, m);                                               \
          if (m < 4) CA(((p) + 1) & 1, (p) < 3 ? (r) : (r) + 1, ((p) + 1) & 3, m); else if (m < 6) CB((r) + 1, 2 * (p) + m - 4); SB(); } CKEEP(r, p); } while (0)
#define CONV(r) do { CPAIR(r, 0); CPAIR(r, 1); CPAIR(r, 2); CPAIR(r, 3); } while (0)
      lds_wait_ge(L0 + L_SY + 24, 8u); __builtin_amdgcn_s_setprio(2);
      STAMP(24);
#pragma unroll
      for (int i = 0; i < 4; ++i) CA(0, 0, 0, i);
#pragma unroll
      for (int mu = 0; mu < 8; ++mu) CB(0, mu);
#pragma nounroll
      for (int it = 0; it < 2; ++it) {
        CONV(0); CPAIR(1, 0); CPAIR(1, 1); CPAIR(1, 2);
        if (it) {
          asm volatile("s_waitcnt vmcnt(0) lgkmcnt(0)" ::: "memory"); LDS_SET(L0 + L_SY + 48 + wj * 4, 1u); LDS_SET(L0 + L_SY + wj * 4, 1u); }
        CPAIR(1, 3);
        cA += 128; cB += 128; lim -= 2; PIN(cA); PIN(cB);
      }
    }
    asm volatile("s_nop 7" ::: "memory");
    STAMP(27);
    {
      const unsigned mine = L0 + L_SY + 48 + wj * 4, other = L0 + L_SY + 48 + (wj ^ 1) * 4;
      const float dsc = vch ? bhi(dpair) : blo(dpair);
      u32x2 uq[4];
#pragma unroll
      for (int c = 0; c < 4; ++c) uq[c] = lds_ld<u32x2>(uq_ + (768 - 256 * c) * 2);
      lds_spin_ge(other, 1u);
      f32x4 y[4];
      if (vrh) {
        asm volatile("" ::: "memory");
#pragma unroll
        for (int c = 0; c < 4; ++c) { lds_st<f32x4>(xo_ + c * 1024, ya[c]); y[c] = ya[4 + c]; PIN(y[c]); }
      } else {
#pragma unroll
        for (int c = 0; c < 4; ++c) { lds_st<f32x4>(xo_ + c * 1024, ya[4 + c]); y[c] = ya[c]; PIN(y[c]); }
        asm volatile("" ::: "memory");
      }
      asm volatile("s_waitcnt lgkmcnt(0)" ::: "memory");
      *(volatile LAS unsigned*)(size_t)mine = 2u;
#pragma unroll
      for (int c = 0; c < 4; ++c)
#pragma unroll
        for (int e = 0; e < 4; ++e) {
          const unsigned uu = (e & 2) ? uq[c].x : uq[c].y;
          float s = __builtin_fmaf((e & 1) ? blo(uu) : bhi(uu), dsc, y[c][e]); PIN(s); y[c][e] = s;
        }
      lds_spin_ge(other, 2u);
      f32x4 o[4];
#pragma unroll
      for (int c = 0; c < 4; ++c) o[c] = lds_ld<f32x4>(xb + c * 1024);
      SB();
      u32x2 zp[4];
#pragma unroll
      for (int c = 0; c < 4; ++c) {
        float z[4];
#pragma unroll
        for (int e = 0; e < 4; ++e) { z[e] = y[c][e] + o[c][e]; asm("" : "+v"(z[e])); }
        zp[c] = u32x2{min(cvtpk(z[0], z[1]), POISON - 1u), min(cvtpk(z[2], z[3]), POISON - 1u)};
      }
#pragma unroll
      for (int c = 0; c < 4; ++c) lds_st<u32x2>(zs_ + c * 512, zp[c]);
#pragma unroll
      for (int i = 0; i < 2; ++i)
        st16_dev(rW, vzs + i * 1024, lds_ld<u32x4>(xb + i * 1024));
    }
    STAMP(26);
    if (lane == 0) lds_inc(L0 + L_SY + 20);
    STAMP(9);
    lds_spin_ge(L0 + L_SY + 80 + wj * 4, 1u);
    STAMP(31);
  } else {
    __builtin_amdgcn_s_setprio(3);
    u32x2 sh[2]; u32x4 xr[2];
#pragma unroll
    for (int i = 0; i < 2; ++i) xr[i] = ld16(rX, (unsigned)((8 * aa + 2 * wj + i) * 1024 + lane * 16));
    {
      const rsrc_t rK = mk_rsrc(kin, S_ * D_ * 2), rV = mk_rsrc(vin, S_ * D_ * 2);
      const unsigned kb = (unsigned)((64 * idx + l3) * 1024 + pan * 128), fl = (unsigned)(l7 ^ ((l3 >> 1) & 1));
#pragma unroll
      for (int i = 0; i < 2; ++i) {
        const int g = 2 * wj + i;
        dma16(rK, kb + ((fl ^ (((g + 1) & 3) << 1)) << 4), g * 8192, L0 + L_KA + (g + 1) * 1024);
        dma16(rV, kb + ((fl ^ ((g & 3) << 1)) << 4), g * 8192, L0 + L_VA + g * 1024);
      }
      if (wj == 0) dma16(rK, idx == 0 ? OOB : kb - 8192 + (fl << 4), 0, L0 + L_KA);
    }
    u32x2 gen = ld8_dev(rW, WS_GEN);
    {
      const rsrc_t rSh = mk_rsrc(shk, D_ * 4 * 2);
#pragma unroll
      for (int i = 0; i < 2; ++i) sh[i] = ld8(rSh, (unsigned)((64 * pan + 16 * wj + 8 * i + 4 * (G & 1) + pp) * 8));
    }
    asm volatile("s_barrier" ::: "memory");
    STAMP(3);
    const int tp = wj & 1, ch = wj >> 1;
    f32x16 acc[2][3];
    f32x4 cs[3];
    unsigned ax[4], aw[2][3], ak[5];
    unsigned vkv = WS_KVT + (unsigned)((64 * pan + 16 * wj + l3) * 4096 + (1984 - 64 * idx + 8 * l7) * 2);
    unsigned vst0 = lane < 2 ? WS_ST + (unsigned)((8 * aa + 2 * wj + lane) * 8) : OOB;
    int src4 = 4 * (16 * ((((7 - l7) >> 2) << 1) | (l3 >> 2)) + 4 * ((7 - l7) & 3) + (l3 & 3));
    const unsigned so_ = ((lane & 15) == 4 * (G & 1)) ? 0x3f803f80u : 0u;
    bf16x8 sel = __builtin_bit_cast(bf16x8, u32x4{so_, so_, so_, so_});
    {
      const unsigned ax0 = L0 + (64 * tp + l31) * 128 + (((4 * tp + hh) ^ ((l31 >> 1) & 7)) << 4);
      const unsigned aw0 = L0 + 16384 + (G & 1) * 12288 + (8 * hh + qq) * 192 + ((pp & 1) << 4) + 8 * (pp >> 1);
#pragma unroll
      for (int s = 0; s < 4; ++s) { ax[s] = ax0 ^ (s << 5); PIN(ax[s]); }
#pragma unroll
      for (int i = 0; i < 3; ++i) {
        const unsigned a_ = aw0 + (((3 * ch + i) ^ (G & 1)) << 5);
        aw[0][i] = a_ + tp * 6144; aw[1][i] = a_ + 6144 - tp * 6144; PIN(aw[0][i]); PIN(aw[1][i]);
      }
#pragma unroll
      for (int j = 0; j < 5; ++j) { const int rr = 8 * (4 * (G >> 1) + pp) + qq + (j < 3 ? 4 + 4 * j : 4 * (j - 3));
        ak[j] = L0 + (j < 3 ? L_KA : L_VA) + rr * 128 + (((2 * wj) ^ fsw(rr)) << 4) + 8 * (G & 1); PIN(ak[j]); }
      PIN(vkv); PIN(vst0); PIN(src4); PIN(sel);
    }
    {
      asm volatile("s_waitcnt vmcnt(7)" : "+v"(xr[0]), "+v"(xr[1])::"memory");
      STAMP(14);
      const f32x2 x0 = {blo(__builtin_amdgcn_readfirstlane(xr[0][0])), blo(__builtin_amdgcn_readfirstlane(xr[1][0]))};
      f32x2 d[8];
#pragma unroll
      for (int e = 0; e < 4; ++e) { d[2 * e] = f32x2{blo(xr[0][e]), blo(xr[1][e])} - x0; d[2 * e + 1] = f32x2{bhi(xr[0][e]), bhi(xr[1][e])} - x0; }
      const f32x2 sm = ((d[0] + d[1]) + (d[2] + d[3])) + ((d[4] + d[5]) + (d[6] + d[7]));
      const f32x2 sq = ((d[0] * d[0] + d[1] * d[1]) + (d[2] * d[2] + d[3] * d[3])) + ((d[4] * d[4] + d[5] * d[5]) + (d[6] * d[6] + d[7] * d[7]));
      float s0 = sm[0], s1 = sm[1], q0 = sq[0], q1 = sq[1];
      wave_sum4(s0, s1, q0, q1);
      const float m = (lane ? s1 : s0) * (1.0f / D_), var = (lane ? q1 : q0) * (1.0f / D_) - m * m;
      unsigned ms0 = cvtpk(m + (lane ? x0[1] : x0[0]), __builtin_amdgcn_sqrtf(rbf(rbf(__builtin_fmaxf(var, 0.f)) + 1e-5f)));
      asm volatile("s_waitcnt vmcnt(0)" : "+v"(gen), "+v"(sh[0]), "+v"(sh[1]), "+v"(ms0)::"memory");
      STAMP(15);
      if (lane == 0) lds_inc(L0 + L_A);
      elo = __builtin_amdgcn_readfirstlane(gen.x) + 1u;
      ehi = __builtin_amdgcn_readfirstlane(gen.y) + (elo == 0u ? 1u : 0u);
      st8_dev(rW, vst0, u32x2{ms0, elo});
      f32x2 sk[4] = {{blo(sh[0].x), blo(sh[1].x)}, {bhi(sh[0].x), bhi(sh[1].x)}, {blo(sh[0].y), blo(sh[1].y)}, {bhi(sh[0].y), bhi(sh[1].y)}};
      PIN(sk[0]); PIN(sk[1]); PIN(sk[2]); PIN(sk[3]);
      lds_spin_ge(L0 + L_A, 4u);
      STAMP(1);
      u32x2 t[2][5];
#pragma unroll
      for (int j = 0; j < 5; ++j) { t[0][j] = __builtin_bit_cast(u32x2, trd(ak[j])); t[1][j] = __builtin_bit_cast(u32x2, trd(ak[j] ^ 16)); }
      SB();
      f32x2 kk[20], r[8];
#pragma unroll
      for (int j = 0; j < 5; ++j) {
        kk[4 * j] = f32x2{blo(t[0][j].x), blo(t[1][j].x)}; kk[4 * j + 1] = f32x2{bhi(t[0][j].x), bhi(t[1][j].x)};
        kk[4 * j + 2] = f32x2{blo(t[0][j].y), blo(t[1][j].y)}; kk[4 * j + 3] = f32x2{bhi(t[0][j].y), bhi(t[1][j].y)};
      }
#pragma unroll
      for (int e = 0; e < 8; ++e) r[e] = kk[e + 1] * sk[3];
#pragma unroll
      for (int j = 2; j >= 0; --j)
#pragma unroll
        for (int e = 0; e < 8; ++e) r[e] = __builtin_elementwise_fma(kk[e + 4 - j], sk[j], r[e]);
#pragma unroll
      for (int e = 0; e < 8; ++e) r[e] *= kk[12 + e];
      u32x4 po[2];
#pragma unroll
      for (int i = 0; i < 2; ++i)
#pragma unroll
        for (int e = 0; e < 4; ++e) po[i][e] = (unsigned)__builtin_amdgcn_ds_bpermute(src4, (int)cvtpk(r[7 - 2 * e][i], r[6 - 2 * e][i]));
      st16_dev(rW, vkv, po[0]); st16_dev(rW, vkv + 32768, po[1]);
      __builtin_amdgcn_s_setprio(1);
    }
    STAMP(2);
    {
      bf16x8 xf[2][2]; s16x4 wf[2][3][2];
#define PINA(t, i) PIN(acc[t][i])
#define M1(p, t, i) do { acc[t][i] = MFMA32(cat(wf[p][i][0], wf[p][i][1]), xf[p][t], acc[t][i]); PINA(t, i); } while (0)
#define MS(p, i, on) do { if (on) { cs[i] = MFMA16(sel, cat(wf[p][i][0], wf[p][i][1]), cs[i]); PIN(cs[i]); } } while (0)
#define RW(s, i) do { wf[(s) & 1][i][0] = trd(aw[(s) >> 1][i] + ((s) & 1) * 3072); wf[(s) & 1][i][1] = trd(aw[(s) >> 1][i] + ((s) & 1) * 3072 + 768); } while (0)
#define RX(s, t) xf[(s) & 1][t] = lds_ld<bf16x8>(ax[s] + (t) * 4096)
#define KEEP(p) do { asm volatile("" ::"v"(wf[p][0][0]), "v"(wf[p][0][1]), "v"(wf[p][1][0]), "v"(wf[p][1][1]), "v"(wf[p][2][0]), "v"(wf[p][2][1]), "v"(xf[p][0]), "v"(xf[p][1])); } while (0)
#define ADV(a) do { a += dl; PIN(a); } while (0)
#define STEP(p, s, on) do { M1(p, 0, 0); RW(s, 0); SB(); M1(p, 1, 0); RX(s, 0); RX(s, 1); SB(); M1(p, 0, 1); RW(s, 1); SB();  \
        M1(p, 1, 1); RW(s, 2); SB(); M1(p, 0, 2);                                                                             \
        if ((s) == 3) { ADV(ax[0]); ADV(ax[1]); ADV(ax[2]); ADV(ax[3]); ADV(aw[0][0]); } SB(); M1(p, 1, 2);                     \
        if ((s) == 3) { ADV(aw[0][1]); ADV(aw[0][2]); ADV(aw[1][0]); ADV(aw[1][1]); ADV(aw[1][2]); }                            \
        MS(p, 0, on); MS(p, 1, on); MS(p, 2, on); KEEP(p); SB(); } while (0)
      LBAR();
      STAMP(16);
      RW(0, 0); RX(0, 0); RX(0, 1); RW(0, 1); RW(0, 2); SB();
      unsigned dl = SLOT;
#pragma unroll
      for (int i = 0; i < 3; ++i) {
        cs[i] = f32x4{0.f, 0.f, 0.f, 0.f};
#pragma unroll
        for (int r = 0; r < 16; ++r) { acc[0][i][r] = 0.f; acc[1][i][r] = 0.f; }
      }
      STEP(0, 1, true);
#pragma nounroll
      for (int c = 0; c < 7; ++c) {
        dl = (c == 3) ? 0u - 3u * SLOT : (unsigned)SLOT;
        STEP(1, 2, true);
        if (c == 2) WAITV(0);
        STEP(0, 3, false);
        LBAR();
        STEP(1, 0, false); STEP(0, 1, true);
      }
      STAMP(4);
      STEP(1, 2, true);
#pragma unroll
      for (int i = 0; i < 3; ++i) lds_st<float>(L0 + L_CS + (wj * 3 + i) * 256 + lane * 4, cs[i][0]);
      STEP(0, 3, false);
      LBAR();
      float co[3]; unsigned ms[2];
#pragma unroll
      for (int i = 0; i < 3; ++i) co[i] = lds_ld<float>(L0 + L_CS + ((wj ^ 1) * 3 + i) * 256 + lane * 4);
#pragma unroll
      for (int t = 0; t < 2; ++t) ms[t] = lds_ld<unsigned>(L0 + L_ST + (64 * tp + 32 * t + l31) * 4);
      SB();
      M1(1, 0, 0); M1(1, 1, 0); M1(1, 0, 1); M1(1, 1, 1); M1(1, 0, 2); M1(1, 1, 2); SB();
      float k0[2], k1[2], s2[2];
      u32x4 bm[2];
#pragma unroll
      for (int t = 0; t < 2; ++t) {
        const unsigned mn = (ms[t] & 0xffffu) ^ 0x8000u;
        bm[t] = u32x4{mn | (mn << 16), mn, 0u, 0u};
        const float sd = bhi(ms[t]), rs = __builtin_amdgcn_rcpf(sd);
        k0[t] = rs * -2.3022081983f; k1[t] = k0[t] * rs * rs * 0.044715f; s2[t] = sd * sd;
      }
#pragma unroll
      for (int i = 0; i < 3; ++i) {
        const float c = cs[i][0] + co[i], c1 = c - rbf(c), c2 = c1 - rbf(c1);
        const u32x4 am = {cvtpk(c, c1), cvtpk(c2, 0.f), 0u, 0u};
#pragma unroll
        for (int t = 0; t < 2; ++t) { acc[t][i] = MFMA32(__builtin_bit_cast(bf16x8, am), __builtin_bit_cast(bf16x8, bm[t]), acc[t][i]); PINA(t, i); }
      }
      STAMP(5);
      const unsigned vhs = WS_H + (unsigned)(((4 * rt + 2 * tp) * 86 + 6 * cg + 3 * ch) * 1024 + lane * 16);
#pragma unroll
      for (int i = 0; i < 3; ++i) {
        const unsigned vo = (vhs + i * 1024) | (6 * cg + 3 * ch + i < 86 ? 0u : OOB);
#pragma unroll
        for (int t = 0; t < 2; ++t) {
          f32x2 g[4], zz[4], gu[4];
#pragma unroll
          for (int r = 0; r < 4; ++r) {
            g[r] = f32x2{acc[t][i][2 * r], acc[t][i][2 * r + 1]};
            zz[r] = g[r] * __builtin_elementwise_fma(g[r] * g[r], f32x2{k1[t], k1[t]}, f32x2{k0[t], k0[t]});
          }
#pragma unroll
          for (int r = 0; r < 4; ++r) {
            zz[r] = f32x2{__builtin_amdgcn_exp2f(zz[r][0]), __builtin_amdgcn_exp2f(zz[r][1])};
            gu[r] = g[r] * f32x2{acc[t][i][8 + 2 * r], acc[t][i][9 + 2 * r]};
          }
          u32x4 hq;
#pragma unroll
          for (int r = 0; r < 4; ++r) {
            const f32x2 dd = __builtin_elementwise_fma(zz[r], f32x2{s2[t], s2[t]}, f32x2{s2[t], s2[t]});
            const f32x2 hv = gu[r] * f32x2{__builtin_amdgcn_rcpf(dd[0]), __builtin_amdgcn_rcpf(dd[1])};
            hq[r] = cvtpk(hv[0], hv[1]);
          }
          st16_dev(rW, vo + t * (86 * 1024), hq);
        }
      }
    }
    if (lane == 0) lds_inc(L0 + L_SY + 24);
    STAMP(6);
    const unsigned vd = VD();
    PARK();
    WAITV(0);
    if (lane == 0 && lds_inc(L0 + L_SY + 16) == 3u && cg < 15) { u32x2 e = {elo, ehi}; st8_dev(rW, WS_FH + (unsigned)((rt * 16 + cg) * 128), e); }
    STAMP(8);
    lds_wait_ge(L0 + L_SY + 64 + wj * 4, 1u);
#pragma unroll
    for (int j = 0; j < 4; ++j) WDU(14 + j, L_B1 + wj * 8192 + j * 2048);
#pragma unroll
    for (int j = 0; j < 4; ++j) WDU(18 + j, L_B2 + wj * 10240 + j * 2048);
    xo = ld16(rX, VXO()); st2 = ld8_dev(rW, VS2());
    {
      const int gf = ks0 / 6, gl = (ks0 + 20 + (wj < 2)) / 6, gi = min(gf + l7, gl);
      const unsigned vfh = WS_FH + (unsigned)(((r64 >> 1) * 16 + gi) * 128);
      u32x2 fh = ld8_dev(rW, vfh);
      asm volatile("s_waitcnt vmcnt(0)" : "+v"(fh)::"memory");
      spin_flags(rW, vfh, fh, elo, ehi);
    }
    LDS_SET(L0 + L_SY + 80 + wj * 4, 1u);
  }
  STAMP(10);
  {
    u32x4 hf[22];
    const unsigned vh = WS_H + (unsigned)(((2 * r64 + (w >> 2)) * 86 + ks0) * 1024 + lane * 16);
#pragma unroll
    for (int j = 0; j < 22; ++j) hf[j] = ld16_dev(rW, vh, min(PH(j), klast) * 1024);
    const unsigned vz = WS_ZT + (unsigned)((64 * ct + 8 * w + l3) * 4096 + r64 * 128 + l7 * 16);
    u32x4 zr = ld16_dev(rW, vz, 0);
    STAMP(28);
    lds_wait_ge(L0 + L_SY + wj * 4, 1u);
    const unsigned a1 = AD() + L_B1 + wj * 8192, a2 = AD() + L_B2 + wj * 10240;
    f32x16 fo[2];
#pragma unroll
    for (int r = 0; r < 16; ++r) { fo[0][r] = 0.f; fo[1][r] = 0.f; }
#define G2(j) do { if ((j) + 2 >= 8 && (j) + 2 < 22) { wpk[((j) + 2) & 7][0] = WDF(UA((j) + 2), 0); wpk[((j) + 2) & 7][1] = WDF(UA((j) + 2), 1); } \
      asm volatile("s_waitcnt vmcnt(%1)" : "+v"(hf[j]) : "i"(22 - (j)) : "memory");                                    \
      fo[0] = MFMA32(wpk[(j) & 7][0], __builtin_bit_cast(bf16x8, hf[j]), fo[0]);                                      \
      fo[1] = MFMA32(wpk[(j) & 7][1], __builtin_bit_cast(bf16x8, hf[j]), fo[1]); PIN(fo[0]); PIN(fo[1]); SB(); } while (0)
    G2(0); STAMP(29); G2(1); G2(2); G2(3); G2(4); G2(5); G2(6); G2(7); G2(8); G2(9); G2(10);
    G2(11); G2(12); G2(13); G2(14); G2(15); G2(16); G2(17); G2(18); G2(19); G2(20); G2(21);
    STAMP(11);
    unsigned pb;
    if (w < 4) { LDS_SET(L0 + L_SY + 96 + wj * 4, 1u); pb = L0 + L_P0 + wj * 8192; }
    else { lds_spin_ge(L0 + L_SY + 96 + wj * 4, 1u); pb = L0 + L_B2 + wj * 10240; }
    lds_spin_ge(L0 + L_SY + 20, 4u);
#pragma unroll
    for (int dt = 0; dt < 2; ++dt)
#pragma unroll
      for (int i = 0; i < 4; ++i)
        lds_st<f32x4>(pb + l31 * 256 + (((8 * dt + 2 * i + hh) ^ l7) << 4), f32x4{fo[dt][4 * i], fo[dt][4 * i + 1], fo[dt][4 * i + 2], fo[dt][4 * i + 3]});
    asm volatile("s_waitcnt vmcnt(0)" : "+v"(zr), "+v"(xo), "+v"(st2)::"memory");
    for (unsigned n = 0; __any(max(max(zr[0], zr[1]), max(zr[2], zr[3])) == POISON) && n < SPIN_MAX; ++n) {
      zr = ld16_dev(rW, vz, 0);
      asm volatile("s_waitcnt vmcnt(0)" : "+v"(zr)::"memory");
    }
    STAMP(30);
    const int r = 8 * w + l3;
    lds_st<u32x4>(L0 + L_ZT + r * 128 + ((l7 ^ fsw(r)) << 4), zr);
  }
  st2 = spin_tag(rW, VS2(), st2, elo);
  LBAR();
  STAMP(12);
  {
    u32x2 zt[2];
    const int o = 4 * (G >> 1) + pp;
#pragma unroll
    for (int j = 0; j < 2; ++j) { const int r = 8 * o + 4 * j + qq;
      zt[j] = __builtin_bit_cast(u32x2, trd(L0 + L_ZT + r * 128 + ((w ^ fsw(r)) << 4) + 8 * (G & 1))); }
    const unsigned pb = L0 + (w < 4 ? L_P0 : L_B2) + (8 * wj + l3) * 256, ps = w < 4 ? 8192 : 10240;
    f32x4 p0 = {0.f, 0.f, 0.f, 0.f}, p1 = p0;
#pragma unroll
    for (int k = 0; k < 4; ++k) {
      p0 += lds_ld<f32x4>(pb + k * ps + (((2 * l7) ^ l3) << 4));
      p1 += lds_ld<f32x4>(pb + k * ps + (((2 * l7 + 1) ^ l3) << 4));
    }
    const int src = 16 * (((l7 >> 2) << 1) | (l3 >> 2)) + 4 * (l7 & 3) + (l3 & 3);
    u32x4 zq;
    zq[0] = (unsigned)__builtin_amdgcn_ds_bpermute(src * 4, (int)zt[0].x); zq[1] = (unsigned)__builtin_amdgcn_ds_bpermute(src * 4, (int)zt[0].y);
    zq[2] = (unsigned)__builtin_amdgcn_ds_bpermute(src * 4, (int)zt[1].x); zq[3] = (unsigned)__builtin_amdgcn_ds_bpermute(src * 4, (int)zt[1].y);
    const float mean = blo(st2.x), rs = __builtin_amdgcn_rcpf(bhi(st2.x));
    float o8[8];
#pragma unroll
    for (int i = 0; i < 8; ++i) {
      const float xv = (i & 1) ? bhi(xo[i >> 1]) : blo(xo[i >> 1]), zv = (i & 1) ? bhi(zq[i >> 1]) : blo(zq[i >> 1]);
      const float F = i < 4 ? p0[i & 3] : p1[i & 3];
      const float gs = F * __builtin_amdgcn_rcpf(1.0f + __builtin_amdgcn_exp2f(F * -1.4426950408889634f));
      o8[i] = __builtin_fmaf((xv - mean) * rs * zv, gs, xv);
    }
    const u32x4 ov = {cvtpk(o8[0], o8[1]), cvtpk(o8[2], o8[3]), cvtpk(o8[4], o8[5]), cvtpk(o8[6], o8[7])};
    *(GAS u32x4*)((GAS char*)out + VXO()) = ov;
  }
  WAITV(0); STAMP(13);
  { const u64 tgt = prb0 + 4000 + 4 * (prb1 - prb0); while (__builtin_amdgcn_s_memrealtime() < tgt) __builtin_amdgcn_s_sleep(1); }
}

extern "C" void kernel_launch(void* const* d_in, const int* in_sizes, int n_in, void* d_out, int out_size, void* d_ws, size_t ws_size,
                              hipStream_t stream) {
  (void)in_sizes; (void)n_in; (void)out_size;
  (void)hipFuncSetAttribute((const void*)h3_geglu_fused, hipFuncAttributeMaxDynamicSharedMemorySize, LDS_BYTES);
  h3_geglu_fused<<<256, 512, LDS_BYTES, stream>>>((const u16*)d_in[0], (const u16*)d_in[1], (const u16*)d_in[2], (const u16*)d_in[3],
                                                  (const u16*)d_in[4], (const u16*)d_in[5], (const u16*)d_in[7], (const u16*)d_in[8],
                                                  (const u16*)d_in[9], (u16*)d_out, (char*)d_ws + WS_BASE(ws_size));
}
```

```cpp
#include <hip/hip_runtime.h>
#include <hip/hip_bf16.h>

typedef unsigned short u16;
typedef unsigned long long u64;
typedef __attribute__((ext_vector_type(8))) short bf16x8;
typedef __attribute__((ext_vector_type(4))) short s16x4;
typedef __attribute__((ext_vector_type(2))) __bf16 bf16x2_t;
typedef __attribute__((ext_vector_type(2))) float f32x2;
typedef __attribute__((ext_vector_type(4))) float f32x4;
typedef __attribute__((ext_vector_type(16))) float f32x16;
typedef __attribute__((ext_vector_type(4))) unsigned u32x4;
typedef __attribute__((ext_vector_type(2))) unsigned u32x2;
#define LAS __attribute__((address_space(3)))
#define GAS __attribute__((address_space(1)))
typedef __amdgpu_buffer_rsrc_t rsrc_t;

constexpr int S_ = 2048, D_ = 512, DF = 1376;
constexpr unsigned WS_GEN = 0, WS_FA = 4096, WS_FH = WS_FA + 256 * 128, WS_FZ = WS_FH + 256 * 128;
constexpr unsigned WS_ST = WS_FZ + 256 * 128;
constexpr unsigned WS_KVT = 1u << 20;
constexpr unsigned WS_ZT = WS_KVT + (2u << 20);
constexpr unsigned WS_H = WS_ZT + (2u << 20);
constexpr unsigned WS_BYTES = WS_H + 64 * 86 * 1024;
constexpr int SLOT = 40960;
constexpr int L_CV = 3 * SLOT;
constexpr int L_KA = L_CV, L_VA = L_KA + 9216;
constexpr int L_B1 = 0, L_B2 = SLOT, L_B3 = 2 * SLOT;
constexpr int L_CS = 32768;
constexpr int L_ST = L_CS + 3072;
constexpr int L_SY = L_ST + 512;
constexpr int L_A = L_VA + 8192;
constexpr int L_P0 = L_CV + 8192;
constexpr int L_U0 = 36864, L_U1 = L_CV + 33280, L_ZT = L_CV;
constexpr int LDS_BYTES = 163840;
constexpr unsigned OOB = 0x80000000u;
constexpr unsigned POISON = 0xffbfffbfu;
constexpr unsigned SPIN_MAX = 1u << 18;

#define LPRIO 2
#define XSLEEP 4
#define ROT1 ((rt + cg) & 7)
#define ROT2 (((xcd * 4 + (idx >> 3)) * 11) >> 4)
#define HWIN 10
#define STEPMODE 1
#define NOSTATS 0
#define WS_BASE(sz) 0
#define ZSLEEP 4
#define ABL_PLAIN 0
#define ZAT 15
#define MFMA32(a, b, c) __builtin_amdgcn_mfma_f32_32x32x16_bf16(a, b, c, 0, 0, 0)
#define MFMA16(a, b, c) __builtin_amdgcn_mfma_f32_16x16x32_bf16(a, b, c, 0, 0, 0)
#define WAIT_BAR(N) asm volatile("s_waitcnt vmcnt(" #N ") lgkmcnt(0)\n\ts_barrier" ::: "memory")
#define LBAR() asm volatile("s_waitcnt lgkmcnt(0)\n\ts_barrier" ::: "memory")
#define WAITV(N) asm volatile("s_waitcnt vmcnt(" #N ")" ::: "memory")
#define SB() __builtin_amdgcn_sched_barrier(0)

__device__ __forceinline__ rsrc_t mk_rsrc(const void* p, unsigned bytes) {
  return __builtin_amdgcn_make_buffer_rsrc((void*)p, (short)0, (int)bytes, 0x00020000);
}
__device__ __forceinline__ void dma16(rsrc_t rs, unsigned voff, unsigned soff, unsigned lds) {
  asm volatile("s_mov_b32 m0, %0\n\ts_nop 0\n\tbuffer_load_dwordx4 %1, %2, %3 offen lds" ::"s"(lds), "v"(voff), "s"(rs), "s"(soff) : "memory");
}
__device__ __forceinline__ u32x4 ld16(rsrc_t rs, unsigned voff) {
  u32x4 r; asm volatile("buffer_load_dwordx4 %0, %1, %2, 0 offen" : "=v"(r) : "v"(voff), "s"(rs) : "memory"); return r;
}
__device__ __forceinline__ u32x4 ld16_dev(rsrc_t rs, unsigned voff, unsigned soff) {
  u32x4 r; asm volatile("buffer_load_dwordx4 %0, %1, %2, %3 offen sc1" : "=v"(r) : "v"(voff), "s"(rs), "s"(soff) : "memory"); return r;
}
__device__ __forceinline__ u32x2 ld8(rsrc_t rs, unsigned voff) {
  u32x2 r; asm volatile("buffer_load_dwordx2 %0, %1, %2, 0 offen" : "=v"(r) : "v"(voff), "s"(rs) : "memory"); return r;
}
__device__ __forceinline__ u32x2 ld8_dev(rsrc_t rs, unsigned voff) {
  u32x2 r; asm volatile("buffer_load_dwordx2 %0, %1, %2, 0 offen sc1" : "=v"(r) : "v"(voff), "s"(rs) : "memory"); return r;
}
#define PIN(x) asm volatile("" : "+v"(x))
__device__ __forceinline__ void st16_dev(rsrc_t rs, unsigned voff, u32x4 v) { __builtin_amdgcn_raw_buffer_store_b128(v, rs, voff, 0, 16); }
__device__ __forceinline__ void st8_dev(rsrc_t rs, unsigned voff, u32x2 v) { __builtin_amdgcn_raw_buffer_store_b64(v, rs, voff, 0, 16); }

__device__ __forceinline__ unsigned cvtpk(float lo, float hi) {
  bf16x2_t r = {(__bf16)lo, (__bf16)hi};
  return __builtin_bit_cast(unsigned, r);
}
__device__ __forceinline__ float blo(unsigned w) { return __uint_as_float(w << 16); }
__device__ __forceinline__ float bhi(unsigned w) { return __uint_as_float(w & 0xffff0000u); }
__device__ __forceinline__ float rbf(float f) { return blo(cvtpk(f, f)); }
__device__ __forceinline__ s16x4 trd(unsigned a) { return __builtin_amdgcn_ds_read_tr16_b64_v4i16((LAS s16x4*)(size_t)a); }
__device__ __forceinline__ bf16x8 cat(s16x4 a, s16x4 b) { return __builtin_shufflevector(a, b, 0, 1, 2, 3, 4, 5, 6, 7); }
template <class T> __device__ __forceinline__ T lds_ld(unsigned a) { return *(const LAS T*)(size_t)a; }
template <class T> __device__ __forceinline__ void lds_st(unsigned a, T v) { *(LAS T*)(size_t)a = v; }
template <int CTRL> __device__ __forceinline__ float dpp_add(float x) {
  return x + __uint_as_float(__builtin_amdgcn_update_dpp(0u, __float_as_uint(x), CTRL, 0xf, 0xf, true));
}
__device__ __forceinline__ float swap_add(float x, bool half) {
  if (half) { auto r = __builtin_amdgcn_permlane32_swap(__float_as_uint(x), __float_as_uint(x), false, false); return __uint_as_float(r[0]) + __uint_as_float(r[1]); }
  auto r = __builtin_amdgcn_permlane16_swap(__float_as_uint(x), __float_as_uint(x), false, false); return __uint_as_float(r[0]) + __uint_as_float(r[1]);
}
__device__ __forceinline__ void wave_sum4(float& a, float& b, float& c, float& d) {
  a = dpp_add<0x128>(a); b = dpp_add<0x128>(b); c = dpp_add<0x128>(c); d = dpp_add<0x128>(d);
  a = dpp_add<0x124>(a); b = dpp_add<0x124>(b); c = dpp_add<0x124>(c); d = dpp_add<0x124>(d);
  a = dpp_add<0x122>(a); b = dpp_add<0x122>(b); c = dpp_add<0x122>(c); d = dpp_add<0x122>(d);
  a = dpp_add<0x121>(a); b = dpp_add<0x121>(b); c = dpp_add<0x121>(c); d = dpp_add<0x121>(d);
  a = swap_add(a, false); b = swap_add(b, false); c = swap_add(c, false); d = swap_add(d, false);
  a = swap_add(a, true); b = swap_add(b, true); c = swap_add(c, true); d = swap_add(d, true);
}
__device__ __forceinline__ void lds_wait_ge(unsigned a, unsigned v) {
  for (unsigned n = 0; *(volatile LAS unsigned*)(size_t)a < v && n < SPIN_MAX * 16; ++n) __builtin_amdgcn_s_sleep(1);
  asm volatile("" ::: "memory");
}
__device__ __forceinline__ void lds_spin_ge(unsigned a, unsigned v) {
  for (unsigned n = 0; *(volatile LAS unsigned*)(size_t)a < v && n < SPIN_MAX * 64; ++n) {}
  asm volatile("" ::: "memory");
}
__device__ __forceinline__ unsigned lds_inc(unsigned a) {
  return __hip_atomic_fetch_add((LAS unsigned*)(size_t)a, 1u, __ATOMIC_RELAXED, __HIP_MEMORY_SCOPE_WORKGROUP);
}
__device__ __forceinline__ int fsw(int r) { return ((r >> 1) & 1) | (((r >> 3) & 3) << 1); }
__device__ __forceinline__ void spin_flags(rsrc_t rs, unsigned voff, u32x2 f, unsigned lo, unsigned hi) {
  for (unsigned n = 0; !__all(f.x == lo && f.y == hi) && n < SPIN_MAX; ++n) {
    __builtin_amdgcn_s_sleep(1);
    f = ld8_dev(rs, voff); WAITV(0); PIN(f);
  }
}
__device__ __forceinline__ u32x2 spin_tag(rsrc_t rs, unsigned voff, u32x2 f, unsigned tag) {
  for (unsigned n = 0; !__all(f.y == tag) && n < SPIN_MAX; ++n) {
    __builtin_amdgcn_s_sleep(1);
    f = ld8_dev(rs, voff); WAITV(0); PIN(f);
  }
  return f;
}
#define STAMP(i)

__global__ __launch_bounds__(512, 2) void h3_geglu_fused(const u16* __restrict__ x, const u16* __restrict__ kin, const u16* __restrict__ vin,
                                                         const u16* __restrict__ shk, const u16* __restrict__ ssk, const u16* __restrict__ ssd,
                                                         const u16* __restrict__ wg, const u16* __restrict__ wu, const u16* __restrict__ wd,
                                                         u16* __restrict__ out, char* __restrict__ ws) {
  extern __shared__ __attribute__((aligned(16))) char smem[];
  const unsigned L0 = (unsigned)(size_t)(LAS char*)smem;
  const int tid = threadIdx.x, lane = tid & 63;
  const int w = __builtin_amdgcn_readfirstlane(tid >> 6), wj = w & 3;
  STAMP(0);
  const int b = blockIdx.x, xcd = b & 7, idx = b >> 3, rh = xcd & 1, q4 = xcd >> 1;
  const int pan = 4 * rh + q4, aa = 32 * pan + idx;
  const int l3 = lane >> 3, l7 = lane & 7, G = lane >> 4, qq = (lane >> 2) & 3, pp = lane & 3, hh = lane >> 5, l31 = lane & 31;
  const rsrc_t rX = mk_rsrc(x, S_ * D_ * 2), rW = mk_rsrc(ws, WS_BYTES), rD = mk_rsrc(wd, DF * D_ * 2);
  const int cg = idx < 24 ? ((q4 * 7 + 1) >> 1) + (idx >> 3) : idx < 28 ? (q4 < 2 ? 3 : 10) : 14 + ((idx >> 1) & 1);
  const int rt = 8 * rh + (idx < 24 ? (idx & 7) : idx < 28 ? (q4 & 1) * 4 + (idx & 3) : 2 * q4 + (idx & 1));
  const int r64 = 4 * pan + (idx >> 3), ct = idx & 7;
  const int ks0 = 22 * wj - (wj == 3 ? 1 : 0);
  u32x4 xo; u32x2 st2;
  unsigned elo, ehi;
#define VD() (unsigned)((16 * ks0 + l3) * 1024 + ct * 128 + ((l7 ^ (((l3 >> 1) & 1) << 2)) << 4))
  const int rot2 = ROT2;
#define PH(j) ((j) + rot2 >= 22 ? (j) + rot2 - 22 : (j) + rot2)
  const int klast = wj < 2 ? 21 : 20;
#define WDU(j, lds) do { const unsigned v_ = vd | (PH(j) > klast ? OOB : 0u);                                       \
    dma16(rD, v_, PH(j) * 16384, L0 + (lds)); dma16(rD, v_, PH(j) * 16384 + 8192, L0 + (lds) + 1024); } while (0)
#define VXO() (unsigned)((64 * r64 + 8 * w + l3) * 1024 + ct * 128 + l7 * 16)
#define VS2() (WS_ST + (unsigned)((64 * r64 + 8 * w + l3) * 8))

  bf16x8 wpk[8][2];
#define AD() (L0 + (8 * hh + qq) * 128 + ((4 * ((qq >> 1) & 1) + 2 * (G & 1) + (pp >> 1)) << 4) + 8 * (pp & 1))
#define WDF(base, dt) cat(trd(((base) ^ ((dt) * 64))), trd(((base) ^ ((dt) * 64)) + 512))
#define UA(j) ((j) < 4 ? a1 + (j) * 2048 : (j) < 9 ? a2 + ((j) - 4) * 2048 : (j) < 14 ? a2 + SLOT + ((j) - 9) * 2048 : (j) < 18 ? a1 + ((j) - 14) * 2048 : a2 + ((j) - 18) * 2048)
#define PARK1(j) do { const unsigned a1 = AD() + L_B1 + wj * 8192, a2 = AD() + L_B2 + wj * 10240; wpk[j][0] = WDF(UA(j), 0); wpk[j][1] = WDF(UA(j), 1); } while (0)
#define PARK() do { const unsigned a1 = AD() + L_B1 + wj * 8192, a2 = AD() + L_B2 + wj * 10240;                         \
    _Pragma("unroll") for (int j = 0; j < 8; ++j) { wpk[j][0] = WDF(UA(j), 0); wpk[j][1] = WDF(UA(j), 1); }              \
    _Pragma("unroll") for (int j = 0; j < 8; ++j) { PIN(wpk[j][0]); PIN(wpk[j][1]); } } while (0)
  f32x2 xn[4];
#define XN() do { st2 = spin_tag(rW, VS2(), st2, elo); const float mean_ = blo(st2.x), rs_ = __builtin_amdgcn_rcpf(bhi(st2.x));       \
    _Pragma("unroll") for (int i = 0; i < 4; ++i) { float a_ = (blo(xo[i]) - mean_) * rs_, b_ = (bhi(xo[i]) - mean_) * rs_; PIN(a_); PIN(b_); xn[i] = f32x2{a_, b_}; } } while (0)
#define LDS_SET(a, v) do { asm volatile("" ::: "memory"); *(volatile LAS unsigned*)(size_t)(a) = (v); } while (0)

  if (w >= 4) {
    __builtin_amdgcn_s_setprio(2);
    u32x2 gen = ld8_dev(rW, WS_GEN);
    const unsigned vx = (cg < 15) ? (unsigned)((128 * rt + 8 * wj + l3) * 1024 + ((l7 ^ ((4 * wj + (l3 >> 1)) & 7)) << 4)) : OOB;
    unsigned vg[3], vu[3];
#pragma unroll
    for (int r = 0; r < 3; ++r) {
      const unsigned P = 64 * r + lane, k = (P * 43691u) >> 19, col = 96 * cg + 8 * (P - 12 * k);
      vg[r] = (col < DF) ? (16 * wj + k) * (DF * 2) + col * 2 : OOB; vu[r] = vg[r] ^ 32u;
    }
    const rsrc_t rG = mk_rsrc(wg, D_ * DF * 2), rU = mk_rsrc(wu, D_ * DF * 2);
    const int rot1 = ROT1;
    unsigned dpv;
    asm volatile("buffer_load_dword %0, %1, %2, 0 offen" : "=v"(dpv) : "v"(aa * 4), "s"(mk_rsrc(ssd, D_ * 2)) : "memory");
#pragma unroll
    for (int i = 0; i < 2; ++i) st16_dev(rW, WS_ZT + (unsigned)(2 * aa * 4096 + (2 * wj + i) * 1024 + lane * 16), u32x4{POISON, POISON, POISON, POISON});
    asm volatile("" ::"s"(rG), "s"(rU), "s"(rD), "v"(vx), "v"(vg[0]), "v"(vg[1]), "v"(vg[2]), "v"(vu[0]), "v"(vu[1]), "v"(vu[2]));
    if (tid == 256) lds_st<unsigned>(L0 + L_A, 0u);
    LBAR();
#pragma nounroll
    for (int c = 0; c < 8; ++c) {
      const int cc = (c + rot1) & 7;
      const unsigned sx = cc * 128, sw = cc * (64 * DF * 2), lx = L0 + (c & 3) * SLOT + wj * 1024, lw = L0 + (c & 3) * SLOT + 16384 + wj * 3072;
      dma16(rX, vx, sx, lx);                 dma16(rG, vg[0], sw, lw);        dma16(rU, vu[0], sw, lw + 12288);
      dma16(rX, vx, sx + 32768, lx + 4096);  dma16(rG, vg[1], sw, lw + 1024); dma16(rU, vu[1], sw, lw + 13312);
      if (c >= 2) asm volatile("s_waitcnt vmcnt(16)\n\ts_barrier" ::: "memory");
      if (c == 2) STAMP(16);
      if (c == 3) STAMP(18);
      if (c == 7) STAMP(23);
      if (c == 5 && tid == 256) {
        unsigned lo_, hi_;
        asm volatile("v_add_co_u32 %0, vcc, 1, %2\n\ts_nop 1\n\tv_addc_co_u32 %1, vcc, 0, %3, vcc" : "=&v"(lo_), "=&v"(hi_) : "v"(gen.x), "v"(gen.y) : "vcc");
        st8_dev(rW, WS_FA + (unsigned)(aa * 128), u32x2{lo_, hi_});
      }
      dma16(rX, vx, sx + 65536, lx + 8192);  dma16(rG, vg[2], sw, lw + 2048); dma16(rU, vu[2], sw, lw + 14336);
      dma16(rX, vx, sx + 98304, lx + 12288);
    }
    PIN(gen); PIN(dpv);
    elo = __builtin_amdgcn_readfirstlane(gen.x) + 1u;
    ehi = __builtin_amdgcn_readfirstlane(gen.y) + (elo == 0u ? 1u : 0u);
    const unsigned dpair = __builtin_amdgcn_readfirstlane(dpv);
    const int ce = tid & 255;
    const unsigned vd = VD();
    const unsigned vfa = WS_FA + (unsigned)((32 * (hh ? ct : pan) + l31) * 128), vst = WS_ST + (unsigned)((128 * rt + 64 * (wj & 1) + lane) * 8);
    const u32x4 zero4 = {0u, 0u, 0u, 0u};
    u32x2 stg = ld8_dev(rW, vst), fa = ld8_dev(rW, vfa);
#pragma unroll
    for (int j = 0; j < 4; ++j) WDU(j, L_B1 + wj * 8192 + j * 2048);
    if (ce < 7) lds_st<u32x4>(L0 + L_SY + ce * 16, zero4);
    WAIT_BAR(20);
#pragma unroll
    for (int j = 0; j < 5; ++j) WDU(4 + j, L_B2 + wj * 10240 + j * 2048);
    asm volatile("s_waitcnt vmcnt(18)\n\ts_barrier" : "+v"(stg), "+v"(fa)::"memory");
    STAMP(4);
    stg = spin_tag(rW, vst, stg, elo);
    if (wj < 2) lds_st<unsigned>(L0 + L_ST + (64 * wj + lane) * 4, stg.x);
    spin_flags(rW, vfa, fa, elo, ehi);
    u32x4 hc[2], hp[2], ur[2];
    {
      const rsrc_t rHk = mk_rsrc(ssk, D_ * S_ * 2);
#pragma unroll
      for (int c = 0; c < 2; ++c) {
        hc[c] = ld16(rHk, (unsigned)((2 * aa + c) * 4096 + ce * 16));
        hp[c] = ld16(rHk, ce ? (unsigned)((2 * aa + c) * 4096 + ce * 16 - 16) : OOB);
        ur[c] = ld16_dev(rW, WS_KVT + (unsigned)(2 * aa * 4096 + ce * 16), c * 4096);
      }
    }
    xo = ld16(rX, VXO()); st2 = ld8_dev(rW, VS2());
#pragma unroll
    for (int j = 0; j < 2; ++j) WDU(9 + j, L_B3 + wj * 10240 + j * 2048);
    WAIT_BAR(12);
    STAMP(5);
    PARK();
    {
      asm volatile("s_waitcnt vmcnt(4)" : "+v"(hc[0]), "+v"(hc[1]), "+v"(hp[0]), "+v"(hp[1]), "+v"(ur[0]), "+v"(ur[1]), "+v"(xo), "+v"(st2)::"memory");
      auto put = [&](unsigned hb, u32x4 pv, u32x4 cu, int blk) {
        const unsigned a0 = __builtin_amdgcn_alignbit(pv[1], pv[0], 16), a1 = __builtin_amdgcn_alignbit(pv[2], pv[1], 16),
                       a2 = __builtin_amdgcn_alignbit(pv[3], pv[2], 16), a3 = __builtin_amdgcn_alignbit(cu[0], pv[3], 16),
                       a4 = __builtin_amdgcn_alignbit(cu[1], cu[0], 16);
        lds_st<u32x4>(hb + blk * 16, u32x4{a0, a1, a2, a3});
        lds_st<u32x4>(hb + 4160 + blk * 16, u32x4{pv[1], pv[2], pv[3], cu[0]});
        lds_st<u32x4>(hb + 8320 + blk * 16, u32x4{a1, a2, a3, a4});
        lds_st<u32x4>(hb + 12480 + blk * 16, u32x4{pv[2], pv[3], cu[0], cu[1]});
      };
#pragma unroll
      for (int c = 0; c < 2; ++c) {
        put(L0 + L_CV + c * 16640, hp[c], hc[c], ce + 1);
        if (ce == 0 || ce == 255) put(L0 + L_CV + c * 16640, ce ? hc[c] : zero4, zero4, ce ? 257 : 0);
        lds_st<u32x4>(L0 + (c ? L_U1 : L_U0) + ce * 16, ur[c]);
      }
      asm volatile("s_waitcnt lgkmcnt(0)" ::: "memory");
      if (lane == 0) lds_inc(L0 + L_SY + 28);
    }
#pragma unroll
    for (int j = 2; j < 5; ++j) WDU(9 + j, L_B3 + wj * 10240 + j * 2048);
    lds_wait_ge(L0 + L_SY + 64 + wj * 4, 1u);
#pragma unroll
    for (int j = 0; j < 4; ++j) WDU(14 + j, L_B1 + wj * 8192 + j * 2048);
#pragma unroll
    for (int j = 0; j < 4; ++j) WDU(18 + j, L_B2 + wj * 10240 + j * 2048);
    XN();
    STAMP(7);
    const int vch = wj >> 1, vrh = wj & 1, c15 = lane & 15;
    f32x4 ya[8];
#pragma unroll
    for (int c = 0; c < 8; ++c) { ya[c] = f32x4{0.f, 0.f, 0.f, 0.f}; PIN(ya[c]); }
    unsigned xo_ = L0 + L_CV + vch * 16640 + (vrh ^ 1) * 4096 + lane * 16, xb = L0 + L_CV + vch * 16640 + vrh * 4096 + lane * 16;
    unsigned zs_ = L0 + L_CV + vch * 16640 + vrh * 4096 + c15 * 32 + G * 8, uq_ = L0 + (vch ? L_U1 : L_U0) + (2044 - 768 - 1024 * vrh - 16 * c15 - 4 * G) * 2;
    unsigned vzs = WS_ZT + (unsigned)((2 * aa + vch) * 4096 + vrh * 2048 + lane * 16);
    PIN(xo_); PIN(xb); PIN(zs_); PIN(uq_); PIN(vzs);
    {
      unsigned cA = L0 + L_CV + vch * 16640 + (c15 & 3) * 4160 + (4 * (c15 >> 2) + 8 * G + 128 * vrh) * 2;
      const unsigned ub = L0 + (vch ? L_U1 : L_U0);
      unsigned cB = ub + (240 - 16 * c15 + 8 * G + 128 * vrh) * 2;
      int lim = ((16 * c15 - 8 * G + 47) >> 5) - 4 * vrh;
      PIN(cA); PIN(cB);
      bf16x8 bfr[2][8]; s16x4 ah[2][4];
#define VLD8(a) (*(const volatile LAS s16x4*)(size_t)(a))
#define CA(set, r, p, i) ah[set][i] = VLD8(cA + (r) * 64 + (((i) & 2) ? 7 - (p) : (p)) * 512 + ((i) & 1) * 8)
#define CB(r, mu) bfr[(r) & 1][mu] = lds_ld<bf16x8>((mu) == 0 ? ((r) >= lim ? L0 + L_SY + 32 : cB + (r) * 64 + 3584) : cB + (r) * 64 + (7 - (mu)) * 512)
#define MF16(c, a, b) asm volatile("v_mfma_f32_16x16x32_bf16 %0, %1, %2, %0" : "+v"(c) : "v"(a), "v"(b))
#define CM(r, p, m) do { if ((m) + (p) < 8) MF16(ya[((p) + (m)) & 7], cat(ah[(p) & 1][0], ah[(p) & 1][1]), bfr[(r) & 1][(m) & 7]);  \
        else MF16(ya[((m) - 1) & 7], cat(ah[(p) & 1][2], ah[(p) & 1][3]), bfr[(r) & 1][((m) + (p)) & 7]); } while (0)
#define CKEEP(r, p) asm volatile("" ::"v"(ah[(p) & 1][0]), "v"(ah[(p) & 1][1]), "v"(ah[(p) & 1][2]), "v"(ah[(p) & 1][3]), "v"(bfr[(r) & 1][0]), "v"(bfr[(r) & 1][1]), \
        "v"(bfr[(r) & 1][2]), "v"(bfr[(r) & 1][3]), "v"(bfr[(r) & 1][4]), "v"(bfr[(r) & 1][5]), "v"(bfr[(r) & 1][6]), "v"(bfr[(r) & 1][7]))
#define CPAIR(r, p) do { _Pragma("unroll") for (int m = 0; m < 9; ++m) { CM(r, p, m);                                               \
          if (m < 4) CA(((p) + 1) & 1, (p) < 3 ? (r) : (r) + 1, ((p) + 1) & 3, m); else if (m < 6) CB((r) + 1, 2 * (p) + m - 4); SB(); } CKEEP(r, p); } while (0)
#define CONV(r) do { CPAIR(r, 0); CPAIR(r, 1); CPAIR(r, 2); CPAIR(r, 3); } while (0)
      lds_wait_ge(L0 + L_SY + 28, 4u);
#pragma unroll
      for (int i = 0; i < 4; ++i) CA(0, 0, 0, i);
#pragma unroll
      for (int mu = 0; mu < 8; ++mu) CB(0, mu);
      lds_wait_ge(L0 + L_SY + 24, 4u);
      STAMP(24);
#pragma nounroll
      for (int it = 0; it < 2; ++it) {
        CONV(0); CPAIR(1, 0); CPAIR(1, 1); CPAIR(1, 2);
        if (it) {
          asm volatile("s_waitcnt vmcnt(0) lgkmcnt(0)" ::: "memory"); LDS_SET(L0 + L_SY + 48 + wj * 4, 1u); LDS_SET(L0 + L_SY + wj * 4, 1u); }
        CPAIR(1, 3);
        cA += 128; cB += 128; lim -= 2; PIN(cA); PIN(cB);
      }
    }
    asm volatile("s_nop 7" ::: "memory");
    STAMP(27);
    {
      const unsigned mine = L0 + L_SY + 48 + wj * 4, other = L0 + L_SY + 48 + (wj ^ 1) * 4;
      const float dsc = vch ? bhi(dpair) : blo(dpair);
      u32x2 uq[4];
#pragma unroll
      for (int c = 0; c < 4; ++c) uq[c] = lds_ld<u32x2>(uq_ + (768 - 256 * c) * 2);
      lds_spin_ge(other, 1u);
      f32x4 y[4];
      if (vrh) {
        asm volatile("" ::: "memory");
#pragma unroll
        for (int c = 0; c < 4; ++c) { lds_st<f32x4>(xo_ + c * 1024, ya[c]); y[c] = ya[4 + c]; PIN(y[c]); }
      } else {
#pragma unroll
        for (int c = 0; c < 4; ++c) { lds_st<f32x4>(xo_ + c * 1024, ya[4 + c]); y[c] = ya[c]; PIN(y[c]); }
        asm volatile("" ::: "memory");
      }
      asm volatile("s_waitcnt lgkmcnt(0)" ::: "memory");
      *(volatile LAS unsigned*)(size_t)mine = 2u;
#pragma unroll
      for (int c = 0; c < 4; ++c)
#pragma unroll
        for (int e = 0; e < 4; ++e) {
          const unsigned uu = (e & 2) ? uq[c].x : uq[c].y;
          float s = __builtin_fmaf((e & 1) ? blo(uu) : bhi(uu), dsc, y[c][e]); PIN(s); y[c][e] = s;
        }
      lds_spin_ge(other, 2u);
      f32x4 o[4];
#pragma unroll
      for (int c = 0; c < 4; ++c) o[c] = lds_ld<f32x4>(xb + c * 1024);
      SB();
      u32x2 zp[4];
#pragma unroll
      for (int c = 0; c < 4; ++c) {
        float z[4];
#pragma unroll
        for (int e = 0; e < 4; ++e) { z[e] = y[c][e] + o[c][e]; asm("" : "+v"(z[e])); }
        zp[c] = u32x2{cvtpk(z[0], z[1]), cvtpk(z[2], z[3])};
      }
#pragma unroll
      for (int c = 0; c < 4; ++c) lds_st<u32x2>(zs_ + c * 512, zp[c]);
#pragma unroll
      for (int i = 0; i < 2; ++i)
        st16_dev(rW, vzs + i * 1024, lds_ld<u32x4>(xb + i * 1024));
    }
    STAMP(26);
    if (lane == 0) lds_inc(L0 + L_SY + 20);
    STAMP(9);
    lds_spin_ge(L0 + L_SY + 80 + wj * 4, 1u);
    STAMP(31);
  } else {
    __builtin_amdgcn_s_setprio(3);
    u32x2 sh[2]; u32x4 xr[2];
#pragma unroll
    for (int i = 0; i < 2; ++i) xr[i] = ld16(rX, (unsigned)((8 * aa + 2 * wj + i) * 1024 + lane * 16));
    {
      const rsrc_t rK = mk_rsrc(kin, S_ * D_ * 2), rV = mk_rsrc(vin, S_ * D_ * 2);
      const unsigned kb = (unsigned)((64 * idx + l3) * 1024 + pan * 128), fl = (unsigned)(l7 ^ ((l3 >> 1) & 1));
#pragma unroll
      for (int i = 0; i < 2; ++i) {
        const int g = 2 * wj + i;
        dma16(rK, kb + ((fl ^ (((g + 1) & 3) << 1)) << 4), g * 8192, L0 + L_KA + (g + 1) * 1024);
        dma16(rV, kb + ((fl ^ ((g & 3) << 1)) << 4), g * 8192, L0 + L_VA + g * 1024);
      }
      if (wj == 0) dma16(rK, idx == 0 ? OOB : kb - 8192 + (fl << 4), 0, L0 + L_KA);
    }
    u32x2 gen = ld8_dev(rW, WS_GEN);
    {
      const rsrc_t rSh = mk_rsrc(shk, D_ * 4 * 2);
#pragma unroll
      for (int i = 0; i < 2; ++i) sh[i] = ld8(rSh, (unsigned)((64 * pan + 16 * wj + 8 * i + 4 * (G & 1) + pp) * 8));
    }
    asm volatile("s_barrier" ::: "memory");
    STAMP(3);
    const int tp = wj & 1, ch = wj >> 1;
    f32x16 acc[2][3];
    f32x4 cs[3];
    unsigned ax[4], aw[2][3], ak[5];
    unsigned vkv = WS_KVT + (unsigned)((64 * pan + 16 * wj + l3) * 4096 + (1984 - 64 * idx + 8 * l7) * 2);
    unsigned vst0 = lane < 2 ? WS_ST + (unsigned)((8 * aa + 2 * wj + lane) * 8) : OOB;
    int src4 = 4 * (16 * ((((7 - l7) >> 2) << 1) | (l3 >> 2)) + 4 * ((7 - l7) & 3) + (l3 & 3));
    const unsigned so_ = ((lane & 15) == 4 * (G & 1)) ? 0x3f803f80u : 0u;
    bf16x8 sel = __builtin_bit_cast(bf16x8, u32x4{so_, so_, so_, so_});
    {
      const unsigned ax0 = L0 + (64 * tp + l31) * 128 + (((4 * tp + hh) ^ ((l31 >> 1) & 7)) << 4);
      const unsigned aw0 = L0 + 16384 + (G & 1) * 12288 + (8 * hh + qq) * 192 + ((pp & 1) << 4) + 8 * (pp >> 1);
#pragma unroll
      for (int s = 0; s < 4; ++s) { ax[s] = ax0 ^ (s << 5); PIN(ax[s]); }
#pragma unroll
      for (int i = 0; i < 3; ++i) {
        const unsigned a_ = aw0 + (((3 * ch + i) ^ (G & 1)) << 5);
        aw[0][i] = a_ + tp * 6144; aw[1][i] = a_ + 6144 - tp * 6144; PIN(aw[0][i]); PIN(aw[1][i]);
      }
#pragma unroll
      for (int j = 0; j < 5; ++j) { const int rr = 8 * (4 * (G >> 1) + pp) + qq + (j < 3 ? 4 + 4 * j : 4 * (j - 3));
        ak[j] = L0 + (j < 3 ? L_KA : L_VA) + rr * 128 + (((2 * wj) ^ fsw(rr)) << 4) + 8 * (G & 1); PIN(ak[j]); }
      PIN(vkv); PIN(vst0); PIN(src4); PIN(sel);
    }
    {
      asm volatile("s_waitcnt vmcnt(7)" : "+v"(xr[0]), "+v"(xr[1])::"memory");
      STAMP(14);
      const f32x2 x0 = {blo(__builtin_amdgcn_readfirstlane(xr[0][0])), blo(__builtin_amdgcn_readfirstlane(xr[1][0]))};
      f32x2 d[8];
#pragma unroll
      for (int e = 0; e < 4; ++e) { d[2 * e] = f32x2{blo(xr[0][e]), blo(xr[1][e])} - x0; d[2 * e + 1] = f32x2{bhi(xr[0][e]), bhi(xr[1][e])} - x0; }
      const f32x2 sm = ((d[0] + d[1]) + (d[2] + d[3])) + ((d[4] + d[5]) + (d[6] + d[7]));
      const f32x2 sq = ((d[0] * d[0] + d[1] * d[1]) + (d[2] * d[2] + d[3] * d[3])) + ((d[4] * d[4] + d[5] * d[5]) + (d[6] * d[6] + d[7] * d[7]));
      float s0 = sm[0], s1 = sm[1], q0 = sq[0], q1 = sq[1];
      wave_sum4(s0, s1, q0, q1);
      const float m = (lane ? s1 : s0) * (1.0f / D_), var = (lane ? q1 : q0) * (1.0f / D_) - m * m;
      unsigned ms0 = cvtpk(m + (lane ? x0[1] : x0[0]), __builtin_amdgcn_sqrtf(rbf(rbf(__builtin_fmaxf(var, 0.f)) + 1e-5f)));
      asm volatile("s_waitcnt vmcnt(0)" : "+v"(gen), "+v"(sh[0]), "+v"(sh[1]), "+v"(ms0)::"memory");
      STAMP(15);
      if (lane == 0) lds_inc(L0 + L_A);
      elo = __builtin_amdgcn_readfirstlane(gen.x) + 1u;
      ehi = __builtin_amdgcn_readfirstlane(gen.y) + (elo == 0u ? 1u : 0u);
      st8_dev(rW, vst0, u32x2{ms0, elo});
      f32x2 sk[4] = {{blo(sh[0].x), blo(sh[1].x)}, {bhi(sh[0].x), bhi(sh[1].x)}, {blo(sh[0].y), blo(sh[1].y)}, {bhi(sh[0].y), bhi(sh[1].y)}};
      PIN(sk[0]); PIN(sk[1]); PIN(sk[2]); PIN(sk[3]);
      lds_spin_ge(L0 + L_A, 4u);
      STAMP(1);
      u32x2 t[2][5];
#pragma unroll
      for (int j = 0; j < 5; ++j) { t[0][j] = __builtin_bit_cast(u32x2, trd(ak[j])); t[1][j] = __builtin_bit_cast(u32x2, trd(ak[j] ^ 16)); }
      SB();
      f32x2 kk[20], r[8];
#pragma unroll
      for (int j = 0; j < 5; ++j) {
        kk[4 * j] = f32x2{blo(t[0][j].x), blo(t[1][j].x)}; kk[4 * j + 1] = f32x2{bhi(t[0][j].x), bhi(t[1][j].x)};
        kk[4 * j + 2] = f32x2{blo(t[0][j].y), blo(t[1][j].y)}; kk[4 * j + 3] = f32x2{bhi(t[0][j].y), bhi(t[1][j].y)};
      }
#pragma unroll
      for (int e = 0; e < 8; ++e) r[e] = kk[e + 1] * sk[3];
#pragma unroll
      for (int j = 2; j >= 0; --j)
#pragma unroll
        for (int e = 0; e < 8; ++e) r[e] = __builtin_elementwise_fma(kk[e + 4 - j], sk[j], r[e]);
#pragma unroll
      for (int e = 0; e < 8; ++e) r[e] *= kk[12 + e];
      u32x4 po[2];
#pragma unroll
      for (int i = 0; i < 2; ++i)
#pragma unroll
        for (int e = 0; e < 4; ++e) po[i][e] = (unsigned)__builtin_amdgcn_ds_bpermute(src4, (int)cvtpk(r[7 - 2 * e][i], r[6 - 2 * e][i]));
      st16_dev(rW, vkv, po[0]); st16_dev(rW, vkv + 32768, po[1]);
      __builtin_amdgcn_s_setprio(1);
    }
    STAMP(2);
    {
      bf16x8 xf[2][2]; s16x4 wf[2][3][2];
#define PINA(t, i) PIN(acc[t][i])
#define M1(p, t, i) do { acc[t][i] = MFMA32(cat(wf[p][i][0], wf[p][i][1]), xf[p][t], acc[t][i]); PINA(t, i); } while (0)
#define MS(p, i, on) do { if (on) { cs[i] = MFMA16(sel, cat(wf[p][i][0], wf[p][i][1]), cs[i]); PIN(cs[i]); } } while (0)
#define RW(s, i) do { wf[(s) & 1][i][0] = trd(aw[(s) >> 1][i] + ((s) & 1) * 3072); wf[(s) & 1][i][1] = trd(aw[(s) >> 1][i] + ((s) & 1) * 3072 + 768); } while (0)
#define RX(s, t) xf[(s) & 1][t] = lds_ld<bf16x8>(ax[s] + (t) * 4096)
#define KEEP(p) do { asm volatile("" ::"v"(wf[p][0][0]), "v"(wf[p][0][1]), "v"(wf[p][1][0]), "v"(wf[p][1][1]), "v"(wf[p][2][0]), "v"(wf[p][2][1]), "v"(xf[p][0]), "v"(xf[p][1])); } while (0)
#define ADV(a) do { a += dl; PIN(a); } while (0)
#define STEP(p, s, on) do { M1(p, 0, 0); RW(s, 0); SB(); M1(p, 1, 0); RX(s, 0); RX(s, 1); SB(); M1(p, 0, 1); RW(s, 1); SB();  \
        M1(p, 1, 1); RW(s, 2); SB(); M1(p, 0, 2);                                                                             \
        if ((s) == 3) { ADV(ax[0]); ADV(ax[1]); ADV(ax[2]); ADV(ax[3]); ADV(aw[0][0]); } SB(); M1(p, 1, 2);                     \
        if ((s) == 3) { ADV(aw[0][1]); ADV(aw[0][2]); ADV(aw[1][0]); ADV(aw[1][1]); ADV(aw[1][2]); }                            \
        MS(p, 0, on); MS(p, 1, on); MS(p, 2, on); KEEP(p); SB(); } while (0)
      LBAR();
      STAMP(16);
      RW(0, 0); RX(0, 0); RX(0, 1); RW(0, 1); RW(0, 2); SB();
      unsigned dl = SLOT;
#pragma unroll
      for (int i = 0; i < 3; ++i) {
        cs[i] = f32x4{0.f, 0.f, 0.f, 0.f};
#pragma unroll
        for (int r = 0; r < 16; ++r) { acc[0][i][r] = 0.f; acc[1][i][r] = 0.f; }
      }
      STEP(0, 1, true);
#pragma nounroll
      for (int c = 0; c < 7; ++c) {
        dl = (c == 3) ? 0u - 3u * SLOT : (unsigned)SLOT;
        STEP(1, 2, true);
        if (c == 2) WAITV(0);
        STEP(0, 3, false);
        LBAR();
        STEP(1, 0, false); STEP(0, 1, true);
      }
      STAMP(4);
      STEP(1, 2, true);
#pragma unroll
      for (int i = 0; i < 3; ++i) lds_st<float>(L0 + L_CS + (wj * 3 + i) * 256 + lane * 4, cs[i][0]);
      STEP(0, 3, false);
      LBAR();
      float co[3]; unsigned ms[2];
#pragma unroll
      for (int i = 0; i < 3; ++i) co[i] = lds_ld<float>(L0 + L_CS + ((wj ^ 1) * 3 + i) * 256 + lane * 4);
#pragma unroll
      for (int t = 0; t < 2; ++t) ms[t] = lds_ld<unsigned>(L0 + L_ST + (64 * tp + 32 * t + l31) * 4);
      SB();
      M1(1, 0, 0); PARK1(0); SB(); M1(1, 1, 0); PARK1(1); SB(); M1(1, 0, 1); PARK1(2); SB();
      M1(1, 1, 1); PARK1(3); SB(); M1(1, 0, 2); PARK1(4); SB(); M1(1, 1, 2); PARK1(5); SB();
      float k0[2], k1[2], s2[2];
      u32x4 bm[2];
#pragma unroll
      for (int t = 0; t < 2; ++t) {
        const unsigned mn = (ms[t] & 0xffffu) ^ 0x8000u;
        bm[t] = u32x4{mn | (mn << 16), mn, 0u, 0u};
        const float sd = bhi(ms[t]), rs = __builtin_amdgcn_rcpf(sd);
        k0[t] = rs * -2.3022081983f; k1[t] = k0[t] * rs * rs * 0.044715f; s2[t] = sd * sd;
      }
#pragma unroll
      for (int i = 0; i < 3; ++i) {
        const float c = cs[i][0] + co[i], c1 = c - rbf(c), c2 = c1 - rbf(c1);
        const u32x4 am = {cvtpk(c, c1), cvtpk(c2, 0.f), 0u, 0u};
#pragma unroll
        for (int t = 0; t < 2; ++t) { acc[t][i] = MFMA32(__builtin_bit_cast(bf16x8, am), __builtin_bit_cast(bf16x8, bm[t]), acc[t][i]); PINA(t, i); }
        if (i < 2) { PARK1(6 + i); SB(); }
      }
#pragma unroll
      for (int j = 0; j < 8; ++j) { PIN(wpk[j][0]); PIN(wpk[j][1]); }
      LDS_SET(L0 + L_SY + 64 + wj * 4, 1u);
      STAMP(5);
      const unsigned vhs = WS_H + (unsigned)(((4 * rt + 2 * tp) * 86 + 6 * cg + 3 * ch) * 1024 + lane * 16);
#pragma unroll
      for (int i = 0; i < 3; ++i) {
        const unsigned vo = (vhs + i * 1024) | (6 * cg + 3 * ch + i < 86 ? 0u : OOB);
#pragma unroll
        for (int t = 0; t < 2; ++t) {
          f32x2 g[4], zz[4], gu[4];
#pragma unroll
          for (int r = 0; r < 4; ++r) {
            g[r] = f32x2{acc[t][i][2 * r], acc[t][i][2 * r + 1]};
            zz[r] = g[r] * __builtin_elementwise_fma(g[r] * g[r], f32x2{k1[t], k1[t]}, f32x2{k0[t], k0[t]});
          }
#pragma unroll
          for (int r = 0; r < 4; ++r) {
            zz[r] = f32x2{__builtin_amdgcn_exp2f(zz[r][0]), __builtin_amdgcn_exp2f(zz[r][1])};
            gu[r] = g[r] * f32x2{acc[t][i][8 + 2 * r], acc[t][i][9 + 2 * r]};
          }
          u32x4 hq;
#pragma unroll
          for (int r = 0; r < 4; ++r) {
            const f32x2 dd = __builtin_elementwise_fma(zz[r], f32x2{s2[t], s2[t]}, f32x2{s2[t], s2[t]});
            const f32x2 hv = gu[r] * f32x2{__builtin_amdgcn_rcpf(dd[0]), __builtin_amdgcn_rcpf(dd[1])};
            hq[r] = cvtpk(hv[0], hv[1]);
          }
          st16_dev(rW, vo + t * (86 * 1024), hq);
        }
      }
    }
    if (lane == 0) lds_inc(L0 + L_SY + 24);
    STAMP(6);
    xo = ld16(rX, VXO()); st2 = ld8_dev(rW, VS2());
    WAITV(2);
    if (lane == 0 && lds_inc(L0 + L_SY + 16) == 3u && cg < 15) { u32x2 e = {elo, ehi}; st8_dev(rW, WS_FH + (unsigned)((rt * 16 + cg) * 128), e); }
    STAMP(8);
    if (b == 240 && wj == 0) {
      u32x2 f[4];
#pragma unroll
      for (int i = 0; i < 4; ++i) f[i] = ld8_dev(rW, WS_FA + (unsigned)((64 * i + lane) * 128));
      asm volatile("s_waitcnt vmcnt(0)" : "+v"(f[0]), "+v"(f[1]), "+v"(f[2]), "+v"(f[3]), "+v"(xo), "+v"(st2)::"memory");
#pragma unroll
      for (int i = 0; i < 4; ++i) spin_flags(rW, WS_FA + (unsigned)((64 * i + lane) * 128), f[i], elo, ehi);
      if (lane == 0) { u32x2 e = {elo, ehi}; st8_dev(rW, WS_GEN, e); }
    }
    {
      const int gf = ks0 / 6, gl = (ks0 + 20 + (wj < 2)) / 6, gi = min(gf + l7, gl);
      const unsigned vfh = WS_FH + (unsigned)(((r64 >> 1) * 16 + gi) * 128);
      u32x2 fh = ld8_dev(rW, vfh);
      asm volatile("s_waitcnt vmcnt(1)" : "+v"(xo), "+v"(st2)::"memory");
      XN();
      asm volatile("s_waitcnt vmcnt(0)" : "+v"(fh)::"memory");
      spin_flags(rW, vfh, fh, elo, ehi);
    }
    LDS_SET(L0 + L_SY + 80 + wj * 4, 1u);
  }
  STAMP(10);
  {
    u32x4 hf[22];
    const unsigned vh = WS_H + (unsigned)(((2 * r64 + (w >> 2)) * 86 + ks0) * 1024 + lane * 16);
#define HL(j) hf[j] = ld16_dev(rW, vh, __builtin_amdgcn_readfirstlane(min(PH(j), klast) * 1024))
#pragma unroll
    for (int j = 0; j < HWIN; ++j) HL(j);
    const unsigned vz = WS_ZT + (unsigned)((64 * ct + 8 * w + l3) * 4096 + r64 * 128 + l7 * 16);
    u32x4 zr;
    STAMP(28);
    const unsigned pq = w < 4 ? L0 + L_P0 : L0 + L_B3, ps = w < 4 ? 8192 : 10240;
    unsigned pw[4], pr[4], zw, zt_, zsrc, vo;
#pragma unroll
    for (int i = 0; i < 4; ++i) {
      pw[i] = pq + wj * ps + l31 * 256 + (((hh ^ l7) ^ (2 * i)) << 4); PIN(pw[i]);
      pr[i] = pq + i * ps + (8 * wj + l3) * 256 + (((2 * l7) ^ l3) << 4); PIN(pr[i]);
    }
    { const int r = 8 * w + l3, r2 = 8 * (4 * (G >> 1) + pp) + qq;
      zw = L0 + L_ZT + r * 128 + ((l7 ^ fsw(r)) << 4); zt_ = L0 + L_ZT + r2 * 128 + ((w ^ fsw(r2)) << 4) + 8 * (G & 1);
      zsrc = 4 * (16 * (((l7 >> 2) << 1) | (l3 >> 2)) + 4 * (l7 & 3) + (l3 & 3)); vo = VXO(); PIN(zw); PIN(zt_); PIN(zsrc); PIN(vo); }
    lds_wait_ge(L0 + L_SY + wj * 4, 1u);
    const unsigned a1 = AD() + L_B1 + wj * 8192, a2 = AD() + L_B2 + wj * 10240;
    f32x16 fo[2];
#pragma unroll
    for (int r = 0; r < 16; ++r) { fo[0][r] = 0.f; fo[1][r] = 0.f; }
#define G2(j) do { if ((j) + 2 >= 8 && (j) + 2 < 22) { wpk[((j) + 2) & 7][0] = WDF(UA((j) + 2), 0); wpk[((j) + 2) & 7][1] = WDF(UA((j) + 2), 1); } \
      asm volatile("s_waitcnt vmcnt(%1)" : "+v"(hf[j]) : "i"((j) + HWIN <= 22 ? HWIN - 1 : (j) <= ZAT ? 21 - (j) : 22 - (j)) : "memory"); \
      fo[0] = MFMA32(wpk[(j) & 7][0], __builtin_bit_cast(bf16x8, hf[j]), fo[0]);                                      \
      fo[1] = MFMA32(wpk[(j) & 7][1], __builtin_bit_cast(bf16x8, hf[j]), fo[1]); PIN(fo[0]); PIN(fo[1]); SB();      \
      if ((j) + HWIN < 22) HL(((j) + HWIN) % 22); if ((j) == ZAT) zr = ld16_dev(rW, vz, 0); } while (0)
    G2(0); STAMP(29); G2(1); G2(2); G2(3); G2(4); G2(5); G2(6); G2(7); G2(8); G2(9); G2(10);
    G2(11); G2(12); G2(13);
    if (w < 4) LDS_SET(L0 + L_SY + 96 + wj * 4, 1u);
    G2(14); G2(15); G2(16); G2(17); G2(18);
    lds_spin_ge(L0 + L_SY + (w < 4 ? 20 : 96 + wj * 4), w < 4 ? 4u : 1u);
    G2(19); G2(20); G2(21);
    STAMP(11);
#pragma unroll
    for (int dt = 0; dt < 2; ++dt)
#pragma unroll
      for (int i = 0; i < 4; ++i)
        lds_st<f32x4>(pw[i] + dt * 128, f32x4{fo[dt][4 * i], fo[dt][4 * i + 1], fo[dt][4 * i + 2], fo[dt][4 * i + 3]});
    asm volatile("s_waitcnt vmcnt(0)" : "+v"(zr)::"memory");
    for (unsigned n = 0; __builtin_expect(__any(max(max(zr[0], zr[1]), max(zr[2], zr[3])) == POISON) && n < SPIN_MAX, 0); ++n) {
      zr = ld16_dev(rW, vz, 0);
      asm volatile("s_waitcnt vmcnt(0)" : "+v"(zr)::"memory");
    }
    STAMP(30);
    if (w >= 4) lds_spin_ge(L0 + L_SY + 20, 4u);
    lds_st<u32x4>(zw, zr);
    LBAR();
    STAMP(12);
    const u32x2 zt0 = __builtin_bit_cast(u32x2, trd(zt_)), zt1 = __builtin_bit_cast(u32x2, trd(zt_ + 512));
    f32x4 pa[4], pb[4];
#pragma unroll
    for (int k = 0; k < 4; ++k) { pa[k] = lds_ld<f32x4>(pr[k]); pb[k] = lds_ld<f32x4>(pr[k] ^ 16); }
    SB();
    const unsigned zq[4] = {(unsigned)__builtin_amdgcn_ds_bpermute(zsrc, (int)zt0.x), (unsigned)__builtin_amdgcn_ds_bpermute(zsrc, (int)zt0.y),
                            (unsigned)__builtin_amdgcn_ds_bpermute(zsrc, (int)zt1.x), (unsigned)__builtin_amdgcn_ds_bpermute(zsrc, (int)zt1.y)};
    const f32x4 p0 = (pa[0] + pa[1]) + (pa[2] + pa[3]), p1 = (pb[0] + pb[1]) + (pb[2] + pb[3]);
    u32x4 ov;
#pragma unroll
    for (int i = 0; i < 4; ++i) {
      const f32x2 F = i < 2 ? f32x2{p0[2 * i], p0[2 * i + 1]} : f32x2{p1[2 * i - 4], p1[2 * i - 3]};
      const f32x2 t = F * f32x2{-1.4426950408889634f, -1.4426950408889634f};
      const f32x2 d = f32x2{__builtin_amdgcn_exp2f(t[0]), __builtin_amdgcn_exp2f(t[1])} + f32x2{1.0f, 1.0f};
      const f32x2 g = F * f32x2{__builtin_amdgcn_rcpf(d[0]), __builtin_amdgcn_rcpf(d[1])};
      const f32x2 o = __builtin_elementwise_fma(xn[i] * f32x2{blo(zq[i]), bhi(zq[i])}, g, f32x2{blo(xo[i]), bhi(xo[i])});
      ov[i] = cvtpk(o[0], o[1]);
    }
    *(GAS u32x4*)((GAS char*)out + vo) = ov;
  }
}

extern "C" void kernel_launch(void* const* d_in, const int* in_sizes, int n_in, void* d_out, int out_size, void* d_ws, size_t ws_size,
                              hipStream_t stream) {
  (void)in_sizes; (void)n_in; (void)out_size;
  (void)hipFuncSetAttribute((const void*)h3_geglu_fused, hipFuncAttributeMaxDynamicSharedMemorySize, LDS_BYTES);
  h3_geglu_fused<<<256, 512, LDS_BYTES, stream>>>((const u16*)d_in[0], (const u16*)d_in[1], (const u16*)d_in[2], (const u16*)d_in[3],
                                                  (const u16*)d_in[4], (const u16*)d_in[5], (const u16*)d_in[7], (const u16*)d_in[8],
                                                  (const u16*)d_in[9], (u16*)d_out, (char*)d_ws + WS_BASE(ws_size));
}
```

```cpp
#define OUTAUX 16
#define S0EARLY 0
#include <hip/hip_runtime.h>
#include <hip/hip_bf16.h>

typedef unsigned short u16;
typedef unsigned long long u64;
typedef __attribute__((ext_vector_type(8))) short bf16x8;
typedef __attribute__((ext_vector_type(4))) short s16x4;
typedef __attribute__((ext_vector_type(2))) __bf16 bf16x2_t;
typedef __attribute__((ext_vector_type(2))) float f32x2;
typedef __attribute__((ext_vector_type(4))) float f32x4;
typedef __attribute__((ext_vector_type(16))) float f32x16;
typedef __attribute__((ext_vector_type(4))) unsigned u32x4;
typedef __attribute__((ext_vector_type(2))) unsigned u32x2;
#define LAS __attribute__((address_space(3)))
#define GAS __attribute__((address_space(1)))
typedef __amdgpu_buffer_rsrc_t rsrc_t;

constexpr int S_ = 2048, D_ = 512, DF = 1376;
constexpr unsigned WS_GEN = 0, WS_FA = 4096, WS_FH = WS_FA + 256 * 128, WS_FZ = WS_FH + 256 * 128;
constexpr unsigned WS_ST = WS_FZ + 256 * 128;
constexpr unsigned WS_KVT = 1u << 20;
constexpr unsigned WS_ZT = WS_KVT + (2u << 20);
constexpr unsigned WS_H = WS_ZT + (2u << 20);
constexpr unsigned WS_BYTES = WS_H + 64 * 86 * 1024;
constexpr int SLOT = 40960;
constexpr int L_CV = 3 * SLOT;
constexpr int L_KA = L_CV, L_VA = L_KA + 9216;
constexpr int L_B1 = 0, L_B2 = SLOT, L_B3 = 2 * SLOT;
constexpr int L_CS = 32768;
constexpr int L_ST = L_CS + 3072;
constexpr int L_SY = L_ST + 512;
constexpr int L_A = L_VA + 8192;
constexpr int L_P0 = L_CV + 8192;
constexpr int L_U0 = 36864, L_U1 = L_CV + 33280, L_ZT = L_CV;
constexpr int LDS_BYTES = 163840;
constexpr unsigned OOB = 0x80000000u;
constexpr unsigned POISON = 0xffffffffu;
constexpr unsigned SPIN_MAX = 1u << 18;

#define LPRIO 2
#define XSLEEP 4
#define ROT1 ((rt + cg) & 7)
#define ROT2 (((xcd * 4 + (idx >> 3)) * 11) >> 4)
#define HWIN 10
#define STEPMODE 1
#define NOSTATS 0
#define WS_BASE(sz) 0
#define ZSLEEP 4
#define ABL_PLAIN 0
#define ZAT 15
#define SETUPPRIO 2
#define MFMA32(a, b, c) __builtin_amdgcn_mfma_f32_32x32x16_bf16(a, b, c, 0, 0, 0)
#define MFMA16(a, b, c) __builtin_amdgcn_mfma_f32_16x16x32_bf16(a, b, c, 0, 0, 0)
#define WAIT_BAR(N) asm volatile("s_waitcnt vmcnt(" #N ") lgkmcnt(0)\n\ts_barrier" ::: "memory")
#define LBAR() asm volatile("s_waitcnt lgkmcnt(0)\n\ts_barrier" ::: "memory")
#define WAITV(N) asm volatile("s_waitcnt vmcnt(" #N ")" ::: "memory")
#define SB() __builtin_amdgcn_sched_barrier(0)

__device__ __forceinline__ rsrc_t mk_rsrc(const void* p, unsigned bytes) {
  return __builtin_amdgcn_make_buffer_rsrc((void*)p, (short)0, (int)bytes, 0x00020000);
}
__device__ __forceinline__ void dma16(rsrc_t rs, unsigned voff, unsigned soff, unsigned lds) {
  asm volatile("s_mov_b32 m0, %0\n\ts_nop 0\n\tbuffer_load_dwordx4 %1, %2, %3 offen lds" ::"s"(lds), "v"(voff), "s"(rs), "s"(soff) : "memory");
}
__device__ __forceinline__ u32x4 ld16(rsrc_t rs, unsigned voff) {
  u32x4 r; asm volatile("buffer_load_dwordx4 %0, %1, %2, 0 offen" : "=v"(r) : "v"(voff), "s"(rs) : "memory"); return r;
}
__device__ __forceinline__ u32x4 ld16_dev(rsrc_t rs, unsigned voff, unsigned soff) {
  u32x4 r; asm volatile("buffer_load_dwordx4 %0, %1, %2, %3 offen sc1" : "=v"(r) : "v"(voff), "s"(rs), "s"(soff) : "memory"); return r;
}
__device__ __forceinline__ u32x2 ld8(rsrc_t rs, unsigned voff) {
  u32x2 r; asm volatile("buffer_load_dwordx2 %0, %1, %2, 0 offen" : "=v"(r) : "v"(voff), "s"(rs) : "memory"); return r;
}
__device__ __forceinline__ u32x2 ld8_dev(rsrc_t rs, unsigned voff) {
  u32x2 r; asm volatile("buffer_load_dwordx2 %0, %1, %2, 0 offen sc1" : "=v"(r) : "v"(voff), "s"(rs) : "memory"); return r;
}
#define PIN(x) asm volatile("" : "+v"(x))
__device__ __forceinline__ void st16_dev(rsrc_t rs, unsigned voff, u32x4 v) { __builtin_amdgcn_raw_buffer_store_b128(v, rs, voff, 0, 16); }
__device__ __forceinline__ void st8_dev(rsrc_t rs, unsigned voff, u32x2 v) { __builtin_amdgcn_raw_buffer_store_b64(v, rs, voff, 0, 16); }

__device__ __forceinline__ unsigned cvtpk(float lo, float hi) {
  bf16x2_t r = {(__bf16)lo, (__bf16)hi};
  return __builtin_bit_cast(unsigned, r);
}
__device__ __forceinline__ float blo(unsigned w) { return __uint_as_float(w << 16); }
__device__ __forceinline__ float bhi(unsigned w) { return __uint_as_float(w & 0xffff0000u); }
__device__ __forceinline__ float rbf(float f) { return blo(cvtpk(f, f)); }
__device__ __forceinline__ s16x4 trd(unsigned a) { return __builtin_amdgcn_ds_read_tr16_b64_v4i16((LAS s16x4*)(size_t)a); }
__device__ __forceinline__ bf16x8 cat(s16x4 a, s16x4 b) { return __builtin_shufflevector(a, b, 0, 1, 2, 3, 4, 5, 6, 7); }
template <class T> __device__ __forceinline__ T lds_ld(unsigned a) { return *(const LAS T*)(size_t)a; }
template <class T> __device__ __forceinline__ void lds_st(unsigned a, T v) { *(LAS T*)(size_t)a = v; }
template <int CTRL> __device__ __forceinline__ float dpp_add(float x) {
  return x + __uint_as_float(__builtin_amdgcn_update_dpp(0u, __float_as_uint(x), CTRL, 0xf, 0xf, true));
}
__device__ __forceinline__ float swap_add(float x, bool half) {
  if (half) { auto r = __builtin_amdgcn_permlane32_swap(__float_as_uint(x), __float_as_uint(x), false, false); return __uint_as_float(r[0]) + __uint_as_float(r[1]); }
  auto r = __builtin_amdgcn_permlane16_swap(__float_as_uint(x), __float_as_uint(x), false, false); return __uint_as_float(r[0]) + __uint_as_float(r[1]);
}
__device__ __forceinline__ void wave_sum4(float& a, float& b, float& c, float& d) {
  a = dpp_add<0x128>(a); b = dpp_add<0x128>(b); c = dpp_add<0x128>(c); d = dpp_add<0x128>(d);
  a = dpp_add<0x124>(a); b = dpp_add<0x124>(b); c = dpp_add<0x124>(c); d = dpp_add<0x124>(d);
  a = dpp_add<0x122>(a); b = dpp_add<0x122>(b); c = dpp_add<0x122>(c); d = dpp_add<0x122>(d);
  a = dpp_add<0x121>(a); b = dpp_add<0x121>(b); c = dpp_add<0x121>(c); d = dpp_add<0x121>(d);
  a = swap_add(a, false); b = swap_add(b, false); c = swap_add(c, false); d = swap_add(d, false);
  a = swap_add(a, true); b = swap_add(b, true); c = swap_add(c, true); d = swap_add(d, true);
}
__device__ __forceinline__ void lds_wait_ge(unsigned a, unsigned v) {
  for (unsigned n = 0; *(volatile LAS unsigned*)(size_t)a < v && n < SPIN_MAX * 16; ++n) __builtin_amdgcn_s_sleep(1);
  asm volatile("" ::: "memory");
}
__device__ __forceinline__ void lds_spin_ge(unsigned a, unsigned v) {
  for (unsigned n = 0; *(volatile LAS unsigned*)(size_t)a < v && n < SPIN_MAX * 64; ++n) {}
  asm volatile("" ::: "memory");
}
__device__ __forceinline__ unsigned lds_inc(unsigned a) {
  return __hip_atomic_fetch_add((LAS unsigned*)(size_t)a, 1u, __ATOMIC_RELAXED, __HIP_MEMORY_SCOPE_WORKGROUP);
}
__device__ __forceinline__ int fsw(int r) { return ((r >> 1) & 1) | (((r >> 3) & 3) << 1); }
__device__ __forceinline__ void spin_flags(rsrc_t rs, unsigned voff, u32x2 f, unsigned lo, unsigned hi) {
  for (unsigned n = 0; !__all(f.x == lo && f.y == hi) && n < SPIN_MAX; ++n) {
    __builtin_amdgcn_s_sleep(1);
    f = ld8_dev(rs, voff); WAITV(0); PIN(f);
  }
}
__device__ __forceinline__ u32x2 spin_tag(rsrc_t rs, unsigned voff, u32x2 f, unsigned tag) {
  for (unsigned n = 0; !__all(f.y == tag) && n < SPIN_MAX; ++n) {
    __builtin_amdgcn_s_sleep(1);
    f = ld8_dev(rs, voff); WAITV(0); PIN(f);
  }
  return f;
}
#define STAMP(i)

__global__ __launch_bounds__(512, 2) void h3_geglu_fused(const u16* __restrict__ x, const u16* __restrict__ kin, const u16* __restrict__ vin,
                                                         const u16* __restrict__ shk, const u16* __restrict__ ssk, const u16* __restrict__ ssd,
                                                         const u16* __restrict__ wg, const u16* __restrict__ wu, const u16* __restrict__ wd,
                                                         u16* __restrict__ out, char* __restrict__ ws) {
  extern __shared__ __attribute__((aligned(16))) char smem[];
  const unsigned L0 = (unsigned)(size_t)(LAS char*)smem;
  const int tid = threadIdx.x, lane = tid & 63;
  const int w = __builtin_amdgcn_readfirstlane(tid >> 6), wj = w & 3;
  STAMP(0);
  const int b = blockIdx.x, xcd = b & 7, idx = b >> 3, rh = xcd & 1, q4 = xcd >> 1;
  const int pan = 4 * rh + q4, aa = 32 * pan + idx;
  const int l3 = lane >> 3, l7 = lane & 7, G = lane >> 4, qq = (lane >> 2) & 3, pp = lane & 3, hh = lane >> 5, l31 = lane & 31;
  const rsrc_t rX = mk_rsrc(x, S_ * D_ * 2), rW = mk_rsrc(ws, WS_BYTES), rD = mk_rsrc(wd, DF * D_ * 2);
  const int cg = idx < 24 ? ((q4 * 7 + 1) >> 1) + (idx >> 3) : idx < 28 ? (q4 < 2 ? 3 : 10) : 14 + ((idx >> 1) & 1);
  const int rt = 8 * rh + (idx < 24 ? (idx & 7) : idx < 28 ? (q4 & 1) * 4 + (idx & 3) : 2 * q4 + (idx & 1));
  const int r64 = 4 * pan + (idx >> 3), ct = idx & 7;
  const int ks0 = 22 * wj - (wj == 3 ? 1 : 0);
  u32x4 xo; u32x2 st2;
  unsigned elo, ehi;
#define VD() (unsigned)((16 * ks0 + l3) * 1024 + ct * 128 + ((l7 ^ (((l3 >> 1) & 1) << 2)) << 4))
  const int rot2 = ROT2;
#define PH(j) ((j) + rot2 >= 22 ? (j) + rot2 - 22 : (j) + rot2)
  const int klast = wj < 2 ? 21 : 20;
#define WDU(j, lds) do { const unsigned v_ = vd | (PH(j) > klast ? OOB : 0u);                                       \
    dma16(rD, v_, PH(j) * 16384, L0 + (lds)); dma16(rD, v_, PH(j) * 16384 + 8192, L0 + (lds) + 1024); } while (0)
#define VXO() (unsigned)((64 * r64 + 8 * w + l3) * 1024 + ct * 128 + l7 * 16)
#define VS2() (WS_ST + (unsigned)((64 * r64 + 8 * w + l3) * 8))

  bf16x8 wpk[8][2];
#define AD() (L0 + (8 * hh + qq) * 128 + ((4 * ((qq >> 1) & 1) + 2 * (G & 1) + (pp >> 1)) << 4) + 8 * (pp & 1))
#define WDF(base, dt) cat(trd(((base) ^ ((dt) * 64))), trd(((base) ^ ((dt) * 64)) + 512))
#define UA(j) ((j) < 4 ? a1 + (j) * 2048 : (j) < 9 ? a2 + ((j) - 4) * 2048 : (j) < 14 ? a2 + SLOT + ((j) - 9) * 2048 : (j) < 18 ? a1 + ((j) - 14) * 2048 : a2 + ((j) - 18) * 2048)
#define PARK() do { const unsigned a1 = AD() + L_B1 + wj * 8192, a2 = AD() + L_B2 + wj * 10240;                         \
    _Pragma("unroll") for (int j = 0; j < 8; ++j) { wpk[j][0] = WDF(UA(j), 0); wpk[j][1] = WDF(UA(j), 1); }              \
    _Pragma("unroll") for (int j = 0; j < 8; ++j) { PIN(wpk[j][0]); PIN(wpk[j][1]); } } while (0)
  f32x2 xn[4];
#define XN() do { st2 = spin_tag(rW, VS2(), st2, elo); const float mean_ = blo(st2.x), rs_ = __builtin_amdgcn_rcpf(bhi(st2.x));       \
    _Pragma("unroll") for (int i = 0; i < 4; ++i) { float a_ = (blo(xo[i]) - mean_) * rs_, b_ = (bhi(xo[i]) - mean_) * rs_; PIN(a_); PIN(b_); xn[i] = f32x2{a_, b_}; } } while (0)
#define LDS_SET(a, v) do { asm volatile("" ::: "memory"); *(volatile LAS unsigned*)(size_t)(a) = (v); } while (0)

  if (w >= 4) {
    __builtin_amdgcn_s_setprio(2);
    u32x2 gen = ld8_dev(rW, WS_GEN);
    const unsigned vx = (cg < 15) ? (unsigned)((128 * rt + 8 * wj + l3) * 1024 + ((l7 ^ ((4 * wj + (l3 >> 1)) & 7)) << 4)) : OOB;
    unsigned vg[3], vu[3];
#pragma unroll
    for (int r = 0; r < 3; ++r) {
      const unsigned P = 64 * r + lane, k = (P * 43691u) >> 19, col = 96 * cg + 8 * (P - 12 * k);
      vg[r] = (col < DF) ? (16 * wj + k) * (DF * 2) + col * 2 : OOB; vu[r] = vg[r] ^ 32u;
    }
    const rsrc_t rG = mk_rsrc(wg, D_ * DF * 2), rU = mk_rsrc(wu, D_ * DF * 2);
    const int rot1 = ROT1;
    unsigned dpv;
    asm volatile("buffer_load_dword %0, %1, %2, 0 offen" : "=v"(dpv) : "v"(aa * 4), "s"(mk_rsrc(ssd, D_ * 2)) : "memory");
#pragma unroll
    for (int i = 0; i < 2; ++i) st16_dev(rW, WS_ZT + (unsigned)(2 * aa * 4096 + (2 * wj + i) * 1024 + lane * 16), u32x4{POISON, POISON, POISON, POISON});
    asm volatile("" ::"s"(rG), "s"(rU), "s"(rD), "v"(vx), "v"(vg[0]), "v"(vg[1]), "v"(vg[2]), "v"(vu[0]), "v"(vu[1]), "v"(vu[2]));
    if (tid == 256) lds_st<unsigned>(L0 + L_A, 0u);
#pragma nounroll
    for (int c = 0; c < 8; ++c) {
      const int cc = (c + rot1) & 7;
      const unsigned sx = cc * 128, sw = cc * (64 * DF * 2), lx = L0 + (c & 3) * SLOT + wj * 1024, lw = L0 + (c & 3) * SLOT + 16384 + wj * 3072;
      dma16(rX, vx, sx, lx);                 dma16(rG, vg[0], sw, lw);        dma16(rU, vu[0], sw, lw + 12288);
      dma16(rX, vx, sx + 32768, lx + 4096);  dma16(rG, vg[1], sw, lw + 1024); dma16(rU, vu[1], sw, lw + 13312);
      if (c == 0) LBAR();
      if (c >= 2) asm volatile("s_waitcnt vmcnt(16)\n\ts_barrier" ::: "memory");
      if (c == 2) STAMP(16);
      if (c == 3) STAMP(18);
      if (c == 7) STAMP(23);
      if (c == 5 && tid == 256) {
        unsigned lo_, hi_;
        asm volatile("v_add_co_u32 %0, vcc, 1, %2\n\ts_nop 1\n\tv_addc_co_u32 %1, vcc, 0, %3, vcc" : "=&v"(lo_), "=&v"(hi_) : "v"(gen.x), "v"(gen.y) : "vcc");
        st8_dev(rW, WS_FA + (unsigned)(aa * 128), u32x2{lo_, hi_});
      }
      dma16(rX, vx, sx + 65536, lx + 8192);  dma16(rG, vg[2], sw, lw + 2048); dma16(rU, vu[2], sw, lw + 14336);
      dma16(rX, vx, sx + 98304, lx + 12288);
    }
    PIN(gen); PIN(dpv);
    elo = __builtin_amdgcn_readfirstlane(gen.x) + 1u;
    ehi = __builtin_amdgcn_readfirstlane(gen.y) + (elo == 0u ? 1u : 0u);
    const unsigned dpair = __builtin_amdgcn_readfirstlane(dpv);
    const int ce = tid & 255;
    const unsigned vd = VD();
    const unsigned vfa = WS_FA + (unsigned)((32 * (hh ? ct : pan) + l31) * 128), vst = WS_ST + (unsigned)((128 * rt + 64 * (wj & 1) + lane) * 8);
    const u32x4 zero4 = {0u, 0u, 0u, 0u};
    u32x2 stg = ld8_dev(rW, vst), fa = ld8_dev(rW, vfa);
#pragma unroll
    for (int j = 0; j < 4; ++j) WDU(j, L_B1 + wj * 8192 + j * 2048);
    if (ce < 7) lds_st<u32x4>(L0 + L_SY + ce * 16, zero4);
    WAIT_BAR(20);
#pragma unroll
    for (int j = 0; j < 5; ++j) WDU(4 + j, L_B2 + wj * 10240 + j * 2048);
    asm volatile("s_waitcnt vmcnt(18)\n\ts_barrier" : "+v"(stg), "+v"(fa)::"memory");
    STAMP(4);
    stg = spin_tag(rW, vst, stg, elo);
    if (wj < 2) lds_st<unsigned>(L0 + L_ST + (64 * wj + lane) * 4, stg.x);
    spin_flags(rW, vfa, fa, elo, ehi);
    u32x4 hc[2], hp[2], ur[2];
    {
      const rsrc_t rHk = mk_rsrc(ssk, D_ * S_ * 2);
#pragma unroll
      for (int c = 0; c < 2; ++c) {
        hc[c] = ld16(rHk, (unsigned)((2 * aa + c) * 4096 + ce * 16));
        hp[c] = ld16(rHk, ce ? (unsigned)((2 * aa + c) * 4096 + ce * 16 - 16) : OOB);
        ur[c] = ld16_dev(rW, WS_KVT + (unsigned)(2 * aa * 4096 + ce * 16), c * 4096);
      }
    }
    xo = ld16(rX, VXO()); st2 = ld8_dev(rW, VS2());
#pragma unroll
    for (int j = 0; j < 2; ++j) WDU(9 + j, L_B3 + wj * 10240 + j * 2048);
    WAIT_BAR(12);
    STAMP(5);
#pragma unroll
    for (int j = 2; j < 5; ++j) WDU(9 + j, L_B3 + wj * 10240 + j * 2048);
    __builtin_amdgcn_s_setprio(SETUPPRIO);
    {
      asm volatile("s_waitcnt vmcnt(10)" : "+v"(hc[0]), "+v"(hc[1]), "+v"(hp[0]), "+v"(hp[1]), "+v"(ur[0]), "+v"(ur[1]), "+v"(xo), "+v"(st2)::"memory");
      auto put = [&](unsigned hb, u32x4 pv, u32x4 cu, int blk) {
        const unsigned a0 = __builtin_amdgcn_alignbit(pv[1], pv[0], 16), a1 = __builtin_amdgcn_alignbit(pv[2], pv[1], 16),
                       a2 = __builtin_amdgcn_alignbit(pv[3], pv[2], 16), a3 = __builtin_amdgcn_alignbit(cu[0], pv[3], 16),
                       a4 = __builtin_amdgcn_alignbit(cu[1], cu[0], 16);
        lds_st<u32x4>(hb + blk * 16, u32x4{a0, a1, a2, a3});
        lds_st<u32x4>(hb + 4160 + blk * 16, u32x4{pv[1], pv[2], pv[3], cu[0]});
        lds_st<u32x4>(hb + 8320 + blk * 16, u32x4{a1, a2, a3, a4});
        lds_st<u32x4>(hb + 12480 + blk * 16, u32x4{pv[2], pv[3], cu[0], cu[1]});
      };
#pragma unroll
      for (int c = 0; c < 2; ++c) {
        put(L0 + L_CV + c * 16640, hp[c], hc[c], ce + 1);
        if (ce == 0 || ce == 255) put(L0 + L_CV + c * 16640, ce ? hc[c] : zero4, zero4, ce ? 257 : 0);
        lds_st<u32x4>(L0 + (c ? L_U1 : L_U0) + ce * 16, ur[c]);
      }
      asm volatile("s_waitcnt lgkmcnt(0)" ::: "memory");
      if (lane == 0) lds_inc(L0 + L_SY + 24);
    }
    PARK();
    LDS_SET(L0 + L_SY + 64 + wj * 4, 1u);
    XN();
    if (b == 0 && wj == 0) {
      u32x2 f[4];
#pragma unroll
      for (int i = 0; i < 4; ++i) f[i] = ld8_dev(rW, WS_FA + (unsigned)((64 * i + lane) * 128));
      asm volatile("s_waitcnt vmcnt(0)" : "+v"(f[0]), "+v"(f[1]), "+v"(f[2]), "+v"(f[3])::"memory");
#pragma unroll
      for (int i = 0; i < 4; ++i) spin_flags(rW, WS_FA + (unsigned)((64 * i + lane) * 128), f[i], elo, ehi);
      if (lane == 0) { u32x2 e = {elo, ehi}; st8_dev(rW, WS_GEN, e); }
    }
    STAMP(7);
    const int vch = wj >> 1, vrh = wj & 1, c15 = lane & 15;
    f32x4 ya[8];
#pragma unroll
    for (int c = 0; c < 8; ++c) { ya[c] = f32x4{0.f, 0.f, 0.f, 0.f}; PIN(ya[c]); }
    unsigned xo_ = L0 + L_CV + vch * 16640 + (vrh ^ 1) * 4096 + lane * 16, xb = L0 + L_CV + vch * 16640 + vrh * 4096 + lane * 16;
    unsigned zs_ = L0 + L_CV + vch * 16640 + vrh * 4096 + c15 * 32 + G * 8, uq_ = L0 + (vch ? L_U1 : L_U0) + (2044 - 768 - 1024 * vrh - 16 * c15 - 4 * G) * 2;
    unsigned vzs = WS_ZT + (unsigned)((2 * aa + vch) * 4096 + vrh * 2048 + lane * 16);
    PIN(xo_); PIN(xb); PIN(zs_); PIN(uq_); PIN(vzs);
    {
      unsigned cA = L0 + L_CV + vch * 16640 + (c15 & 3) * 4160 + (4 * (c15 >> 2) + 8 * G + 128 * vrh) * 2;
      const unsigned ub = L0 + (vch ? L_U1 : L_U0);
      unsigned cB = ub + (240 - 16 * c15 + 8 * G + 128 * vrh) * 2;
      int lim = ((16 * c15 - 8 * G + 47) >> 5) - 4 * vrh;
      PIN(cA); PIN(cB);
      bf16x8 bfr[2][8]; s16x4 ah[2][4];
#define VLD8(a) (*(const volatile LAS s16x4*)(size_t)(a))
#define CA(set, r, p, i) ah[set][i] = VLD8(cA + (r) * 64 + (((i) & 2) ? 7 - (p) : (p)) * 512 + ((i) & 1) * 8)
#define CB(r, mu) bfr[(r) & 1][mu] = lds_ld<bf16x8>((mu) == 0 ? ((r) >= lim ? L0 + L_SY + 32 : cB + (r) * 64 + 3584) : cB + (r) * 64 + (7 - (mu)) * 512)
#define MF16(c, a, b) asm volatile("v_mfma_f32_16x16x32_bf16 %0, %1, %2, %0" : "+v"(c) : "v"(a), "v"(b))
#define CM(r, p, m) do { if ((m) + (p) < 8) MF16(ya[((p) + (m)) & 7], cat(ah[(p) & 1][0], ah[(p) & 1][1]), bfr[(r) & 1][(m) & 7]);  \
        else MF16(ya[((m) - 1) & 7], cat(ah[(p) & 1][2], ah[(p) & 1][3]), bfr[(r) & 1][((m) + (p)) & 7]); } while (0)
#define CKEEP(r, p) asm volatile("" ::"v"(ah[(p) & 1][0]), "v"(ah[(p) & 1][1]), "v"(ah[(p) & 1][2]), "v"(ah[(p) & 1][3]), "v"(bfr[(r) & 1][0]), "v"(bfr[(r) & 1][1]), \
        "v"(bfr[(r) & 1][2]), "v"(bfr[(r) & 1][3]), "v"(bfr[(r) & 1][4]), "v"(bfr[(r) & 1][5]), "v"(bfr[(r) & 1][6]), "v"(bfr[(r) & 1][7]))
#define CPAIR(r, p) do { _Pragma("unroll") for (int m = 0; m < 9; ++m) { CM(r, p, m);                                               \
          if (m < 4) CA(((p) + 1) & 1, (p) < 3 ? (r) : (r) + 1, ((p) + 1) & 3, m); else if (m < 6) CB((r) + 1, 2 * (p) + m - 4); SB(); } CKEEP(r, p); } while (0)
#define CONV(r) do { CPAIR(r, 0); CPAIR(r, 1); CPAIR(r, 2); CPAIR(r, 3); } while (0)
      lds_wait_ge(L0 + L_SY + 24, 8u); __builtin_amdgcn_s_setprio(2);
      STAMP(24);
#pragma unroll
      for (int i = 0; i < 4; ++i) CA(0, 0, 0, i);
#pragma unroll
      for (int mu = 0; mu < 8; ++mu) CB(0, mu);
#pragma nounroll
      for (int it = 0; it < 2; ++it) {
        CONV(0); CPAIR(1, 0); CPAIR(1, 1); CPAIR(1, 2);
        if (it) {
          asm volatile("s_waitcnt vmcnt(0) lgkmcnt(0)" ::: "memory"); LDS_SET(L0 + L_SY + 48 + wj * 4, 1u); LDS_SET(L0 + L_SY + wj * 4, 1u); }
        CPAIR(1, 3);
        cA += 128; cB += 128; lim -= 2; PIN(cA); PIN(cB);
      }
    }
    asm volatile("s_nop 7" ::: "memory");
    STAMP(27);
    {
      const unsigned mine = L0 + L_SY + 48 + wj * 4, other = L0 + L_SY + 48 + (wj ^ 1) * 4;
      const float dsc = vch ? bhi(dpair) : blo(dpair);
      u32x2 uq[4];
#pragma unroll
      for (int c = 0; c < 4; ++c) uq[c] = lds_ld<u32x2>(uq_ + (768 - 256 * c) * 2);
      lds_spin_ge(other, 1u);
      f32x4 y[4];
      if (vrh) {
        asm volatile("" ::: "memory");
#pragma unroll
        for (int c = 0; c < 4; ++c) { lds_st<f32x4>(xo_ + c * 1024, ya[c]); y[c] = ya[4 + c]; PIN(y[c]); }
      } else {
#pragma unroll
        for (int c = 0; c < 4; ++c) { lds_st<f32x4>(xo_ + c * 1024, ya[4 + c]); y[c] = ya[c]; PIN(y[c]); }
        asm volatile("" ::: "memory");
      }
      asm volatile("s_waitcnt lgkmcnt(0)" ::: "memory");
      *(volatile LAS unsigned*)(size_t)mine = 2u;
#pragma unroll
      for (int c = 0; c < 4; ++c)
#pragma unroll
        for (int e = 0; e < 4; ++e) {
          const unsigned uu = (e & 2) ? uq[c].x : uq[c].y;
          float s = __builtin_fmaf((e & 1) ? blo(uu) : bhi(uu), dsc, y[c][e]); PIN(s); y[c][e] = s;
        }
      lds_spin_ge(other, 2u);
      f32x4 o[4];
#pragma unroll
      for (int c = 0; c < 4; ++c) o[c] = lds_ld<f32x4>(xb + c * 1024);
      SB();
      u32x2 zp[4];
#pragma unroll
      for (int c = 0; c < 4; ++c) {
        float z[4];
#pragma unroll
        for (int e = 0; e < 4; ++e) { z[e] = y[c][e] + o[c][e]; asm("" : "+v"(z[e])); }
        zp[c] = u32x2{min(cvtpk(z[0], z[1]), POISON - 1u), min(cvtpk(z[2], z[3]), POISON - 1u)};
      }
#pragma unroll
      for (int c = 0; c < 4; ++c) lds_st<u32x2>(zs_ + c * 512, zp[c]);
#pragma unroll
      for (int i = 0; i < 2; ++i)
        st16_dev(rW, vzs + i * 1024, lds_ld<u32x4>(xb + i * 1024));
    }
    STAMP(26);
    if (lane == 0) lds_inc(L0 + L_SY + 20);
    STAMP(9);
    lds_spin_ge(L0 + L_SY + 80 + wj * 4, 1u);
    STAMP(31);
  } else {
    __builtin_amdgcn_s_setprio(3);
    u32x2 sh[2]; u32x4 xr[2];
#pragma unroll
    for (int i = 0; i < 2; ++i) xr[i] = ld16(rX, (unsigned)((8 * aa + 2 * wj + i) * 1024 + lane * 16));
    {
      const rsrc_t rK = mk_rsrc(kin, S_ * D_ * 2), rV = mk_rsrc(vin, S_ * D_ * 2);
      const unsigned kb = (unsigned)((64 * idx + l3) * 1024 + pan * 128), fl = (unsigned)(l7 ^ ((l3 >> 1) & 1));
#pragma unroll
      for (int i = 0; i < 2; ++i) {
        const int g = 2 * wj + i;
        dma16(rK, kb + ((fl ^ (((g + 1) & 3) << 1)) << 4), g * 8192, L0 + L_KA + (g + 1) * 1024);
        dma16(rV, kb + ((fl ^ ((g & 3) << 1)) << 4), g * 8192, L0 + L_VA + g * 1024);
      }
      if (wj == 0) dma16(rK, idx == 0 ? OOB : kb - 8192 + (fl << 4), 0, L0 + L_KA);
    }
    u32x2 gen = ld8_dev(rW, WS_GEN);
    {
      const rsrc_t rSh = mk_rsrc(shk, D_ * 4 * 2);
#pragma unroll
      for (int i = 0; i < 2; ++i) sh[i] = ld8(rSh, (unsigned)((64 * pan + 16 * wj + 8 * i + 4 * (G & 1) + pp) * 8));
    }
    asm volatile("s_barrier" ::: "memory");
    STAMP(3);
    const int tp = wj & 1, ch = wj >> 1;
    f32x16 acc[2][3];
    f32x4 cs[3];
    unsigned ax[4], aw[2][3], ak[5];
    unsigned vkv = WS_KVT + (unsigned)((64 * pan + 16 * wj + l3) * 4096 + (1984 - 64 * idx + 8 * l7) * 2);
    unsigned vst0 = lane < 2 ? WS_ST + (unsigned)((8 * aa + 2 * wj + lane) * 8) : OOB;
    int src4 = 4 * (16 * ((((7 - l7) >> 2) << 1) | (l3 >> 2)) + 4 * ((7 - l7) & 3) + (l3 & 3));
    const unsigned so_ = ((lane & 15) == 4 * (G & 1)) ? 0x3f803f80u : 0u;
    bf16x8 sel = __builtin_bit_cast(bf16x8, u32x4{so_, so_, so_, so_});
    {
      const unsigned ax0 = L0 + (64 * tp + l31) * 128 + (((4 * tp + hh) ^ ((l31 >> 1) & 7)) << 4);
      const unsigned aw0 = L0 + 16384 + (G & 1) * 12288 + (8 * hh + qq) * 192 + ((pp & 1) << 4) + 8 * (pp >> 1);
#pragma unroll
      for (int s = 0; s < 4; ++s) { ax[s] = ax0 ^ (s << 5); PIN(ax[s]); }
#pragma unroll
      for (int i = 0; i < 3; ++i) {
        const unsigned a_ = aw0 + (((3 * ch + i) ^ (G & 1)) << 5);
        aw[0][i] = a_ + tp * 6144; aw[1][i] = a_ + 6144 - tp * 6144; PIN(aw[0][i]); PIN(aw[1][i]);
      }
#pragma unroll
      for (int j = 0; j < 5; ++j) { const int rr = 8 * (4 * (G >> 1) + pp) + qq + (j < 3 ? 4 + 4 * j : 4 * (j - 3));
        ak[j] = L0 + (j < 3 ? L_KA : L_VA) + rr * 128 + (((2 * wj) ^ fsw(rr)) << 4) + 8 * (G & 1); PIN(ak[j]); }
      PIN(vkv); PIN(vst0); PIN(src4); PIN(sel);
    }
    {
      asm volatile("s_waitcnt vmcnt(7)" : "+v"(xr[0]), "+v"(xr[1])::"memory");
      STAMP(14);
      const f32x2 x0 = {blo(__builtin_amdgcn_readfirstlane(xr[0][0])), blo(__builtin_amdgcn_readfirstlane(xr[1][0]))};
      f32x2 d[8];
#pragma unroll
      for (int e = 0; e < 4; ++e) { d[2 * e] = f32x2{blo(xr[0][e]), blo(xr[1][e])} - x0; d[2 * e + 1] = f32x2{bhi(xr[0][e]), bhi(xr[1][e])} - x0; }
      const f32x2 sm = ((d[0] + d[1]) + (d[2] + d[3])) + ((d[4] + d[5]) + (d[6] + d[7]));
      const f32x2 sq = ((d[0] * d[0] + d[1] * d[1]) + (d[2] * d[2] + d[3] * d[3])) + ((d[4] * d[4] + d[5] * d[5]) + (d[6] * d[6] + d[7] * d[7]));
      float s0 = sm[0], s1 = sm[1], q0 = sq[0], q1 = sq[1];
      wave_sum4(s0, s1, q0, q1);
      const float m = (lane ? s1 : s0) * (1.0f / D_), var = (lane ? q1 : q0) * (1.0f / D_) - m * m;
      unsigned ms0 = cvtpk(m + (lane ? x0[1] : x0[0]), __builtin_amdgcn_sqrtf(rbf(rbf(__builtin_fmaxf(var, 0.f)) + 1e-5f)));
      asm volatile("s_waitcnt vmcnt(0)" : "+v"(gen), "+v"(sh[0]), "+v"(sh[1]), "+v"(ms0)::"memory");
      STAMP(15);
      if (lane == 0) lds_inc(L0 + L_A);
      elo = __builtin_amdgcn_readfirstlane(gen.x) + 1u;
      ehi = __builtin_amdgcn_readfirstlane(gen.y) + (elo == 0u ? 1u : 0u);
      st8_dev(rW, vst0, u32x2{ms0, elo});
      f32x2 sk[4] = {{blo(sh[0].x), blo(sh[1].x)}, {bhi(sh[0].x), bhi(sh[1].x)}, {blo(sh[0].y), blo(sh[1].y)}, {bhi(sh[0].y), bhi(sh[1].y)}};
      PIN(sk[0]); PIN(sk[1]); PIN(sk[2]); PIN(sk[3]);
      lds_spin_ge(L0 + L_A, 4u);
      STAMP(1);
      u32x2 t[2][5];
#pragma unroll
      for (int j = 0; j < 5; ++j) { t[0][j] = __builtin_bit_cast(u32x2, trd(ak[j])); t[1][j] = __builtin_bit_cast(u32x2, trd(ak[j] ^ 16)); }
      SB();
      f32x2 kk[20], r[8];
#pragma unroll
      for (int j = 0; j < 5; ++j) {
        kk[4 * j] = f32x2{blo(t[0][j].x), blo(t[1][j].x)}; kk[4 * j + 1] = f32x2{bhi(t[0][j].x), bhi(t[1][j].x)};
        kk[4 * j + 2] = f32x2{blo(t[0][j].y), blo(t[1][j].y)}; kk[4 * j + 3] = f32x2{bhi(t[0][j].y), bhi(t[1][j].y)};
      }
#pragma unroll
      for (int e = 0; e < 8; ++e) r[e] = kk[e + 1] * sk[3];
#pragma unroll
      for (int j = 2; j >= 0; --j)
#pragma unroll
        for (int e = 0; e < 8; ++e) r[e] = __builtin_elementwise_fma(kk[e + 4 - j], sk[j], r[e]);
#pragma unroll
      for (int e = 0; e < 8; ++e) r[e] *= kk[12 + e];
      u32x4 po[2];
#pragma unroll
      for (int i = 0; i < 2; ++i)
#pragma unroll
        for (int e = 0; e < 4; ++e) po[i][e] = (unsigned)__builtin_amdgcn_ds_bpermute(src4, (int)cvtpk(r[7 - 2 * e][i], r[6 - 2 * e][i]));
      st16_dev(rW, vkv, po[0]); st16_dev(rW, vkv + 32768, po[1]);
      __builtin_amdgcn_s_setprio(1);
    }
    STAMP(2);
    {
      bf16x8 xf[2][2]; s16x4 wf[2][3][2];
#define PINA(t, i) PIN(acc[t][i])
#define M1(p, t, i) do { acc[t][i] = MFMA32(cat(wf[p][i][0], wf[p][i][1]), xf[p][t], acc[t][i]); PINA(t, i); } while (0)
#define MS(p, i, on) do { if (on) { cs[i] = MFMA16(sel, cat(wf[p][i][0], wf[p][i][1]), cs[i]); PIN(cs[i]); } } while (0)
#define RW(s, i) do { wf[(s) & 1][i][0] = trd(aw[(s) >> 1][i] + ((s) & 1) * 3072); wf[(s) & 1][i][1] = trd(aw[(s) >> 1][i] + ((s) & 1) * 3072 + 768); } while (0)
#define RX(s, t) xf[(s) & 1][t] = lds_ld<bf16x8>(ax[s] + (t) * 4096)
#define KEEP(p) do { asm volatile("" ::"v"(wf[p][0][0]), "v"(wf[p][0][1]), "v"(wf[p][1][0]), "v"(wf[p][1][1]), "v"(wf[p][2][0]), "v"(wf[p][2][1]), "v"(xf[p][0]), "v"(xf[p][1])); } while (0)
#define ADV(a) do { a += dl; PIN(a); } while (0)
#define STEP(p, s, on) do { M1(p, 0, 0); RW(s, 0); SB(); M1(p, 1, 0); RX(s, 0); RX(s, 1); SB(); M1(p, 0, 1); RW(s, 1); SB();  \
        M1(p, 1, 1); RW(s, 2); SB(); M1(p, 0, 2);                                                                             \
        if ((s) == 3) { ADV(ax[0]); ADV(ax[1]); ADV(ax[2]); ADV(ax[3]); ADV(aw[0][0]); } SB(); M1(p, 1, 2);                     \
        if ((s) == 3) { ADV(aw[0][1]); ADV(aw[0][2]); ADV(aw[1][0]); ADV(aw[1][1]); ADV(aw[1][2]); }                            \
        MS(p, 0, on); MS(p, 1, on); MS(p, 2, on); KEEP(p); SB(); } while (0)
      LBAR();
      STAMP(16);
      RW(0, 0); RX(0, 0); RX(0, 1); RW(0, 1); RW(0, 2); SB();
      unsigned dl = SLOT;
#pragma unroll
      for (int i = 0; i < 3; ++i) {
        cs[i] = f32x4{0.f, 0.f, 0.f, 0.f};
#pragma unroll
        for (int r = 0; r < 16; ++r) { acc[0][i][r] = 0.f; acc[1][i][r] = 0.f; }
      }
      STEP(0, 1, true);
#pragma nounroll
      for (int c = 0; c < 7; ++c) {
        dl = (c == 3) ? 0u - 3u * SLOT : (unsigned)SLOT;
        STEP(1, 2, true);
        if (c == 2) WAITV(0);
        STEP(0, 3, false);
        LBAR();
        STEP(1, 0, false); STEP(0, 1, true);
      }
      STAMP(4);
      STEP(1, 2, true);
#pragma unroll
      for (int i = 0; i < 3; ++i) lds_st<float>(L0 + L_CS + (wj * 3 + i) * 256 + lane * 4, cs[i][0]);
      STEP(0, 3, false);
      LBAR();
      float co[3]; unsigned ms[2];
#pragma unroll
      for (int i = 0; i < 3; ++i) co[i] = lds_ld<float>(L0 + L_CS + ((wj ^ 1) * 3 + i) * 256 + lane * 4);
#pragma unroll
      for (int t = 0; t < 2; ++t) ms[t] = lds_ld<unsigned>(L0 + L_ST + (64 * tp + 32 * t + l31) * 4);
      SB();
      M1(1, 0, 0); M1(1, 1, 0); M1(1, 0, 1); M1(1, 1, 1); M1(1, 0, 2); M1(1, 1, 2); SB();
      float k0[2], k1[2], s2[2];
      u32x4 bm[2];
#pragma unroll
      for (int t = 0; t < 2; ++t) {
        const unsigned mn = (ms[t] & 0xffffu) ^ 0x8000u;
        bm[t] = u32x4{mn | (mn << 16), mn, 0u, 0u};
        const float sd = bhi(ms[t]), rs = __builtin_amdgcn_rcpf(sd);
        k0[t] = rs * -2.3022081983f; k1[t] = k0[t] * rs * rs * 0.044715f; s2[t] = sd * sd;
      }
#pragma unroll
      for (int i = 0; i < 3; ++i) {
        const float c = cs[i][0] + co[i], c1 = c - rbf(c), c2 = c1 - rbf(c1);
        const u32x4 am = {cvtpk(c, c1), cvtpk(c2, 0.f), 0u, 0u};
#pragma unroll
        for (int t = 0; t < 2; ++t) { acc[t][i] = MFMA32(__builtin_bit_cast(bf16x8, am), __builtin_bit_cast(bf16x8, bm[t]), acc[t][i]); PINA(t, i); }
      }
      STAMP(5);
      const unsigned vhs = WS_H + (unsigned)(((4 * rt + 2 * tp) * 86 + 6 * cg + 3 * ch) * 1024 + lane * 16);
#pragma unroll
      for (int i = 0; i < 3; ++i) {
        const unsigned vo = (vhs + i * 1024) | (6 * cg + 3 * ch + i < 86 ? 0u : OOB);
#pragma unroll
        for (int t = 0; t < 2; ++t) {
          f32x2 g[4], zz[4], gu[4];
#pragma unroll
          for (int r = 0; r < 4; ++r) {
            g[r] = f32x2{acc[t][i][2 * r], acc[t][i][2 * r + 1]};
            zz[r] = g[r] * __builtin_elementwise_fma(g[r] * g[r], f32x2{k1[t], k1[t]}, f32x2{k0[t], k0[t]});
          }
#pragma unroll
          for (int r = 0; r < 4; ++r) {
            zz[r] = f32x2{__builtin_amdgcn_exp2f(zz[r][0]), __builtin_amdgcn_exp2f(zz[r][1])};
            gu[r] = g[r] * f32x2{acc[t][i][8 + 2 * r], acc[t][i][9 + 2 * r]};
          }
          u32x4 hq;
#pragma unroll
          for (int r = 0; r < 4; ++r) {
            const f32x2 dd = __builtin_elementwise_fma(zz[r], f32x2{s2[t], s2[t]}, f32x2{s2[t], s2[t]});
            const f32x2 hv = gu[r] * f32x2{__builtin_amdgcn_rcpf(dd[0]), __builtin_amdgcn_rcpf(dd[1])};
            hq[r] = cvtpk(hv[0], hv[1]);
          }
          st16_dev(rW, vo + t * (86 * 1024), hq);
        }
      }
    }
    if (lane == 0) lds_inc(L0 + L_SY + 24);
    STAMP(6);
    const unsigned vd = VD();
    PARK();
    WAITV(0);
    if (lane == 0 && lds_inc(L0 + L_SY + 16) == 3u && cg < 15) { u32x2 e = {elo, ehi}; st8_dev(rW, WS_FH + (unsigned)((rt * 16 + cg) * 128), e); }
    STAMP(8);
    lds_wait_ge(L0 + L_SY + 64 + wj * 4, 1u);
#pragma unroll
    for (int j = 0; j < 4; ++j) WDU(14 + j, L_B1 + wj * 8192 + j * 2048);
#pragma unroll
    for (int j = 0; j < 4; ++j) WDU(18 + j, L_B2 + wj * 10240 + j * 2048);
    xo = ld16(rX, VXO()); st2 = ld8_dev(rW, VS2());
    {
      const int gf = ks0 / 6, gl = (ks0 + 20 + (wj < 2)) / 6, gi = min(gf + l7, gl);
      const unsigned vfh = WS_FH + (unsigned)(((r64 >> 1) * 16 + gi) * 128);
      u32x2 fh = ld8_dev(rW, vfh);
      asm volatile("s_waitcnt vmcnt(0)" : "+v"(fh), "+v"(xo), "+v"(st2)::"memory");
      if (!__all(fh.x == elo && fh.y == ehi)) fh = ld8_dev(rW, vfh);
      XN();
      asm volatile("s_waitcnt vmcnt(0)" : "+v"(fh)::"memory");
      spin_flags(rW, vfh, fh, elo, ehi);
    }
    LDS_SET(L0 + L_SY + 80 + wj * 4, 1u);
  }
  STAMP(10);
  {
    u32x4 hf[22];
    const unsigned vh = WS_H + (unsigned)(((2 * r64 + (w >> 2)) * 86 + ks0) * 1024 + lane * 16);
#define HL(j) hf[j] = ld16_dev(rW, vh, __builtin_amdgcn_readfirstlane(min(PH(j), klast) * 1024))
#pragma unroll
    for (int j = 0; j < HWIN; ++j) HL(j);
    const unsigned vz = WS_ZT + (unsigned)((64 * ct + 8 * w + l3) * 4096 + r64 * 128 + l7 * 16);
    u32x4 zr;
    STAMP(28);
    const unsigned pq = w < 4 ? L0 + L_P0 : L0 + L_B3, ps = w < 4 ? 8192 : 10240;
    unsigned pw[4], pr[4], zw, zt_, zsrc, vo;
#pragma unroll
    for (int i = 0; i < 4; ++i) {
      pw[i] = pq + wj * ps + l31 * 256 + (((hh ^ l7) ^ (2 * i)) << 4); PIN(pw[i]);
      pr[i] = pq + i * ps + (8 * wj + l3) * 256 + (((2 * l7) ^ l3) << 4); PIN(pr[i]);
    }
    { const int r = 8 * w + l3, r2 = 8 * (4 * (G >> 1) + pp) + qq;
      zw = L0 + L_ZT + r * 128 + ((l7 ^ fsw(r)) << 4); zt_ = L0 + L_ZT + r2 * 128 + ((w ^ fsw(r2)) << 4) + 8 * (G & 1);
      zsrc = 4 * (16 * (((l7 >> 2) << 1) | (l3 >> 2)) + 4 * (l7 & 3) + (l3 & 3)); vo = VXO(); PIN(zw); PIN(zt_); PIN(zsrc); PIN(vo); }
    lds_wait_ge(L0 + L_SY + wj * 4, 1u);
    const unsigned a1 = AD() + L_B1 + wj * 8192, a2 = AD() + L_B2 + wj * 10240;
    f32x16 fo[2];
#pragma unroll
    for (int r = 0; r < 16; ++r) { fo[0][r] = 0.f; fo[1][r] = 0.f; }
#define G2(j) do { if ((j) + 2 >= 8 && (j) + 2 < 22) { wpk[((j) + 2) & 7][0] = WDF(UA((j) + 2), 0); wpk[((j) + 2) & 7][1] = WDF(UA((j) + 2), 1); } \
      asm volatile("s_waitcnt vmcnt(%1)" : "+v"(hf[j]) : "i"((j) + HWIN <= 22 ? HWIN - 1 : (j) <= ZAT ? 21 - (j) : 22 - (j)) : "memory"); \
      fo[0] = MFMA32(wpk[(j) & 7][0], __builtin_bit_cast(bf16x8, hf[j]), fo[0]);                                      \
      fo[1] = MFMA32(wpk[(j) & 7][1], __builtin_bit_cast(bf16x8, hf[j]), fo[1]); PIN(fo[0]); PIN(fo[1]); SB();      \
      if ((j) + HWIN < 22) HL(((j) + HWIN) % 22); if ((j) == ZAT) zr = ld16_dev(rW, vz, 0); } while (0)
    G2(0); STAMP(29); G2(1); G2(2); G2(3); G2(4); G2(5); G2(6); G2(7); G2(8); G2(9); G2(10);
    G2(11); G2(12); G2(13);
    if (w < 4) LDS_SET(L0 + L_SY + 96 + wj * 4, 1u);
    G2(14); G2(15); G2(16); G2(17); G2(18);
    lds_spin_ge(L0 + L_SY + (w < 4 ? 20 : 96 + wj * 4), w < 4 ? 4u : 1u);
    G2(19); G2(20); G2(21);
    STAMP(11);
#pragma unroll
    for (int dt = 0; dt < 2; ++dt)
#pragma unroll
      for (int i = 0; i < 4; ++i)
        lds_st<f32x4>(pw[i] + dt * 128, f32x4{fo[dt][4 * i], fo[dt][4 * i + 1], fo[dt][4 * i + 2], fo[dt][4 * i + 3]});
    asm volatile("s_waitcnt vmcnt(0)" : "+v"(zr)::"memory");
    for (unsigned n = 0; __builtin_expect(__any(max(max(zr[0], zr[1]), max(zr[2], zr[3])) == POISON) && n < SPIN_MAX, 0); ++n) {
      zr = ld16_dev(rW, vz, 0);
      asm volatile("s_waitcnt vmcnt(0)" : "+v"(zr)::"memory");
    }
    STAMP(30);
    if (w >= 4) lds_spin_ge(L0 + L_SY + 20, 4u);
    lds_st<u32x4>(zw, zr);
    LBAR();
    STAMP(12);
    const u32x2 zt0 = __builtin_bit_cast(u32x2, trd(zt_)), zt1 = __builtin_bit_cast(u32x2, trd(zt_ + 512));
    f32x4 pa[4], pb[4];
#pragma unroll
    for (int k = 0; k < 4; ++k) { pa[k] = lds_ld<f32x4>(pr[k]); pb[k] = lds_ld<f32x4>(pr[k] ^ 16); }
    SB();
    const unsigned zq[4] = {(unsigned)__builtin_amdgcn_ds_bpermute(zsrc, (int)zt0.x), (unsigned)__builtin_amdgcn_ds_bpermute(zsrc, (int)zt0.y),
                            (unsigned)__builtin_amdgcn_ds_bpermute(zsrc, (int)zt1.x), (unsigned)__builtin_amdgcn_ds_bpermute(zsrc, (int)zt1.y)};
    const f32x4 p0 = (pa[0] + pa[1]) + (pa[2] + pa[3]), p1 = (pb[0] + pb[1]) + (pb[2] + pb[3]);
    u32x4 ov;
#pragma unroll
    for (int i = 0; i < 4; ++i) {
      const f32x2 F = i < 2 ? f32x2{p0[2 * i], p0[2 * i + 1]} : f32x2{p1[2 * i - 4], p1[2 * i - 3]};
      const f32x2 t = F * f32x2{-1.4426950408889634f, -1.4426950408889634f};
      const f32x2 d = f32x2{__builtin_amdgcn_exp2f(t[0]), __builtin_amdgcn_exp2f(t[1])} + f32x2{1.0f, 1.0f};
      const f32x2 g = F * f32x2{__builtin_amdgcn_rcpf(d[0]), __builtin_amdgcn_rcpf(d[1])};
      const f32x2 o = __builtin_elementwise_fma(xn[i] * f32x2{blo(zq[i]), bhi(zq[i])}, g, f32x2{blo(xo[i]), bhi(xo[i])});
      ov[i] = cvtpk(o[0], o[1]);
    }
    __builtin_amdgcn_raw_buffer_store_b128(ov, mk_rsrc(out, S_ * D_ * 2), vo, 0, OUTAUX);
  }
}

extern "C" void kernel_launch(void* const* d_in, const int* in_sizes, int n_in, void* d_out, int out_size, void* d_ws, size_t ws_size,
                              hipStream_t stream) {
  (void)in_sizes; (void)n_in; (void)out_size;
  (void)hipFuncSetAttribute((const void*)h3_geglu_fused, hipFuncAttributeMaxDynamicSharedMemorySize, LDS_BYTES);
  h3_geglu_fused<<<256, 512, LDS_BYTES, stream>>>((const u16*)d_in[0], (const u16*)d_in[1], (const u16*)d_in[2], (const u16*)d_in[3],
                                                  (const u16*)d_in[4], (const u16*)d_in[5], (const u16*)d_in[7], (const u16*)d_in[8],
                                                  (const u16*)d_in[9], (u16*)d_out, (char*)d_ws + WS_BASE(ws_size));
}
```

```cpp
#define S0POS 2
#include <hip/hip_runtime.h>
#include <hip/hip_bf16.h>

typedef unsigned short u16;
typedef unsigned long long u64;
typedef __attribute__((ext_vector_type(8))) short bf16x8;
typedef __attribute__((ext_vector_type(4))) short s16x4;
typedef __attribute__((ext_vector_type(2))) __bf16 bf16x2_t;
typedef __attribute__((ext_vector_type(2))) float f32x2;
typedef __attribute__((ext_vector_type(4))) float f32x4;
typedef __attribute__((ext_vector_type(16))) float f32x16;
typedef __attribute__((ext_vector_type(4))) unsigned u32x4;
typedef __attribute__((ext_vector_type(2))) unsigned u32x2;
#define LAS __attribute__((address_space(3)))
#define GAS __attribute__((address_space(1)))
typedef __amdgpu_buffer_rsrc_t rsrc_t;

constexpr int S_ = 2048, D_ = 512, DF = 1376;
constexpr unsigned WS_GEN = 0, WS_FA = 4096, WS_FH = WS_FA + 256 * 128, WS_FZ = WS_FH + 256 * 128;
constexpr unsigned WS_ST = WS_FZ + 256 * 128;
constexpr unsigned WS_KVT = 1u << 20;
constexpr unsigned WS_ZT = WS_KVT + (2u << 20);
constexpr unsigned WS_H = WS_ZT + (2u << 20);
constexpr unsigned WS_BYTES = WS_H + 64 * 86 * 1024;
constexpr int SLOT = 40960;
constexpr int L_CV = 3 * SLOT;
constexpr int L_KA = L_CV, L_VA = L_KA + 9216;
constexpr int L_B1 = 0, L_B2 = SLOT, L_B3 = 2 * SLOT;
constexpr int L_CS = 32768;
constexpr int L_ST = L_CS + 3072;
constexpr int L_SY = L_ST + 512;
constexpr int L_A = L_VA + 8192;
constexpr int L_P0 = L_CV + 8192;
constexpr int L_U0 = 36864, L_U1 = L_CV + 33280, L_ZT = L_CV;
constexpr int LDS_BYTES = 163840;
constexpr unsigned OOB = 0x80000000u;
constexpr unsigned POISON = 0xffffffffu;
constexpr unsigned SPIN_MAX = 1u << 18;

#define LPRIO 2
#define XSLEEP 4
#define ROT1 ((rt + cg) & 7)
#define ROT2 (((xcd * 4 + (idx >> 3)) * 11) >> 4)
#define HWIN 10
#define STEPMODE 1
#define NOSTATS 0
#define WS_BASE(sz) 0
#define ZSLEEP 4
#define ABL_PLAIN 0
#define ZAT 15
#define OUTAUX 16
#define S0EARLY 1
#define SETUPPRIO 2
#define MFMA32(a, b, c) __builtin_amdgcn_mfma_f32_32x32x16_bf16(a, b, c, 0, 0, 0)
#define MFMA16(a, b, c) __builtin_amdgcn_mfma_f32_16x16x32_bf16(a, b, c, 0, 0, 0)
#define WAIT_BAR(N) asm volatile("s_waitcnt vmcnt(" #N ") lgkmcnt(0)\n\ts_barrier" ::: "memory")
#define LBAR() asm volatile("s_waitcnt lgkmcnt(0)\n\ts_barrier" ::: "memory")
#define WAITV(N) asm volatile("s_waitcnt vmcnt(" #N ")" ::: "memory")
#define SB() __builtin_amdgcn_sched_barrier(0)

__device__ __forceinline__ rsrc_t mk_rsrc(const void* p, unsigned bytes) {
  return __builtin_amdgcn_make_buffer_rsrc((void*)p, (short)0, (int)bytes, 0x00020000);
}
__device__ __forceinline__ void dma16(rsrc_t rs, unsigned voff, unsigned soff, unsigned lds) {
  asm volatile("s_mov_b32 m0, %0\n\ts_nop 0\n\tbuffer_load_dwordx4 %1, %2, %3 offen lds" ::"s"(lds), "v"(voff), "s"(rs), "s"(soff) : "memory");
}
__device__ __forceinline__ u32x4 ld16(rsrc_t rs, unsigned voff) {
  u32x4 r; asm volatile("buffer_load_dwordx4 %0, %1, %2, 0 offen" : "=v"(r) : "v"(voff), "s"(rs) : "memory"); return r;
}
__device__ __forceinline__ u32x4 ld16_dev(rsrc_t rs, unsigned voff, unsigned soff) {
  u32x4 r; asm volatile("buffer_load_dwordx4 %0, %1, %2, %3 offen sc1" : "=v"(r) : "v"(voff), "s"(rs), "s"(soff) : "memory"); return r;
}
__device__ __forceinline__ u32x2 ld8(rsrc_t rs, unsigned voff) {
  u32x2 r; asm volatile("buffer_load_dwordx2 %0, %1, %2, 0 offen" : "=v"(r) : "v"(voff), "s"(rs) : "memory"); return r;
}
__device__ __forceinline__ u32x2 ld8_dev(rsrc_t rs, unsigned voff) {
  u32x2 r; asm volatile("buffer_load_dwordx2 %0, %1, %2, 0 offen sc1" : "=v"(r) : "v"(voff), "s"(rs) : "memory"); return r;
}
#define PIN(x) asm volatile("" : "+v"(x))
__device__ __forceinline__ void st16_dev(rsrc_t rs, unsigned voff, u32x4 v) { __builtin_amdgcn_raw_buffer_store_b128(v, rs, voff, 0, 16); }
__device__ __forceinline__ void st8_dev(rsrc_t rs, unsigned voff, u32x2 v) { __builtin_amdgcn_raw_buffer_store_b64(v, rs, voff, 0, 16); }

__device__ __forceinline__ unsigned cvtpk(float lo, float hi) {
  bf16x2_t r = {(__bf16)lo, (__bf16)hi};
  return __builtin_bit_cast(unsigned, r);
}
__device__ __forceinline__ float blo(unsigned w) { return __uint_as_float(w << 16); }
__device__ __forceinline__ float bhi(unsigned w) { return __uint_as_float(w & 0xffff0000u); }
__device__ __forceinline__ float rbf(float f) { return blo(cvtpk(f, f)); }
__device__ __forceinline__ s16x4 trd(unsigned a) { return __builtin_amdgcn_ds_read_tr16_b64_v4i16((LAS s16x4*)(size_t)a); }
__device__ __forceinline__ bf16x8 cat(s16x4 a, s16x4 b) { return __builtin_shufflevector(a, b, 0, 1, 2, 3, 4, 5, 6, 7); }
template <class T> __device__ __forceinline__ T lds_ld(unsigned a) { return *(const LAS T*)(size_t)a; }
template <class T> __device__ __forceinline__ void lds_st(unsigned a, T v) { *(LAS T*)(size_t)a = v; }
template <int CTRL> __device__ __forceinline__ float dpp_add(float x) {
  return x + __uint_as_float(__builtin_amdgcn_update_dpp(0u, __float_as_uint(x), CTRL, 0xf, 0xf, true));
}
__device__ __forceinline__ float swap_add(float x, bool half) {
  if (half) { auto r = __builtin_amdgcn_permlane32_swap(__float_as_uint(x), __float_as_uint(x), false, false); return __uint_as_float(r[0]) + __uint_as_float(r[1]); }
  auto r = __builtin_amdgcn_permlane16_swap(__float_as_uint(x), __float_as_uint(x), false, false); return __uint_as_float(r[0]) + __uint_as_float(r[1]);
}
__device__ __forceinline__ void wave_sum4(float& a, float& b, float& c, float& d) {
  a = dpp_add<0x128>(a); b = dpp_add<0x128>(b); c = dpp_add<0x128>(c); d = dpp_add<0x128>(d);
  a = dpp_add<0x124>(a); b = dpp_add<0x124>(b); c = dpp_add<0x124>(c); d = dpp_add<0x124>(d);
  a = dpp_add<0x122>(a); b = dpp_add<0x122>(b); c = dpp_add<0x122>(c); d = dpp_add<0x122>(d);
  a = dpp_add<0x121>(a); b = dpp_add<0x121>(b); c = dpp_add<0x121>(c); d = dpp_add<0x121>(d);
  a = swap_add(a, false); b = swap_add(b, false); c = swap_add(c, false); d = swap_add(d, false);
  a = swap_add(a, true); b = swap_add(b, true); c = swap_add(c, true); d = swap_add(d, true);
}
__device__ __forceinline__ void lds_wait_ge(unsigned a, unsigned v) {
  for (unsigned n = 0; *(volatile LAS unsigned*)(size_t)a < v && n < SPIN_MAX * 16; ++n) __builtin_amdgcn_s_sleep(1);
  asm volatile("" ::: "memory");
}
__device__ __forceinline__ void lds_spin_ge(unsigned a, unsigned v) {
  for (unsigned n = 0; *(volatile LAS unsigned*)(size_t)a < v && n < SPIN_MAX * 64; ++n) {}
  asm volatile("" ::: "memory");
}
__device__ __forceinline__ unsigned lds_inc(unsigned a) {
  return __hip_atomic_fetch_add((LAS unsigned*)(size_t)a, 1u, __ATOMIC_RELAXED, __HIP_MEMORY_SCOPE_WORKGROUP);
}
__device__ __forceinline__ int fsw(int r) { return ((r >> 1) & 1) | (((r >> 3) & 3) << 1); }
__device__ __forceinline__ void spin_flags(rsrc_t rs, unsigned voff, u32x2 f, unsigned lo, unsigned hi) {
  for (unsigned n = 0; !__all(f.x == lo && f.y == hi) && n < SPIN_MAX; ++n) {
    __builtin_amdgcn_s_sleep(1);
    f = ld8_dev(rs, voff); WAITV(0); PIN(f);
  }
}
__device__ __forceinline__ u32x2 spin_tag(rsrc_t rs, unsigned voff, u32x2 f, unsigned tag) {
  for (unsigned n = 0; !__all(f.y == tag) && n < SPIN_MAX; ++n) {
    __builtin_amdgcn_s_sleep(1);
    f = ld8_dev(rs, voff); WAITV(0); PIN(f);
  }
  return f;
}
#define STAMP(i)

__global__ __launch_bounds__(512, 2) void h3_geglu_fused(const u16* __restrict__ x, const u16* __restrict__ kin, const u16* __restrict__ vin,
                                                         const u16* __restrict__ shk, const u16* __restrict__ ssk, const u16* __restrict__ ssd,
                                                         const u16* __restrict__ wg, const u16* __restrict__ wu, const u16* __restrict__ wd,
                                                         u16* __restrict__ out, char* __restrict__ ws) {
  extern __shared__ __attribute__((aligned(16))) char smem[];
  const unsigned L0 = (unsigned)(size_t)(LAS char*)smem;
  const int tid = threadIdx.x, lane = tid & 63;
  const int w = __builtin_amdgcn_readfirstlane(tid >> 6), wj = w & 3;
  STAMP(0);
  const int b = blockIdx.x, xcd = b & 7, idx = b >> 3, rh = xcd & 1, q4 = xcd >> 1;
  const int pan = 4 * rh + q4, aa = 32 * pan + idx;
  const int l3 = lane >> 3, l7 = lane & 7, G = lane >> 4, qq = (lane >> 2) & 3, pp = lane & 3, hh = lane >> 5, l31 = lane & 31;
  const int e8 = idx & 7, m3 = (23 - idx) >> 31, m4 = (3 - e8) >> 31;
  const int cgA = ((q4 * 7 + 1) >> 1) + (idx >> 3), cgS = 3 + 7 * (q4 >> 1), cgB = cgS + ((14 + ((e8 >> 1) & 1) - cgS) & m4);
  const int rS = (q4 & 1) * 4 + e8, rB = rS + ((2 * q4 + (e8 & 1) - rS) & m4);
  const int cg = cgA + ((cgB - cgA) & m3), rt = 8 * rh + e8 + ((rB - e8) & m3);
  const int r64 = 4 * pan + (idx >> 3), ct = idx & 7;
  const int ks0 = 22 * wj - ((wj + 1) >> 2);
  u32x4 xo; u32x2 st2;
  unsigned elo, ehi;
#define VD() (unsigned)((16 * ks0 + l3) * 1024 + ct * 128 + ((l7 ^ (((l3 >> 1) & 1) << 2)) << 4))
  const int rot2 = ROT2;
#define PH(j) ((j) + rot2 >= 22 ? (j) + rot2 - 22 : (j) + rot2)
  const int klast = 21 - (wj >> 1);
#define WDU(j, lds) do { const unsigned v_ = vd | (PH(j) > klast ? OOB : 0u);                                       \
    dma16(rD, v_, PH(j) * 16384, L0 + (lds)); dma16(rD, v_, PH(j) * 16384 + 8192, L0 + (lds) + 1024); } while (0)
#define VXO() (unsigned)((64 * r64 + 8 * w + l3) * 1024 + ct * 128 + l7 * 16)
#define VS2() (WS_ST + (unsigned)((64 * r64 + 8 * w + l3) * 8))

  bf16x8 wpk[8][2];
#define AD() (L0 + (8 * hh + qq) * 128 + ((4 * ((qq >> 1) & 1) + 2 * (G & 1) + (pp >> 1)) << 4) + 8 * (pp & 1))
#define WDF(base, dt) cat(trd(((base) ^ ((dt) * 64))), trd(((base) ^ ((dt) * 64)) + 512))
#define UA(j) ((j) < 4 ? a1 + (j) * 2048 : (j) < 9 ? a2 + ((j) - 4) * 2048 : (j) < 14 ? a2 + SLOT + ((j) - 9) * 2048 : (j) < 18 ? a1 + ((j) - 14) * 2048 : a2 + ((j) - 18) * 2048)
#define PARK() do { const unsigned a1 = AD() + L_B1 + wj * 8192, a2 = AD() + L_B2 + wj * 10240;                         \
    _Pragma("unroll") for (int j = 0; j < 8; ++j) { wpk[j][0] = WDF(UA(j), 0); wpk[j][1] = WDF(UA(j), 1); }              \
    _Pragma("unroll") for (int j = 0; j < 8; ++j) { PIN(wpk[j][0]); PIN(wpk[j][1]); } } while (0)
  f32x2 xn[4];
#define XN() do { st2 = spin_tag(rW, VS2(), st2, elo); const float mean_ = blo(st2.x), rs_ = __builtin_amdgcn_rcpf(bhi(st2.x));       \
    _Pragma("unroll") for (int i = 0; i < 4; ++i) { float a_ = (blo(xo[i]) - mean_) * rs_, b_ = (bhi(xo[i]) - mean_) * rs_; PIN(a_); PIN(b_); xn[i] = f32x2{a_, b_}; } } while (0)
#define LDS_SET(a, v) do { asm volatile("" ::: "memory"); *(volatile LAS unsigned*)(size_t)(a) = (v); } while (0)

  const unsigned vla = (unsigned)((128 * rt + 8 * wj + l3) * 1024 + ((l7 ^ ((4 * wj + (l3 >> 1)) & 7)) << 4)) | ((unsigned)((14 - cg) >> 31) & OOB);
  const unsigned vca = (unsigned)((8 * aa + 2 * wj) * 1024 + lane * 16);
  unsigned v1st = vca ^ ((vla ^ vca) & (unsigned)((3 - w) >> 31));
  asm volatile("" : "+v"(v1st) : "s"(cg), "s"(rt), "s"(aa), "s"(r64), "s"(ks0), "s"(rot2), "s"(klast), "v"(l3), "v"(l7), "v"(G), "v"(qq), "v"(pp), "v"(hh), "v"(l31));
  SB();
  asm volatile("" ::"s"(x), "s"(kin), "s"(vin), "s"(shk), "s"(ssk), "s"(ssd), "s"(wg), "s"(wu), "s"(wd), "s"(out), "s"(ws));
  const rsrc_t rX = mk_rsrc(x, S_ * D_ * 2), rW = mk_rsrc(ws, WS_BYTES), rD = mk_rsrc(wd, DF * D_ * 2);
  if (w >= 4) {
    __builtin_amdgcn_s_setprio(2);
    const unsigned vx = v1st;
    const int rot1 = ROT1;
#pragma unroll
    for (int n = 0; n < 4; ++n) dma16(rX, vx, rot1 * 128 + n * 32768, L0 + wj * 1024 + n * 4096);
    unsigned vg[3], vu[3];
    const rsrc_t rG = mk_rsrc(wg, D_ * DF * 2), rU = mk_rsrc(wu, D_ * DF * 2);
#pragma unroll
    for (int r = 0; r < 3; ++r) {
      const unsigned P = 64 * r + lane, k = (P * 43691u) >> 19, col = 96 * cg + 8 * (P - 12 * k);
      vg[r] = (col < DF) ? (16 * wj + k) * (DF * 2) + col * 2 : OOB; vu[r] = vg[r] ^ 32u;
      dma16(rG, vg[r], rot1 * (64 * DF * 2), L0 + 16384 + wj * 3072 + r * 1024);
      dma16(rU, vu[r], rot1 * (64 * DF * 2), L0 + 16384 + 12288 + wj * 3072 + r * 1024);
      if (r == S0POS) asm volatile("s_barrier" ::: "memory");
    }
    u32x2 gen = ld8_dev(rW, WS_GEN);
    unsigned dpv;
    asm volatile("buffer_load_dword %0, %1, %2, 0 offen" : "=v"(dpv) : "v"(aa * 4), "s"(mk_rsrc(ssd, D_ * 2)) : "memory");
#pragma unroll
    for (int i = 0; i < 2; ++i) st16_dev(rW, WS_ZT + (unsigned)(2 * aa * 4096 + (2 * wj + i) * 1024 + lane * 16), u32x4{POISON, POISON, POISON, POISON});
#pragma nounroll
    for (int c = 1; c < 8; ++c) {
      const int cc = (c + rot1) & 7;
      const unsigned sx = cc * 128, sw = cc * (64 * DF * 2), lx = L0 + (c & 3) * SLOT + wj * 1024, lw = L0 + (c & 3) * SLOT + 16384 + wj * 3072;
      dma16(rX, vx, sx, lx);                 dma16(rG, vg[0], sw, lw);        dma16(rU, vu[0], sw, lw + 12288);
      dma16(rX, vx, sx + 32768, lx + 4096);  dma16(rG, vg[1], sw, lw + 1024); dma16(rU, vu[1], sw, lw + 13312);
      if (c >= 2) asm volatile("s_waitcnt vmcnt(16)\n\ts_barrier" ::: "memory");
      if (c == 2) STAMP(16);
      if (c == 3) STAMP(18);
      if (c == 7) STAMP(23);
      if (c == 5 && tid == 256) {
        unsigned lo_, hi_;
        asm volatile("v_add_co_u32 %0, vcc, 1, %2\n\ts_nop 1\n\tv_addc_co_u32 %1, vcc, 0, %3, vcc" : "=&v"(lo_), "=&v"(hi_) : "v"(gen.x), "v"(gen.y) : "vcc");
        st8_dev(rW, WS_FA + (unsigned)(aa * 128), u32x2{lo_, hi_});
      }
      dma16(rX, vx, sx + 65536, lx + 8192);  dma16(rG, vg[2], sw, lw + 2048); dma16(rU, vu[2], sw, lw + 14336);
      dma16(rX, vx, sx + 98304, lx + 12288);
    }
    PIN(gen); PIN(dpv);
    elo = __builtin_amdgcn_readfirstlane(gen.x) + 1u;
    ehi = __builtin_amdgcn_readfirstlane(gen.y) + (elo == 0u ? 1u : 0u);
    const unsigned dpair = __builtin_amdgcn_readfirstlane(dpv);
    const int ce = tid & 255;
    const unsigned vd = VD();
    const unsigned vfa = WS_FA + (unsigned)((32 * (hh ? ct : pan) + l31) * 128), vst = WS_ST + (unsigned)((128 * rt + 64 * (wj & 1) + lane) * 8);
    const u32x4 zero4 = {0u, 0u, 0u, 0u};
    u32x2 stg = ld8_dev(rW, vst), fa = ld8_dev(rW, vfa);
#pragma unroll
    for (int j = 0; j < 4; ++j) WDU(j, L_B1 + wj * 8192 + j * 2048);
    if (ce < 7) lds_st<u32x4>(L0 + L_SY + ce * 16, zero4);
    WAIT_BAR(20);
#pragma unroll
    for (int j = 0; j < 5; ++j) WDU(4 + j, L_B2 + wj * 10240 + j * 2048);
    asm volatile("s_waitcnt vmcnt(18)\n\ts_barrier" : "+v"(stg), "+v"(fa)::"memory");
    STAMP(4);
    stg = spin_tag(rW, vst, stg, elo);
    if (wj < 2) lds_st<unsigned>(L0 + L_ST + (64 * wj + lane) * 4, stg.x);
    spin_flags(rW, vfa, fa, elo, ehi);
    u32x4 hc[2], hp[2], ur[2];
    {
      const rsrc_t rHk = mk_rsrc(ssk, D_ * S_ * 2);
#pragma unroll
      for (int c = 0; c < 2; ++c) {
        hc[c] = ld16(rHk, (unsigned)((2 * aa + c) * 4096 + ce * 16));
        hp[c] = ld16(rHk, ce ? (unsigned)((2 * aa + c) * 4096 + ce * 16 - 16) : OOB);
        ur[c] = ld16_dev(rW, WS_KVT + (unsigned)(2 * aa * 4096 + ce * 16), c * 4096);
      }
    }
    xo = ld16(rX, VXO()); st2 = ld8_dev(rW, VS2());
#pragma unroll
    for (int j = 0; j < 2; ++j) WDU(9 + j, L_B3 + wj * 10240 + j * 2048);
    WAIT_BAR(12);
    STAMP(5);
#pragma unroll
    for (int j = 2; j < 5; ++j) WDU(9 + j, L_B3 + wj * 10240 + j * 2048);
    __builtin_amdgcn_s_setprio(SETUPPRIO);
    {
      asm volatile("s_waitcnt vmcnt(10)" : "+v"(hc[0]), "+v"(hc[1]), "+v"(hp[0]), "+v"(hp[1]), "+v"(ur[0]), "+v"(ur[1]), "+v"(xo), "+v"(st2)::"memory");
      auto put = [&](unsigned hb, u32x4 pv, u32x4 cu, int blk) {
        const unsigned a0 = __builtin_amdgcn_alignbit(pv[1], pv[0], 16), a1 = __builtin_amdgcn_alignbit(pv[2], pv[1], 16),
                       a2 = __builtin_amdgcn_alignbit(pv[3], pv[2], 16), a3 = __builtin_amdgcn_alignbit(cu[0], pv[3], 16),
                       a4 = __builtin_amdgcn_alignbit(cu[1], cu[0], 16);
        lds_st<u32x4>(hb + blk * 16, u32x4{a0, a1, a2, a3});
        lds_st<u32x4>(hb + 4160 + blk * 16, u32x4{pv[1], pv[2], pv[3], cu[0]});
        lds_st<u32x4>(hb + 8320 + blk * 16, u32x4{a1, a2, a3, a4});
        lds_st<u32x4>(hb + 12480 + blk * 16, u32x4{pv[2], pv[3], cu[0], cu[1]});
      };
#pragma unroll
      for (int c = 0; c < 2; ++c) {
        put(L0 + L_CV + c * 16640, hp[c], hc[c], ce + 1);
        if (ce == 0 || ce == 255) put(L0 + L_CV + c * 16640, ce ? hc[c] : zero4, zero4, ce ? 257 : 0);
        lds_st<u32x4>(L0 + (c ? L_U1 : L_U0) + ce * 16, ur[c]);
      }
      asm volatile("s_waitcnt lgkmcnt(0)" ::: "memory");
      if (lane == 0) lds_inc(L0 + L_SY + 24);
    }
    PARK();
    LDS_SET(L0 + L_SY + 64 + wj * 4, 1u);
    XN();
    if (b == 0 && wj == 0) {
      u32x2 f[4];
#pragma unroll
      for (int i = 0; i < 4; ++i) f[i] = ld8_dev(rW, WS_FA + (unsigned)((64 * i + lane) * 128));
      asm volatile("s_waitcnt vmcnt(0)" : "+v"(f[0]), "+v"(f[1]), "+v"(f[2]), "+v"(f[3])::"memory");
#pragma unroll
      for (int i = 0; i < 4; ++i) spin_flags(rW, WS_FA + (unsigned)((64 * i + lane) * 128), f[i], elo, ehi);
      if (lane == 0) { u32x2 e = {elo, ehi}; st8_dev(rW, WS_GEN, e); }
    }
    STAMP(7);
    const int vch = wj >> 1, vrh = wj & 1, c15 = lane & 15;
    f32x4 ya[8];
#pragma unroll
    for (int c = 0; c < 8; ++c) { ya[c] = f32x4{0.f, 0.f, 0.f, 0.f}; PIN(ya[c]); }
    unsigned xo_ = L0 + L_CV + vch * 16640 + (vrh ^ 1) * 4096 + lane * 16, xb = L0 + L_CV + vch * 16640 + vrh * 4096 + lane * 16;
    unsigned zs_ = L0 + L_CV + vch * 16640 + vrh * 4096 + c15 * 32 + G * 8, uq_ = L0 + (vch ? L_U1 : L_U0) + (2044 - 768 - 1024 * vrh - 16 * c15 - 4 * G) * 2;
    unsigned vzs = WS_ZT + (unsigned)((2 * aa + vch) * 4096 + vrh * 2048 + lane * 16);
    PIN(xo_); PIN(xb); PIN(zs_); PIN(uq_); PIN(vzs);
    {
      unsigned cA = L0 + L_CV + vch * 16640 + (c15 & 3) * 4160 + (4 * (c15 >> 2) + 8 * G + 128 * vrh) * 2;
      const unsigned ub = L0 + (vch ? L_U1 : L_U0);
      unsigned cB = ub + (240 - 16 * c15 + 8 * G + 128 * vrh) * 2;
      int lim = ((16 * c15 - 8 * G + 47) >> 5) - 4 * vrh;
      PIN(cA); PIN(cB);
      bf16x8 bfr[2][8]; s16x4 ah[2][4];
#define VLD8(a) (*(const volatile LAS s16x4*)(size_t)(a))
#define CA(set, r, p, i) ah[set][i] = VLD8(cA + (r) * 64 + (((i) & 2) ? 7 - (p) : (p)) * 512 + ((i) & 1) * 8)
#define CB(r, mu) bfr[(r) & 1][mu] = lds_ld<bf16x8>((mu) == 0 ? ((r) >= lim ? L0 + L_SY + 32 : cB + (r) * 64 + 3584) : cB + (r) * 64 + (7 - (mu)) * 512)
#define MF16(c, a, b) asm volatile("v_mfma_f32_16x16x32_bf16 %0, %1, %2, %0" : "+v"(c) : "v"(a), "v"(b))
#define CM(r, p, m) do { if ((m) + (p) < 8) MF16(ya[((p) + (m)) & 7], cat(ah[(p) & 1][0], ah[(p) & 1][1]), bfr[(r) & 1][(m) & 7]);  \
        else MF16(ya[((m) - 1) & 7], cat(ah[(p) & 1][2], ah[(p) & 1][3]), bfr[(r) & 1][((m) + (p)) & 7]); } while (0)
#define CKEEP(r, p) asm volatile("" ::"v"(ah[(p) & 1][0]), "v"(ah[(p) & 1][1]), "v"(ah[(p) & 1][2]), "v"(ah[(p) & 1][3]), "v"(bfr[(r) & 1][0]), "v"(bfr[(r) & 1][1]), \
        "v"(bfr[(r) & 1][2]), "v"(bfr[(r) & 1][3]), "v"(bfr[(r) & 1][4]), "v"(bfr[(r) & 1][5]), "v"(bfr[(r) & 1][6]), "v"(bfr[(r) & 1][7]))
#define CPAIR(r, p) do { _Pragma("unroll") for (int m = 0; m < 9; ++m) { CM(r, p, m);                                               \
          if (m < 4) CA(((p) + 1) & 1, (p) < 3 ? (r) : (r) + 1, ((p) + 1) & 3, m); else if (m < 6) CB((r) + 1, 2 * (p) + m - 4); SB(); } CKEEP(r, p); } while (0)
#define CONV(r) do { CPAIR(r, 0); CPAIR(r, 1); CPAIR(r, 2); CPAIR(r, 3); } while (0)
      lds_wait_ge(L0 + L_SY + 24, 8u); __builtin_amdgcn_s_setprio(2);
      STAMP(24);
#pragma unroll
      for (int i = 0; i < 4; ++i) CA(0, 0, 0, i);
#pragma unroll
      for (int mu = 0; mu < 8; ++mu) CB(0, mu);
#pragma nounroll
      for (int it = 0; it < 2; ++it) {
        CONV(0); CPAIR(1, 0); CPAIR(1, 1); CPAIR(1, 2);
        if (it) {
          asm volatile("s_waitcnt vmcnt(0) lgkmcnt(0)" ::: "memory"); LDS_SET(L0 + L_SY + 48 + wj * 4, 1u); LDS_SET(L0 + L_SY + wj * 4, 1u); }
        CPAIR(1, 3);
        cA += 128; cB += 128; lim -= 2; PIN(cA); PIN(cB);
      }
    }
    asm volatile("s_nop 7" ::: "memory");
    STAMP(27);
    {
      const unsigned mine = L0 + L_SY + 48 + wj * 4, other = L0 + L_SY + 48 + (wj ^ 1) * 4;
      const float dsc = vch ? bhi(dpair) : blo(dpair);
      u32x2 uq[4];
#pragma unroll
      for (int c = 0; c < 4; ++c) uq[c] = lds_ld<u32x2>(uq_ + (768 - 256 * c) * 2);
      lds_spin_ge(other, 1u);
      f32x4 y[4];
      if (vrh) {
        asm volatile("" ::: "memory");
#pragma unroll
        for (int c = 0; c < 4; ++c) { lds_st<f32x4>(xo_ + c * 1024, ya[c]); y[c] = ya[4 + c]; PIN(y[c]); }
      } else {
#pragma unroll
        for (int c = 0; c < 4; ++c) { lds_st<f32x4>(xo_ + c * 1024, ya[4 + c]); y[c] = ya[c]; PIN(y[c]); }
        asm volatile("" ::: "memory");
      }
      asm volatile("s_waitcnt lgkmcnt(0)" ::: "memory");
      *(volatile LAS unsigned*)(size_t)mine = 2u;
#pragma unroll
      for (int c = 0; c < 4; ++c)
#pragma unroll
        for (int e = 0; e < 4; ++e) {
          const unsigned uu = (e & 2) ? uq[c].x : uq[c].y;
          float s = __builtin_fmaf((e & 1) ? blo(uu) : bhi(uu), dsc, y[c][e]); PIN(s); y[c][e] = s;
        }
      lds_spin_ge(other, 2u);
      f32x4 o[4];
#pragma unroll
      for (int c = 0; c < 4; ++c) o[c] = lds_ld<f32x4>(xb + c * 1024);
      SB();
      u32x2 zp[4];
#pragma unroll
      for (int c = 0; c < 4; ++c) {
        float z[4];
#pragma unroll
        for (int e = 0; e < 4; ++e) { z[e] = y[c][e] + o[c][e]; asm("" : "+v"(z[e])); }
        zp[c] = u32x2{min(cvtpk(z[0], z[1]), POISON - 1u), min(cvtpk(z[2], z[3]), POISON - 1u)};
      }
#pragma unroll
      for (int c = 0; c < 4; ++c) lds_st<u32x2>(zs_ + c * 512, zp[c]);
#pragma unroll
      for (int i = 0; i < 2; ++i)
        st16_dev(rW, vzs + i * 1024, lds_ld<u32x4>(xb + i * 1024));
    }
    STAMP(26);
    if (lane == 0) lds_inc(L0 + L_SY + 20);
    STAMP(9);
    lds_spin_ge(L0 + L_SY + 80 + wj * 4, 1u);
    STAMP(31);
  } else {
    __builtin_amdgcn_s_setprio(3);
    u32x2 sh[2]; u32x4 xr[2];
#pragma unroll
    for (int i = 0; i < 2; ++i) xr[i] = ld16(rX, v1st + i * 1024);
    {
      const rsrc_t rK = mk_rsrc(kin, S_ * D_ * 2), rV = mk_rsrc(vin, S_ * D_ * 2);
      const unsigned kb = (unsigned)((64 * idx + l3) * 1024 + pan * 128), fl = (unsigned)(l7 ^ ((l3 >> 1) & 1));
#pragma unroll
      for (int i = 0; i < 2; ++i) {
        const int g = 2 * wj + i;
        dma16(rK, kb + ((fl ^ (((g + 1) & 3) << 1)) << 4), g * 8192, L0 + L_KA + (g + 1) * 1024);
        dma16(rV, kb + ((fl ^ ((g & 3) << 1)) << 4), g * 8192, L0 + L_VA + g * 1024);
      }
      if (wj == 0) dma16(rK, idx == 0 ? OOB : kb - 8192 + (fl << 4), 0, L0 + L_KA);
    }
    u32x2 gen = ld8_dev(rW, WS_GEN);
    {
      const rsrc_t rSh = mk_rsrc(shk, D_ * 4 * 2);
#pragma unroll
      for (int i = 0; i < 2; ++i) sh[i] = ld8(rSh, (unsigned)((64 * pan + 16 * wj + 8 * i + 4 * (G & 1) + pp) * 8));
    }
    asm volatile("s_barrier" ::: "memory");
    STAMP(3);
    const int tp = wj & 1, ch = wj >> 1;
    f32x16 acc[2][3];
    f32x4 cs[3];
    unsigned ax[4], aw[2][3], ak[5];
    unsigned vkv = WS_KVT + (unsigned)((64 * pan + 16 * wj + l3) * 4096 + (1984 - 64 * idx + 8 * l7) * 2);
    unsigned vst0 = lane < 2 ? WS_ST + (unsigned)((8 * aa + 2 * wj + lane) * 8) : OOB;
    int src4 = 4 * (16 * ((((7 - l7) >> 2) << 1) | (l3 >> 2)) + 4 * ((7 - l7) & 3) + (l3 & 3));
    const unsigned so_ = ((lane & 15) == 4 * (G & 1)) ? 0x3f803f80u : 0u;
    bf16x8 sel = __builtin_bit_cast(bf16x8, u32x4{so_, so_, so_, so_});
    {
      const unsigned ax0 = L0 + (64 * tp + l31) * 128 + (((4 * tp + hh) ^ ((l31 >> 1) & 7)) << 4);
      const unsigned aw0 = L0 + 16384 + (G & 1) * 12288 + (8 * hh + qq) * 192 + ((pp & 1) << 4) + 8 * (pp >> 1);
#pragma unroll
      for (int s = 0; s < 4; ++s) { ax[s] = ax0 ^ (s << 5); PIN(ax[s]); }
#pragma unroll
      for (int i = 0; i < 3; ++i) {
        const unsigned a_ = aw0 + (((3 * ch + i) ^ (G & 1)) << 5);
        aw[0][i] = a_ + tp * 6144; aw[1][i] = a_ + 6144 - tp * 6144; PIN(aw[0][i]); PIN(aw[1][i]);
      }
#pragma unroll
      for (int j = 0; j < 5; ++j) { const int rr = 8 * (4 * (G >> 1) + pp) + qq + (j < 3 ? 4 + 4 * j : 4 * (j - 3));
        ak[j] = L0 + (j < 3 ? L_KA : L_VA) + rr * 128 + (((2 * wj) ^ fsw(rr)) << 4) + 8 * (G & 1); PIN(ak[j]); }
      PIN(vkv); PIN(vst0); PIN(src4); PIN(sel);
    }
    {
      asm volatile("s_waitcnt vmcnt(7)" : "+v"(xr[0]), "+v"(xr[1])::"memory");
      STAMP(14);
      const f32x2 x0 = {blo(__builtin_amdgcn_readfirstlane(xr[0][0])), blo(__builtin_amdgcn_readfirstlane(xr[1][0]))};
      f32x2 d[8];
#pragma unroll
      for (int e = 0; e < 4; ++e) { d[2 * e] = f32x2{blo(xr[0][e]), blo(xr[1][e])} - x0; d[2 * e + 1] = f32x2{bhi(xr[0][e]), bhi(xr[1][e])} - x0; }
      const f32x2 sm = ((d[0] + d[1]) + (d[2] + d[3])) + ((d[4] + d[5]) + (d[6] + d[7]));
      const f32x2 sq = ((d[0] * d[0] + d[1] * d[1]) + (d[2] * d[2] + d[3] * d[3])) + ((d[4] * d[4] + d[5] * d[5]) + (d[6] * d[6] + d[7] * d[7]));
      float s0 = sm[0], s1 = sm[1], q0 = sq[0], q1 = sq[1];
      wave_sum4(s0, s1, q0, q1);
      const float m = (lane ? s1 : s0) * (1.0f / D_), var = (lane ? q1 : q0) * (1.0f / D_) - m * m;
      unsigned ms0 = cvtpk(m + (lane ? x0[1] : x0[0]), __builtin_amdgcn_sqrtf(rbf(rbf(__builtin_fmaxf(var, 0.f)) + 1e-5f)));
      asm volatile("s_waitcnt vmcnt(0)" : "+v"(gen), "+v"(sh[0]), "+v"(sh[1]), "+v"(ms0)::"memory");
      STAMP(15);
      elo = __builtin_amdgcn_readfirstlane(gen.x) + 1u;
      ehi = __builtin_amdgcn_readfirstlane(gen.y) + (elo == 0u ? 1u : 0u);
      *(volatile LAS u32x2*)(size_t)(L0 + L_A + wj * 8) = u32x2{elo, ehi};
      st8_dev(rW, vst0, u32x2{ms0, elo});
      f32x2 sk[4] = {{blo(sh[0].x), blo(sh[1].x)}, {bhi(sh[0].x), bhi(sh[1].x)}, {blo(sh[0].y), blo(sh[1].y)}, {bhi(sh[0].y), bhi(sh[1].y)}};
      PIN(sk[0]); PIN(sk[1]); PIN(sk[2]); PIN(sk[3]);
      for (unsigned n = 0; n < SPIN_MAX * 64; ++n) {
        const u32x2 f = *(volatile LAS u32x2*)(size_t)(L0 + L_A + pp * 8);
        if (__all(f.x == elo && f.y == ehi)) break;
      }
      asm volatile("" ::: "memory");
      STAMP(1);
      u32x2 t[2][5];
#pragma unroll
      for (int j = 0; j < 5; ++j) { t[0][j] = __builtin_bit_cast(u32x2, trd(ak[j])); t[1][j] = __builtin_bit_cast(u32x2, trd(ak[j] ^ 16)); }
      SB();
      f32x2 kk[20], r[8];
#pragma unroll
      for (int j = 0; j < 5; ++j) {
        kk[4 * j] = f32x2{blo(t[0][j].x), blo(t[1][j].x)}; kk[4 * j + 1] = f32x2{bhi(t[0][j].x), bhi(t[1][j].x)};
        kk[4 * j + 2] = f32x2{blo(t[0][j].y), blo(t[1][j].y)}; kk[4 * j + 3] = f32x2{bhi(t[0][j].y), bhi(t[1][j].y)};
      }
#pragma unroll
      for (int e = 0; e < 8; ++e) r[e] = kk[e + 1] * sk[3];
#pragma unroll
      for (int j = 2; j >= 0; --j)
#pragma unroll
        for (int e = 0; e < 8; ++e) r[e] = __builtin_elementwise_fma(kk[e + 4 - j], sk[j], r[e]);
#pragma unroll
      for (int e = 0; e < 8; ++e) r[e] *= kk[12 + e];
      u32x4 po[2];
#pragma unroll
      for (int i = 0; i < 2; ++i)
#pragma unroll
        for (int e = 0; e < 4; ++e) po[i][e] = (unsigned)__builtin_amdgcn_ds_bpermute(src4, (int)cvtpk(r[7 - 2 * e][i], r[6 - 2 * e][i]));
      st16_dev(rW, vkv, po[0]); st16_dev(rW, vkv + 32768, po[1]);
      __builtin_amdgcn_s_setprio(1);
    }
    STAMP(2);
    {
      bf16x8 xf[2][2]; s16x4 wf[2][3][2];
#define PINA(t, i) PIN(acc[t][i])
#define M1(p, t, i) do { acc[t][i] = MFMA32(cat(wf[p][i][0], wf[p][i][1]), xf[p][t], acc[t][i]); PINA(t, i); } while (0)
#define MS(p, i, on) do { if (on) { cs[i] = MFMA16(sel, cat(wf[p][i][0], wf[p][i][1]), cs[i]); PIN(cs[i]); } } while (0)
#define RW(s, i) do { wf[(s) & 1][i][0] = trd(aw[(s) >> 1][i] + ((s) & 1) * 3072); wf[(s) & 1][i][1] = trd(aw[(s) >> 1][i] + ((s) & 1) * 3072 + 768); } while (0)
#define RX(s, t) xf[(s) & 1][t] = lds_ld<bf16x8>(ax[s] + (t) * 4096)
#define KEEP(p) do { asm volatile("" ::"v"(wf[p][0][0]), "v"(wf[p][0][1]), "v"(wf[p][1][0]), "v"(wf[p][1][1]), "v"(wf[p][2][0]), "v"(wf[p][2][1]), "v"(xf[p][0]), "v"(xf[p][1])); } while (0)
#define ADV(a) do { a += dl; PIN(a); } while (0)
#define STEP(p, s, on) do { M1(p, 0, 0); RW(s, 0); SB(); M1(p, 1, 0); RX(s, 0); RX(s, 1); SB(); M1(p, 0, 1); RW(s, 1); SB();  \
        M1(p, 1, 1); RW(s, 2); SB(); M1(p, 0, 2);                                                                             \
        if ((s) == 3) { ADV(ax[0]); ADV(ax[1]); ADV(ax[2]); ADV(ax[3]); ADV(aw[0][0]); } SB(); M1(p, 1, 2);                     \
        if ((s) == 3) { ADV(aw[0][1]); ADV(aw[0][2]); ADV(aw[1][0]); ADV(aw[1][1]); ADV(aw[1][2]); }                            \
        MS(p, 0, on); MS(p, 1, on); MS(p, 2, on); KEEP(p); SB(); } while (0)
      LBAR();
      STAMP(16);
      RW(0, 0); RX(0, 0); RX(0, 1); RW(0, 1); RW(0, 2); SB();
      unsigned dl = SLOT;
#pragma unroll
      for (int i = 0; i < 3; ++i) {
        cs[i] = f32x4{0.f, 0.f, 0.f, 0.f};
#pragma unroll
        for (int r = 0; r < 16; ++r) { acc[0][i][r] = 0.f; acc[1][i][r] = 0.f; }
      }
      STEP(0, 1, true);
#pragma nounroll
      for (int c = 0; c < 7; ++c) {
        dl = (c == 3) ? 0u - 3u * SLOT : (unsigned)SLOT;
        STEP(1, 2, true);
        if (c == 2) WAITV(0);
        STEP(0, 3, false);
        LBAR();
        STEP(1, 0, false); STEP(0, 1, true);
      }
      STAMP(4);
      STEP(1, 2, true);
#pragma unroll
      for (int i = 0; i < 3; ++i) lds_st<float>(L0 + L_CS + (wj * 3 + i) * 256 + lane * 4, cs[i][0]);
      STEP(0, 3, false);
      LBAR();
      float co[3]; unsigned ms[2];
#pragma unroll
      for (int i = 0; i < 3; ++i) co[i] = lds_ld<float>(L0 + L_CS + ((wj ^ 1) * 3 + i) * 256 + lane * 4);
#pragma unroll
      for (int t = 0; t < 2; ++t) ms[t] = lds_ld<unsigned>(L0 + L_ST + (64 * tp + 32 * t + l31) * 4);
      SB();
      M1(1, 0, 0); M1(1, 1, 0); M1(1, 0, 1); M1(1, 1, 1); M1(1, 0, 2); M1(1, 1, 2); SB();
      float k0[2], k1[2], s2[2];
      u32x4 bm[2];
#pragma unroll
      for (int t = 0; t < 2; ++t) {
        const unsigned mn = (ms[t] & 0xffffu) ^ 0x8000u;
        bm[t] = u32x4{mn | (mn << 16), mn, 0u, 0u};
        const float sd = bhi(ms[t]), rs = __builtin_amdgcn_rcpf(sd);
        k0[t] = rs * -2.3022081983f; k1[t] = k0[t] * rs * rs * 0.044715f; s2[t] = sd * sd;
      }
#pragma unroll
      for (int i = 0; i < 3; ++i) {
        const float c = cs[i][0] + co[i], c1 = c - rbf(c), c2 = c1 - rbf(c1);
        const u32x4 am = {cvtpk(c, c1), cvtpk(c2, 0.f), 0u, 0u};
#pragma unroll
        for (int t = 0; t < 2; ++t) { acc[t][i] = MFMA32(__builtin_bit_cast(bf16x8, am), __builtin_bit_cast(bf16x8, bm[t]), acc[t][i]); PINA(t, i); }
      }
      STAMP(5);
      const unsigned vhs = WS_H + (unsigned)(((4 * rt + 2 * tp) * 86 + 6 * cg + 3 * ch) * 1024 + lane * 16);
#pragma unroll
      for (int i = 0; i < 3; ++i) {
        const unsigned vo = (vhs + i * 1024) | (6 * cg + 3 * ch + i < 86 ? 0u : OOB);
#pragma unroll
        for (int t = 0; t < 2; ++t) {
          f32x2 g[4], zz[4], gu[4];
#pragma unroll
          for (int r = 0; r < 4; ++r) {
            g[r] = f32x2{acc[t][i][2 * r], acc[t][i][2 * r + 1]};
            zz[r] = g[r] * __builtin_elementwise_fma(g[r] * g[r], f32x2{k1[t], k1[t]}, f32x2{k0[t], k0[t]});
          }
#pragma unroll
          for (int r = 0; r < 4; ++r) {
            zz[r] = f32x2{__builtin_amdgcn_exp2f(zz[r][0]), __builtin_amdgcn_exp2f(zz[r][1])};
            gu[r] = g[r] * f32x2{acc[t][i][8 + 2 * r], acc[t][i][9 + 2 * r]};
          }
          u32x4 hq;
#pragma unroll
          for (int r = 0; r < 4; ++r) {
            const f32x2 dd = __builtin_elementwise_fma(zz[r], f32x2{s2[t], s2[t]}, f32x2{s2[t], s2[t]});
            const f32x2 hv = gu[r] * f32x2{__builtin_amdgcn_rcpf(dd[0]), __builtin_amdgcn_rcpf(dd[1])};
            hq[r] = cvtpk(hv[0], hv[1]);
          }
          st16_dev(rW, vo + t * (86 * 1024), hq);
        }
      }
    }
    if (lane == 0) lds_inc(L0 + L_SY + 24);
    STAMP(6);
    const unsigned vd = VD();
    PARK();
    WAITV(0);
    if (lane == 0 && lds_inc(L0 + L_SY + 16) == 3u && cg < 15) { u32x2 e = {elo, ehi}; st8_dev(rW, WS_FH + (unsigned)((rt * 16 + cg) * 128), e); }
    STAMP(8);
    lds_wait_ge(L0 + L_SY + 64 + wj * 4, 1u);
#pragma unroll
    for (int j = 0; j < 4; ++j) WDU(14 + j, L_B1 + wj * 8192 + j * 2048);
#pragma unroll
    for (int j = 0; j < 4; ++j) WDU(18 + j, L_B2 + wj * 10240 + j * 2048);
    xo = ld16(rX, VXO()); st2 = ld8_dev(rW, VS2());
    {
      const int gf = ks0 / 6, gl = (ks0 + 20 + (wj < 2)) / 6, gi = min(gf + l7, gl);
      const unsigned vfh = WS_FH + (unsigned)(((r64 >> 1) * 16 + gi) * 128);
      u32x2 fh = ld8_dev(rW, vfh);
      asm volatile("s_waitcnt vmcnt(0)" : "+v"(fh), "+v"(xo), "+v"(st2)::"memory");
      if (!__all(fh.x == elo && fh.y == ehi)) fh = ld8_dev(rW, vfh);
      XN();
      asm volatile("s_waitcnt vmcnt(0)" : "+v"(fh)::"memory");
      spin_flags(rW, vfh, fh, elo, ehi);
    }
    LDS_SET(L0 + L_SY + 80 + wj * 4, 1u);
  }
  STAMP(10);
  {
    u32x4 hf[22];
    const unsigned vh = WS_H + (unsigned)(((2 * r64 + (w >> 2)) * 86 + ks0) * 1024 + lane * 16);
#define HL(j) hf[j] = ld16_dev(rW, vh, __builtin_amdgcn_readfirstlane(min(PH(j), klast) * 1024))
#pragma unroll
    for (int j = 0; j < HWIN; ++j) HL(j);
    const unsigned vz = WS_ZT + (unsigned)((64 * ct + 8 * w + l3) * 4096 + r64 * 128 + l7 * 16);
    u32x4 zr;
    STAMP(28);
    const unsigned pq = w < 4 ? L0 + L_P0 : L0 + L_B3, ps = w < 4 ? 8192 : 10240;
    unsigned pw[4], pr[4], zw, zt_, zsrc, vo;
#pragma unroll
    for (int i = 0; i < 4; ++i) {
      pw[i] = pq + wj * ps + l31 * 256 + (((hh ^ l7) ^ (2 * i)) << 4); PIN(pw[i]);
      pr[i] = pq + i * ps + (8 * wj + l3) * 256 + (((2 * l7) ^ l3) << 4); PIN(pr[i]);
    }
    { const int r = 8 * w + l3, r2 = 8 * (4 * (G >> 1) + pp) + qq;
      zw = L0 + L_ZT + r * 128 + ((l7 ^ fsw(r)) << 4); zt_ = L0 + L_ZT + r2 * 128 + ((w ^ fsw(r2)) << 4) + 8 * (G & 1);
      zsrc = 4 * (16 * (((l7 >> 2) << 1) | (l3 >> 2)) + 4 * (l7 & 3) + (l3 & 3)); vo = VXO(); PIN(zw); PIN(zt_); PIN(zsrc); PIN(vo); }
    lds_wait_ge(L0 + L_SY + wj * 4, 1u);
    const unsigned a1 = AD() + L_B1 + wj * 8192, a2 = AD() + L_B2 + wj * 10240;
    f32x16 fo[2];
#pragma unroll
    for (int r = 0; r < 16; ++r) { fo[0][r] = 0.f; fo[1][r] = 0.f; }
#define G2(j) do { if ((j) + 2 >= 8 && (j) + 2 < 22) { wpk[((j) + 2) & 7][0] = WDF(UA((j) + 2), 0); wpk[((j) + 2) & 7][1] = WDF(UA((j) + 2), 1); } \
      asm volatile("s_waitcnt vmcnt(%1)" : "+v"(hf[j]) : "i"((j) + HWIN <= 22 ? HWIN - 1 : (j) <= ZAT ? 21 - (j) : 22 - (j)) : "memory"); \
      fo[0] = MFMA32(wpk[(j) & 7][0], __builtin_bit_cast(bf16x8, hf[j]), fo[0]);                                      \
      fo[1] = MFMA32(wpk[(j) & 7][1], __builtin_bit_cast(bf16x8, hf[j]), fo[1]); PIN(fo[0]); PIN(fo[1]); SB();      \
      if ((j) + HWIN < 22) HL(((j) + HWIN) % 22); if ((j) == ZAT) zr = ld16_dev(rW, vz, 0); } while (0)
    G2(0); STAMP(29); G2(1); G2(2); G2(3); G2(4); G2(5); G2(6); G2(7); G2(8); G2(9); G2(10);
    G2(11); G2(12); G2(13);
    if (w < 4) LDS_SET(L0 + L_SY + 96 + wj * 4, 1u);
    G2(14); G2(15); G2(16); G2(17); G2(18);
    lds_spin_ge(L0 + L_SY + (w < 4 ? 20 : 96 + wj * 4), w < 4 ? 4u : 1u);
    G2(19); G2(20); G2(21);
    STAMP(11);
#pragma unroll
    for (int dt = 0; dt < 2; ++dt)
#pragma unroll
      for (int i = 0; i < 4; ++i)
        lds_st<f32x4>(pw[i] + dt * 128, f32x4{fo[dt][4 * i], fo[dt][4 * i + 1], fo[dt][4 * i + 2], fo[dt][4 * i + 3]});
    asm volatile("s_waitcnt vmcnt(0)" : "+v"(zr)::"memory");
    for (unsigned n = 0; __builtin_expect(__any(max(max(zr[0], zr[1]), max(zr[2], zr[3])) == POISON) && n < SPIN_MAX, 0); ++n) {
      zr = ld16_dev(rW, vz, 0);
      asm volatile("s_waitcnt vmcnt(0)" : "+v"(zr)::"memory");
    }
    STAMP(30);
    if (w >= 4) lds_spin_ge(L0 + L_SY + 20, 4u);
    lds_st<u32x4>(zw, zr);
    LBAR();
    STAMP(12);
    const u32x2 zt0 = __builtin_bit_cast(u32x2, trd(zt_)), zt1 = __builtin_bit_cast(u32x2, trd(zt_ + 512));
    f32x4 pa[4], pb[4];
#pragma unroll
    for (int k = 0; k < 4; ++k) { pa[k] = lds_ld<f32x4>(pr[k]); pb[k] = lds_ld<f32x4>(pr[k] ^ 16); }
    SB();
    const unsigned zq[4] = {(unsigned)__builtin_amdgcn_ds_bpermute(zsrc, (int)zt0.x), (unsigned)__builtin_amdgcn_ds_bpermute(zsrc, (int)zt0.y),
                            (unsigned)__builtin_amdgcn_ds_bpermute(zsrc, (int)zt1.x), (unsigned)__builtin_amdgcn_ds_bpermute(zsrc, (int)zt1.y)};
    const f32x4 p0 = (pa[0] + pa[1]) + (pa[2] + pa[3]), p1 = (pb[0] + pb[1]) + (pb[2] + pb[3]);
    u32x4 ov;
#pragma unroll
    for (int i = 0; i < 4; ++i) {
      const f32x2 F = i < 2 ? f32x2{p0[2 * i], p0[2 * i + 1]} : f32x2{p1[2 * i - 4], p1[2 * i - 3]};
      const f32x2 t = F * f32x2{-1.4426950408889634f, -1.4426950408889634f};
      const f32x2 d = f32x2{__builtin_amdgcn_exp2f(t[0]), __builtin_amdgcn_exp2f(t[1])} + f32x2{1.0f, 1.0f};
      const f32x2 g = F * f32x2{__builtin_amdgcn_rcpf(d[0]), __builtin_amdgcn_rcpf(d[1])};
      const f32x2 o = __builtin_elementwise_fma(xn[i] * f32x2{blo(zq[i]), bhi(zq[i])}, g, f32x2{blo(xo[i]), bhi(xo[i])});
      ov[i] = cvtpk(o[0], o[1]);
    }
    __builtin_amdgcn_raw_buffer_store_b128(ov, mk_rsrc(out, S_ * D_ * 2), vo, 0, OUTAUX);
  }
}

extern "C" void kernel_launch(void* const* d_in, const int* in_sizes, int n_in, void* d_out, int out_size, void* d_ws, size_t ws_size,
                              hipStream_t stream) {
  (void)in_sizes; (void)n_in; (void)out_size;
  (void)hipFuncSetAttribute((const void*)h3_geglu_fused, hipFuncAttributeMaxDynamicSharedMemorySize, LDS_BYTES);
  h3_geglu_fused<<<256, 512, LDS_BYTES, stream>>>((const u16*)d_in[0], (const u16*)d_in[1], (const u16*)d_in[2], (const u16*)d_in[3],
                                                  (const u16*)d_in[4], (const u16*)d_in[5], (const u16*)d_in[7], (const u16*)d_in[8],
                                                  (const u16*)d_in[9], (u16*)d_out, (char*)d_ws + WS_BASE(ws_size));
}
```

```cpp
#include <hip/hip_runtime.h>
#include <hip/hip_bf16.h>

typedef unsigned short u16;
typedef unsigned long long u64;
typedef __attribute__((ext_vector_type(8))) short bf16x8;
typedef __attribute__((ext_vector_type(4))) short s16x4;
typedef __attribute__((ext_vector_type(2))) __bf16 bf16x2_t;
typedef __attribute__((ext_vector_type(2))) float f32x2;
typedef __attribute__((ext_vector_type(4))) float f32x4;
typedef __attribute__((ext_vector_type(16))) float f32x16;
typedef __attribute__((ext_vector_type(4))) unsigned u32x4;
typedef __attribute__((ext_vector_type(2))) unsigned u32x2;
#define LAS __attribute__((address_space(3)))
#define GAS __attribute__((address_space(1)))
typedef __amdgpu_buffer_rsrc_t rsrc_t;

constexpr int S_ = 2048, D_ = 512, DF = 1376;
constexpr unsigned WS_GEN = 0, WS_FA = 4096, WS_FH = WS_FA + 256 * 128, WS_FZ = WS_FH + 256 * 128;
constexpr unsigned WS_ST = WS_FZ + 256 * 128;
constexpr unsigned WS_KVT = 1u << 20;
constexpr unsigned WS_ZT = WS_KVT + (2u << 20);
constexpr unsigned WS_H = WS_ZT + (2u << 20);
constexpr unsigned WS_BYTES = WS_H + 64 * 86 * 1024;
constexpr int SLOT = 40960;
constexpr int L_CV = 3 * SLOT;
constexpr int L_KA = L_CV, L_VA = L_KA + 9216;
constexpr int L_B1 = 0, L_B2 = SLOT, L_B3 = 2 * SLOT;
constexpr int L_CS = 32768;
constexpr int L_ST = L_CS + 3072;
constexpr int L_SY = L_ST + 512;
constexpr int L_A = L_VA + 8192;
constexpr int L_P0 = L_CV + 8192;
constexpr int L_U0 = 36864, L_U1 = L_CV + 33280, L_ZT = L_CV;
constexpr int LDS_BYTES = 163840;
constexpr unsigned OOB = 0x80000000u;
constexpr unsigned POISON = 0xffffffffu;
constexpr unsigned SPIN_MAX = 1u << 18;

#define LPRIO 2
#define XSLEEP 4
#define ROT1 ((rt + cg) & 7)
#define ROT2 (((xcd * 4 + (idx >> 3)) * 11) >> 4)
#define HWIN 10
#define STEPMODE 1
#define NOSTATS 0
#define WS_BASE(sz) 0
#define ZSLEEP 4
#define ABL_PLAIN 0
#define ZAT 15
#define OUTAUX 16
#define S0EARLY 1
#define SETUPPRIO 2
#define S0POS 3
#define S0C 1
#define NOMS 0
#define MFMA32(a, b, c) __builtin_amdgcn_mfma_f32_32x32x16_bf16(a, b, c, 0, 0, 0)
#define MFMA16(a, b, c) __builtin_amdgcn_mfma_f32_16x16x32_bf16(a, b, c, 0, 0, 0)
#define WAIT_BAR(N) asm volatile("s_waitcnt vmcnt(" #N ") lgkmcnt(0)\n\ts_barrier" ::: "memory")
#define LBAR() asm volatile("s_waitcnt lgkmcnt(0)\n\ts_barrier" ::: "memory")
#define WAITV(N) asm volatile("s_waitcnt vmcnt(" #N ")" ::: "memory")
#define SB() __builtin_amdgcn_sched_barrier(0)

__device__ __forceinline__ rsrc_t mk_rsrc(const void* p, unsigned bytes) {
  return __builtin_amdgcn_make_buffer_rsrc((void*)p, (short)0, (int)bytes, 0x00020000);
}
__device__ __forceinline__ void dma16(rsrc_t rs, unsigned voff, unsigned soff, unsigned lds) {
  asm volatile("s_mov_b32 m0, %0\n\ts_nop 0\n\tbuffer_load_dwordx4 %1, %2, %3 offen lds" ::"s"(lds), "v"(voff), "s"(rs), "s"(soff) : "memory");
}
__device__ __forceinline__ u32x4 ld16(rsrc_t rs, unsigned voff) {
  u32x4 r; asm volatile("buffer_load_dwordx4 %0, %1, %2, 0 offen" : "=v"(r) : "v"(voff), "s"(rs) : "memory"); return r;
}
__device__ __forceinline__ u32x4 ld16_dev(rsrc_t rs, unsigned voff, unsigned soff) {
  u32x4 r; asm volatile("buffer_load_dwordx4 %0, %1, %2, %3 offen sc1" : "=v"(r) : "v"(voff), "s"(rs), "s"(soff) : "memory"); return r;
}
__device__ __forceinline__ u32x2 ld8(rsrc_t rs, unsigned voff) {
  u32x2 r; asm volatile("buffer_load_dwordx2 %0, %1, %2, 0 offen" : "=v"(r) : "v"(voff), "s"(rs) : "memory"); return r;
}
__device__ __forceinline__ u32x2 ld8_dev(rsrc_t rs, unsigned voff) {
  u32x2 r; asm volatile("buffer_load_dwordx2 %0, %1, %2, 0 offen sc1" : "=v"(r) : "v"(voff), "s"(rs) : "memory"); return r;
}
#define PIN(x) asm volatile("" : "+v"(x))
__device__ __forceinline__ void st16_dev(rsrc_t rs, unsigned voff, u32x4 v) { __builtin_amdgcn_raw_buffer_store_b128(v, rs, voff, 0, 16); }
__device__ __forceinline__ void st8_dev(rsrc_t rs, unsigned voff, u32x2 v) { __builtin_amdgcn_raw_buffer_store_b64(v, rs, voff, 0, 16); }

__device__ __forceinline__ unsigned cvtpk(float lo, float hi) {
  bf16x2_t r = {(__bf16)lo, (__bf16)hi};
  return __builtin_bit_cast(unsigned, r);
}
__device__ __forceinline__ float blo(unsigned w) { return __uint_as_float(w << 16); }
__device__ __forceinline__ float bhi(unsigned w) { return __uint_as_float(w & 0xffff0000u); }
__device__ __forceinline__ float rbf(float f) { return blo(cvtpk(f, f)); }
__device__ __forceinline__ s16x4 trd(unsigned a) { return __builtin_amdgcn_ds_read_tr16_b64_v4i16((LAS s16x4*)(size_t)a); }
__device__ __forceinline__ bf16x8 cat(s16x4 a, s16x4 b) { return __builtin_shufflevector(a, b, 0, 1, 2, 3, 4, 5, 6, 7); }
template <class T> __device__ __forceinline__ T lds_ld(unsigned a) { return *(const LAS T*)(size_t)a; }
template <class T> __device__ __forceinline__ void lds_st(unsigned a, T v) { *(LAS T*)(size_t)a = v; }
template <int CTRL> __device__ __forceinline__ float dpp_add(float x) {
  return x + __uint_as_float(__builtin_amdgcn_update_dpp(0u, __float_as_uint(x), CTRL, 0xf, 0xf, true));
}
__device__ __forceinline__ float swap_add(float x, bool half) {
  if (half) { auto r = __builtin_amdgcn_permlane32_swap(__float_as_uint(x), __float_as_uint(x), false, false); return __uint_as_float(r[0]) + __uint_as_float(r[1]); }
  auto r = __builtin_amdgcn_permlane16_swap(__float_as_uint(x), __float_as_uint(x), false, false); return __uint_as_float(r[0]) + __uint_as_float(r[1]);
}
__device__ __forceinline__ void wave_sum4(float& a, float& b, float& c, float& d) {
  a = dpp_add<0x128>(a); b = dpp_add<0x128>(b); c = dpp_add<0x128>(c); d = dpp_add<0x128>(d);
  a = dpp_add<0x124>(a); b = dpp_add<0x124>(b); c = dpp_add<0x124>(c); d = dpp_add<0x124>(d);
  a = dpp_add<0x122>(a); b = dpp_add<0x122>(b); c = dpp_add<0x122>(c); d = dpp_add<0x122>(d);
  a = dpp_add<0x121>(a); b = dpp_add<0x121>(b); c = dpp_add<0x121>(c); d = dpp_add<0x121>(d);
  a = swap_add(a, false); b = swap_add(b, false); c = swap_add(c, false); d = swap_add(d, false);
  a = swap_add(a, true); b = swap_add(b, true); c = swap_add(c, true); d = swap_add(d, true);
}
__device__ __forceinline__ void lds_wait_ge(unsigned a, unsigned v) {
  for (unsigned n = 0; *(volatile LAS unsigned*)(size_t)a < v && n < SPIN_MAX * 16; ++n) __builtin_amdgcn_s_sleep(1);
  asm volatile("" ::: "memory");
}
__device__ __forceinline__ void lds_spin_ge(unsigned a, unsigned v) {
  for (unsigned n = 0; *(volatile LAS unsigned*)(size_t)a < v && n < SPIN_MAX * 64; ++n) {}
  asm volatile("" ::: "memory");
}
__device__ __forceinline__ unsigned lds_inc(unsigned a) {
  return __hip_atomic_fetch_add((LAS unsigned*)(size_t)a, 1u, __ATOMIC_RELAXED, __HIP_MEMORY_SCOPE_WORKGROUP);
}
__device__ __forceinline__ int fsw(int r) { return ((r >> 1) & 1) | (((r >> 3) & 3) << 1); }
__device__ __forceinline__ void spin_flags(rsrc_t rs, unsigned voff, u32x2 f, unsigned lo, unsigned hi) {
  for (unsigned n = 0; !__all(f.x == lo && f.y == hi) && n < SPIN_MAX; ++n) {
    __builtin_amdgcn_s_sleep(1);
    f = ld8_dev(rs, voff); WAITV(0); PIN(f);
  }
}
__device__ __forceinline__ u32x2 spin_tag(rsrc_t rs, unsigned voff, u32x2 f, unsigned tag) {
  for (unsigned n = 0; !__all(f.y == tag) && n < SPIN_MAX; ++n) {
    __builtin_amdgcn_s_sleep(1);
    f = ld8_dev(rs, voff); WAITV(0); PIN(f);
  }
  return f;
}
#define STAMP(i)

__global__ __launch_bounds__(512, 2) void h3_geglu_fused(const u16* __restrict__ x, const u16* __restrict__ kin, const u16* __restrict__ vin,
                                                         const u16* __restrict__ shk, const u16* __restrict__ ssk, const u16* __restrict__ ssd,
                                                         const u16* __restrict__ wg, const u16* __restrict__ wu, const u16* __restrict__ wd,
                                                         u16* __restrict__ out, char* __restrict__ ws) {
  extern __shared__ __attribute__((aligned(16))) char smem[];
  const unsigned L0 = (unsigned)(size_t)(LAS char*)smem;
  const int tid = threadIdx.x, lane = tid & 63;
  const int w = __builtin_amdgcn_readfirstlane(tid >> 6), wj = w & 3;
  STAMP(0);
  const int b = blockIdx.x, xcd = b & 7, idx = b >> 3, rh = xcd & 1, q4 = xcd >> 1;
  const int pan = 4 * rh + q4, aa = 32 * pan + idx;
  const int l3 = lane >> 3, l7 = lane & 7, G = lane >> 4, qq = (lane >> 2) & 3, pp = lane & 3, hh = lane >> 5, l31 = lane & 31;
  const int e8 = idx & 7, m3 = (23 - idx) >> 31, m4 = (3 - e8) >> 31;
  const int cgA = ((q4 * 7 + 1) >> 1) + (idx >> 3), cgS = 3 + 7 * (q4 >> 1), cgB = cgS + ((14 + ((e8 >> 1) & 1) - cgS) & m4);
  const int rS = (q4 & 1) * 4 + e8, rB = rS + ((2 * q4 + (e8 & 1) - rS) & m4);
  const int cg = cgA + ((cgB - cgA) & m3), rt = 8 * rh + e8 + ((rB - e8) & m3);
  const int r64 = 4 * pan + (idx >> 3), ct = idx & 7;
  const int ks0 = 22 * wj - ((wj + 1) >> 2);
  u32x4 xo; u32x2 st2;
  unsigned elo, ehi;
#define VD() (unsigned)((16 * ks0 + l3) * 1024 + ct * 128 + ((l7 ^ (((l3 >> 1) & 1) << 2)) << 4))
  const int rot2 = ROT2;
#define PH(j) ((j) + rot2 >= 22 ? (j) + rot2 - 22 : (j) + rot2)
  const int klast = 21 - (wj >> 1);
#define WDU(j, lds) do { const unsigned v_ = vd | (PH(j) > klast ? OOB : 0u);                                       \
    dma16(rD, v_, PH(j) * 16384, L0 + (lds)); dma16(rD, v_, PH(j) * 16384 + 8192, L0 + (lds) + 1024); } while (0)
#define VXO() (unsigned)((64 * r64 + 8 * w + l3) * 1024 + ct * 128 + l7 * 16)
#define VS2() (WS_ST + (unsigned)((64 * r64 + 8 * w + l3) * 8))

  bf16x8 wpk[8][2];
#define AD() (L0 + (8 * hh + qq) * 128 + ((4 * ((qq >> 1) & 1) + 2 * (G & 1) + (pp >> 1)) << 4) + 8 * (pp & 1))
#define WDF(base, dt) cat(trd(((base) ^ ((dt) * 64))), trd(((base) ^ ((dt) * 64)) + 512))
#define UA(j) ((j) < 4 ? a1 + (j) * 2048 : (j) < 9 ? a2 + ((j) - 4) * 2048 : (j) < 14 ? a2 + SLOT + ((j) - 9) * 2048 : (j) < 18 ? a1 + ((j) - 14) * 2048 : a2 + ((j) - 18) * 2048)
#define PARK() do { const unsigned a1 = AD() + L_B1 + wj * 8192, a2 = AD() + L_B2 + wj * 10240;                         \
    _Pragma("unroll") for (int j = 0; j < 8; ++j) { wpk[j][0] = WDF(UA(j), 0); wpk[j][1] = WDF(UA(j), 1); }              \
    _Pragma("unroll") for (int j = 0; j < 8; ++j) { PIN(wpk[j][0]); PIN(wpk[j][1]); } } while (0)
  f32x2 xn[4];
#define XN() do { st2 = spin_tag(rW, VS2(), st2, elo); const float mean_ = blo(st2.x), rs_ = __builtin_amdgcn_rcpf(bhi(st2.x));       \
    _Pragma("unroll") for (int i = 0; i < 4; ++i) { float a_ = (blo(xo[i]) - mean_) * rs_, b_ = (bhi(xo[i]) - mean_) * rs_; PIN(a_); PIN(b_); xn[i] = f32x2{a_, b_}; } } while (0)
#define LDS_SET(a, v) do { asm volatile("" ::: "memory"); *(volatile LAS unsigned*)(size_t)(a) = (v); } while (0)

  const unsigned vla = (unsigned)((128 * rt + 8 * wj + l3) * 1024 + ((l7 ^ ((4 * wj + (l3 >> 1)) & 7)) << 4)) | ((unsigned)((14 - cg) >> 31) & OOB);
  const unsigned vca = (unsigned)((8 * aa + 2 * wj) * 1024 + lane * 16);
  unsigned v1st = vca ^ ((vla ^ vca) & (unsigned)((3 - w) >> 31));
  asm volatile("" : "+v"(v1st) : "s"(cg), "s"(rt), "s"(aa), "s"(r64), "s"(ks0), "s"(rot2), "s"(klast), "v"(l3), "v"(l7), "v"(G), "v"(qq), "v"(pp), "v"(hh), "v"(l31));
  SB();
  asm volatile("" ::"s"(x), "s"(kin), "s"(vin), "s"(shk), "s"(ssk), "s"(ssd), "s"(wg), "s"(wu), "s"(wd), "s"(out), "s"(ws));
  const rsrc_t rX = mk_rsrc(x, S_ * D_ * 2), rW = mk_rsrc(ws, WS_BYTES), rD = mk_rsrc(wd, DF * D_ * 2);
  if (w >= 4) {
    __builtin_amdgcn_s_setprio(2);
    const unsigned vx = v1st;
    const int rot1 = ROT1;
#pragma unroll
    for (int n = 0; n < 4; ++n) dma16(rX, vx, rot1 * 128 + n * 32768, L0 + wj * 1024 + n * 4096);
    unsigned vg[3], vu[3];
    const rsrc_t rG = mk_rsrc(wg, D_ * DF * 2), rU = mk_rsrc(wu, D_ * DF * 2);
#pragma unroll
    for (int r = 0; r < 3; ++r) {
      const unsigned P = 64 * r + lane, k = (P * 43691u) >> 19, col = 96 * cg + 8 * (P - 12 * k);
      vg[r] = (col < DF) ? (16 * wj + k) * (DF * 2) + col * 2 : OOB; vu[r] = vg[r] ^ 32u;
      dma16(rG, vg[r], rot1 * (64 * DF * 2), L0 + 16384 + wj * 3072 + r * 1024);
      dma16(rU, vu[r], rot1 * (64 * DF * 2), L0 + 16384 + 12288 + wj * 3072 + r * 1024);
      if (r == S0POS && S0POS < 3) asm volatile("s_barrier" ::: "memory");
    }
    u32x2 gen = ld8_dev(rW, WS_GEN);
    unsigned dpv;
    asm volatile("buffer_load_dword %0, %1, %2, 0 offen" : "=v"(dpv) : "v"(aa * 4), "s"(mk_rsrc(ssd, D_ * 2)) : "memory");
#pragma unroll
    for (int i = 0; i < 2; ++i) st16_dev(rW, WS_ZT + (unsigned)(2 * aa * 4096 + (2 * wj + i) * 1024 + lane * 16), u32x4{POISON, POISON, POISON, POISON});
#pragma nounroll
    for (int c = 1; c < 8; ++c) {
      const int cc = (c + rot1) & 7;
      const unsigned sx = cc * 128, sw = cc * (64 * DF * 2), lx = L0 + (c & 3) * SLOT + wj * 1024, lw = L0 + (c & 3) * SLOT + 16384 + wj * 3072;
      dma16(rX, vx, sx, lx);                 dma16(rG, vg[0], sw, lw);        dma16(rU, vu[0], sw, lw + 12288);
      dma16(rX, vx, sx + 32768, lx + 4096);  dma16(rG, vg[1], sw, lw + 1024); dma16(rU, vu[1], sw, lw + 13312);
      if (S0POS == 3 && c == 1) asm volatile("s_barrier" ::: "memory");
      if (c >= 2) asm volatile("s_waitcnt vmcnt(16)\n\ts_barrier" ::: "memory");
      if (c == 2) STAMP(16);
      if (c == 3) STAMP(18);
      if (c == 7) STAMP(23);
      if (c == 5 && tid == 256) {
        unsigned lo_, hi_;
        asm volatile("v_add_co_u32 %0, vcc, 1, %2\n\ts_nop 1\n\tv_addc_co_u32 %1, vcc, 0, %3, vcc" : "=&v"(lo_), "=&v"(hi_) : "v"(gen.x), "v"(gen.y) : "vcc");
        st8_dev(rW, WS_FA + (unsigned)(aa * 128), u32x2{lo_, hi_});
      }
      dma16(rX, vx, sx + 65536, lx + 8192);  dma16(rG, vg[2], sw, lw + 2048); dma16(rU, vu[2], sw, lw + 14336);
      dma16(rX, vx, sx + 98304, lx + 12288);
    }
    PIN(gen); PIN(dpv);
    elo = __builtin_amdgcn_readfirstlane(gen.x) + 1u;
    ehi = __builtin_amdgcn_readfirstlane(gen.y) + (elo == 0u ? 1u : 0u);
    const unsigned dpair = __builtin_amdgcn_readfirstlane(dpv);
    const int ce = tid & 255;
    const unsigned vd = VD();
    const unsigned vfa = WS_FA + (unsigned)((32 * (hh ? ct : pan) + l31) * 128), vst = WS_ST + (unsigned)((128 * rt + 64 * (wj & 1) + lane) * 8);
    const u32x4 zero4 = {0u, 0u, 0u, 0u};
    u32x2 stg = ld8_dev(rW, vst), fa = ld8_dev(rW, vfa);
#pragma unroll
    for (int j = 0; j < 4; ++j) WDU(j, L_B1 + wj * 8192 + j * 2048);
    if (ce < 7) lds_st<u32x4>(L0 + L_SY + ce * 16, zero4);
    WAIT_BAR(20);
#pragma unroll
    for (int j = 0; j < 5; ++j) WDU(4 + j, L_B2 + wj * 10240 + j * 2048);
    asm volatile("s_waitcnt vmcnt(18)\n\ts_barrier" : "+v"(stg), "+v"(fa)::"memory");
    STAMP(4);
    stg = spin_tag(rW, vst, stg, elo);
    if (wj < 2) lds_st<unsigned>(L0 + L_ST + (64 * wj + lane) * 4, stg.x);
    spin_flags(rW, vfa, fa, elo, ehi);
    u32x4 hc[2], hp[2], ur[2];
    {
      const rsrc_t rHk = mk_rsrc(ssk, D_ * S_ * 2);
#pragma unroll
      for (int c = 0; c < 2; ++c) {
        hc[c] = ld16(rHk, (unsigned)((2 * aa + c) * 4096 + ce * 16));
        hp[c] = ld16(rHk, ce ? (unsigned)((2 * aa + c) * 4096 + ce * 16 - 16) : OOB);
        ur[c] = ld16_dev(rW, WS_KVT + (unsigned)(2 * aa * 4096 + ce * 16), c * 4096);
      }
    }
    xo = ld16(rX, VXO()); st2 = ld8_dev(rW, VS2());
#pragma unroll
    for (int j = 0; j < 2; ++j) WDU(9 + j, L_B3 + wj * 10240 + j * 2048);
    WAIT_BAR(12);
    STAMP(5);
#pragma unroll
    for (int j = 2; j < 5; ++j) WDU(9 + j, L_B3 + wj * 10240 + j * 2048);
    __builtin_amdgcn_s_setprio(SETUPPRIO);
    {
      asm volatile("s_waitcnt vmcnt(10)" : "+v"(hc[0]), "+v"(hc[1]), "+v"(hp[0]), "+v"(hp[1]), "+v"(ur[0]), "+v"(ur[1]), "+v"(xo), "+v"(st2)::"memory");
      auto put = [&](unsigned hb, u32x4 pv, u32x4 cu, int blk) {
        const unsigned a0 = __builtin_amdgcn_alignbit(pv[1], pv[0], 16), a1 = __builtin_amdgcn_alignbit(pv[2], pv[1], 16),
                       a2 = __builtin_amdgcn_alignbit(pv[3], pv[2], 16), a3 = __builtin_amdgcn_alignbit(cu[0], pv[3], 16),
                       a4 = __builtin_amdgcn_alignbit(cu[1], cu[0], 16);
        lds_st<u32x4>(hb + blk * 16, u32x4{a0, a1, a2, a3});
        lds_st<u32x4>(hb + 4160 + blk * 16, u32x4{pv[1], pv[2], pv[3], cu[0]});
        lds_st<u32x4>(hb + 8320 + blk * 16, u32x4{a1, a2, a3, a4});
        lds_st<u32x4>(hb + 12480 + blk * 16, u32x4{pv[2], pv[3], cu[0], cu[1]});
      };
#pragma unroll
      for (int c = 0; c < 2; ++c) {
        put(L0 + L_CV + c * 16640, hp[c], hc[c], ce + 1);
        if (ce == 0 || ce == 255) put(L0 + L_CV + c * 16640, ce ? hc[c] : zero4, zero4, ce ? 257 : 0);
        lds_st<u32x4>(L0 + (c ? L_U1 : L_U0) + ce * 16, ur[c]);
      }
      asm volatile("s_waitcnt lgkmcnt(0)" ::: "memory");
      if (lane == 0) lds_inc(L0 + L_SY + 24);
    }
    PARK();
    LDS_SET(L0 + L_SY + 64 + wj * 4, 1u);
    XN();
    if (b == 0 && wj == 0) {
      u32x2 f[4];
#pragma unroll
      for (int i = 0; i < 4; ++i) f[i] = ld8_dev(rW, WS_FA + (unsigned)((64 * i + lane) * 128));
      asm volatile("s_waitcnt vmcnt(0)" : "+v"(f[0]), "+v"(f[1]), "+v"(f[2]), "+v"(f[3])::"memory");
#pragma unroll
      for (int i = 0; i < 4; ++i) spin_flags(rW, WS_FA + (unsigned)((64 * i + lane) * 128), f[i], elo, ehi);
      if (lane == 0) { u32x2 e = {elo, ehi}; st8_dev(rW, WS_GEN, e); }
    }
    STAMP(7);
    const int vch = wj >> 1, vrh = wj & 1, c15 = lane & 15;
    f32x4 ya[8];
#pragma unroll
    for (int c = 0; c < 8; ++c) { ya[c] = f32x4{0.f, 0.f, 0.f, 0.f}; PIN(ya[c]); }
    unsigned xo_ = L0 + L_CV + vch * 16640 + (vrh ^ 1) * 4096 + lane * 16, xb = L0 + L_CV + vch * 16640 + vrh * 4096 + lane * 16;
    unsigned zs_ = L0 + L_CV + vch * 16640 + vrh * 4096 + c15 * 32 + G * 8, uq_ = L0 + (vch ? L_U1 : L_U0) + (2044 - 768 - 1024 * vrh - 16 * c15 - 4 * G) * 2;
    unsigned vzs = WS_ZT + (unsigned)((2 * aa + vch) * 4096 + vrh * 2048 + lane * 16);
    PIN(xo_); PIN(xb); PIN(zs_); PIN(uq_); PIN(vzs);
    {
      unsigned cA = L0 + L_CV + vch * 16640 + (c15 & 3) * 4160 + (4 * (c15 >> 2) + 8 * G + 128 * vrh) * 2;
      const unsigned ub = L0 + (vch ? L_U1 : L_U0);
      unsigned cB = ub + (240 - 16 * c15 + 8 * G + 128 * vrh) * 2;
      int lim = ((16 * c15 - 8 * G + 47) >> 5) - 4 * vrh;
      PIN(cA); PIN(cB);
      bf16x8 bfr[2][8]; s16x4 ah[2][4];
#define VLD8(a) (*(const volatile LAS s16x4*)(size_t)(a))
#define CA(set, r, p, i) ah[set][i] = VLD8(cA + (r) * 64 + (((i) & 2) ? 7 - (p) : (p)) * 512 + ((i) & 1) * 8)
#define CB(r, mu) bfr[(r) & 1][mu] = lds_ld<bf16x8>((mu) == 0 ? ((r) >= lim ? L0 + L_SY + 32 : cB + (r) * 64 + 3584) : cB + (r) * 64 + (7 - (mu)) * 512)
#define MF16(c, a, b) asm volatile("v_mfma_f32_16x16x32_bf16 %0, %1, %2, %0" : "+v"(c) : "v"(a), "v"(b))
#define CM(r, p, m) do { if ((m) + (p) < 8) MF16(ya[((p) + (m)) & 7], cat(ah[(p) & 1][0], ah[(p) & 1][1]), bfr[(r) & 1][(m) & 7]);  \
        else MF16(ya[((m) - 1) & 7], cat(ah[(p) & 1][2], ah[(p) & 1][3]), bfr[(r) & 1][((m) + (p)) & 7]); } while (0)
#define CKEEP(r, p) asm volatile("" ::"v"(ah[(p) & 1][0]), "v"(ah[(p) & 1][1]), "v"(ah[(p) & 1][2]), "v"(ah[(p) & 1][3]), "v"(bfr[(r) & 1][0]), "v"(bfr[(r) & 1][1]), \
        "v"(bfr[(r) & 1][2]), "v"(bfr[(r) & 1][3]), "v"(bfr[(r) & 1][4]), "v"(bfr[(r) & 1][5]), "v"(bfr[(r) & 1][6]), "v"(bfr[(r) & 1][7]))
#define CPAIR(r, p) do { _Pragma("unroll") for (int m = 0; m < 9; ++m) { CM(r, p, m);                                               \
          if (m < 4) CA(((p) + 1) & 1, (p) < 3 ? (r) : (r) + 1, ((p) + 1) & 3, m); else if (m < 6) CB((r) + 1, 2 * (p) + m - 4); SB(); } CKEEP(r, p); } while (0)
#define CONV(r) do { CPAIR(r, 0); CPAIR(r, 1); CPAIR(r, 2); CPAIR(r, 3); } while (0)
      lds_wait_ge(L0 + L_SY + 24, 8u); __builtin_amdgcn_s_setprio(2);
      STAMP(24);
#pragma unroll
      for (int i = 0; i < 4; ++i) CA(0, 0, 0, i);
#pragma unroll
      for (int mu = 0; mu < 8; ++mu) CB(0, mu);
#pragma nounroll
      for (int it = 0; it < 2; ++it) {
        CONV(0); CPAIR(1, 0); CPAIR(1, 1); CPAIR(1, 2);
        if (it) {
          asm volatile("s_waitcnt vmcnt(0) lgkmcnt(0)" ::: "memory"); LDS_SET(L0 + L_SY + 48 + wj * 4, 1u); LDS_SET(L0 + L_SY + wj * 4, 1u); }
        CPAIR(1, 3);
        cA += 128; cB += 128; lim -= 2; PIN(cA); PIN(cB);
      }
    }
    asm volatile("s_nop 7" ::: "memory");
    STAMP(27);
    {
      const unsigned mine = L0 + L_SY + 48 + wj * 4, other = L0 + L_SY + 48 + (wj ^ 1) * 4;
      const float dsc = vch ? bhi(dpair) : blo(dpair);
      u32x2 uq[4];
#pragma unroll
      for (int c = 0; c < 4; ++c) uq[c] = lds_ld<u32x2>(uq_ + (768 - 256 * c) * 2);
      lds_spin_ge(other, 1u);
      f32x4 y[4];
      if (vrh) {
        asm volatile("" ::: "memory");
#pragma unroll
        for (int c = 0; c < 4; ++c) { lds_st<f32x4>(xo_ + c * 1024, ya[c]); y[c] = ya[4 + c]; PIN(y[c]); }
      } else {
#pragma unroll
        for (int c = 0; c < 4; ++c) { lds_st<f32x4>(xo_ + c * 1024, ya[4 + c]); y[c] = ya[c]; PIN(y[c]); }
        asm volatile("" ::: "memory");
      }
      asm volatile("s_waitcnt lgkmcnt(0)" ::: "memory");
      *(volatile LAS unsigned*)(size_t)mine = 2u;
#pragma unroll
      for (int c = 0; c < 4; ++c)
#pragma unroll
        for (int e = 0; e < 4; ++e) {
          const unsigned uu = (e & 2) ? uq[c].x : uq[c].y;
          float s = __builtin_fmaf((e & 1) ? blo(uu) : bhi(uu), dsc, y[c][e]); PIN(s); y[c][e] = s;
        }
      lds_spin_ge(other, 2u);
      f32x4 o[4];
#pragma unroll
      for (int c = 0; c < 4; ++c) o[c] = lds_ld<f32x4>(xb + c * 1024);
      SB();
      u32x2 zp[4];
#pragma unroll
      for (int c = 0; c < 4; ++c) {
        float z[4];
#pragma unroll
        for (int e = 0; e < 4; ++e) { z[e] = y[c][e] + o[c][e]; asm("" : "+v"(z[e])); }
        zp[c] = u32x2{min(cvtpk(z[0], z[1]), POISON - 1u), min(cvtpk(z[2], z[3]), POISON - 1u)};
      }
#pragma unroll
      for (int c = 0; c < 4; ++c) lds_st<u32x2>(zs_ + c * 512, zp[c]);
#pragma unroll
      for (int i = 0; i < 2; ++i)
        st16_dev(rW, vzs + i * 1024, lds_ld<u32x4>(xb + i * 1024));
    }
    STAMP(26);
    if (lane == 0) lds_inc(L0 + L_SY + 20);
    STAMP(9);
    lds_spin_ge(L0 + L_SY + 80 + wj * 4, 1u);
    STAMP(31);
  } else {
    __builtin_amdgcn_s_setprio(3);
    u32x2 sh[2]; u32x4 xr[2];
#pragma unroll
    for (int i = 0; i < 2; ++i) xr[i] = ld16(rX, v1st + i * 1024);
    {
      const rsrc_t rK = mk_rsrc(kin, S_ * D_ * 2), rV = mk_rsrc(vin, S_ * D_ * 2);
      const unsigned kb = (unsigned)((64 * idx + l3) * 1024 + pan * 128), fl = (unsigned)(l7 ^ ((l3 >> 1) & 1));
#pragma unroll
      for (int i = 0; i < 2; ++i) {
        const int g = 2 * wj + i;
        dma16(rK, kb + ((fl ^ (((g + 1) & 3) << 1)) << 4), g * 8192, L0 + L_KA + (g + 1) * 1024);
        dma16(rV, kb + ((fl ^ ((g & 3) << 1)) << 4), g * 8192, L0 + L_VA + g * 1024);
      }
      if (wj == 0) dma16(rK, idx == 0 ? OOB : kb - 8192 + (fl << 4), 0, L0 + L_KA);
    }
    u32x2 gen = ld8_dev(rW, WS_GEN);
    {
      const rsrc_t rSh = mk_rsrc(shk, D_ * 4 * 2);
#pragma unroll
      for (int i = 0; i < 2; ++i) sh[i] = ld8(rSh, (unsigned)((64 * pan + 16 * wj + 8 * i + 4 * (G & 1) + pp) * 8));
    }
    STAMP(3);
    const int tp = wj & 1, ch = wj >> 1;
    f32x16 acc[2][3];
    f32x4 cs[3];
    unsigned ax[4], aw[2][3], ak[5];
    unsigned vkv = WS_KVT + (unsigned)((64 * pan + 16 * wj + l3) * 4096 + (1984 - 64 * idx + 8 * l7) * 2);
    unsigned vst0 = lane < 2 ? WS_ST + (unsigned)((8 * aa + 2 * wj + lane) * 8) : OOB;
    int src4 = 4 * (16 * ((((7 - l7) >> 2) << 1) | (l3 >> 2)) + 4 * ((7 - l7) & 3) + (l3 & 3));
    const unsigned so_ = ((lane & 15) == 4 * (G & 1)) ? 0x3f803f80u : 0u;
    bf16x8 sel = __builtin_bit_cast(bf16x8, u32x4{so_, so_, so_, so_});
    {
      const unsigned ax0 = L0 + (64 * tp + l31) * 128 + (((4 * tp + hh) ^ ((l31 >> 1) & 7)) << 4);
      const unsigned aw0 = L0 + 16384 + (G & 1) * 12288 + (8 * hh + qq) * 192 + ((pp & 1) << 4) + 8 * (pp >> 1);
#pragma unroll
      for (int s = 0; s < 4; ++s) { ax[s] = ax0 ^ (s << 5); PIN(ax[s]); }
#pragma unroll
      for (int i = 0; i < 3; ++i) {
        const unsigned a_ = aw0 + (((3 * ch + i) ^ (G & 1)) << 5);
        aw[0][i] = a_ + tp * 6144; aw[1][i] = a_ + 6144 - tp * 6144; PIN(aw[0][i]); PIN(aw[1][i]);
      }
#pragma unroll
      for (int j = 0; j < 5; ++j) { const int rr = 8 * (4 * (G >> 1) + pp) + qq + (j < 3 ? 4 + 4 * j : 4 * (j - 3));
        ak[j] = L0 + (j < 3 ? L_KA : L_VA) + rr * 128 + (((2 * wj) ^ fsw(rr)) << 4) + 8 * (G & 1); PIN(ak[j]); }
      PIN(vkv); PIN(vst0); PIN(src4); PIN(sel);
    }
    {
      asm volatile("s_waitcnt vmcnt(7)" : "+v"(xr[0]), "+v"(xr[1])::"memory");
    asm volatile("s_barrier" ::: "memory");
      STAMP(14);
      const f32x2 x0 = {blo(__builtin_amdgcn_readfirstlane(xr[0][0])), blo(__builtin_amdgcn_readfirstlane(xr[1][0]))};
      f32x2 d[8];
#pragma unroll
      for (int e = 0; e < 4; ++e) { d[2 * e] = f32x2{blo(xr[0][e]), blo(xr[1][e])} - x0; d[2 * e + 1] = f32x2{bhi(xr[0][e]), bhi(xr[1][e])} - x0; }
      const f32x2 sm = ((d[0] + d[1]) + (d[2] + d[3])) + ((d[4] + d[5]) + (d[6] + d[7]));
      const f32x2 sq = ((d[0] * d[0] + d[1] * d[1]) + (d[2] * d[2] + d[3] * d[3])) + ((d[4] * d[4] + d[5] * d[5]) + (d[6] * d[6] + d[7] * d[7]));
      float s0 = sm[0], s1 = sm[1], q0 = sq[0], q1 = sq[1];
      wave_sum4(s0, s1, q0, q1);
      const float m = (lane ? s1 : s0) * (1.0f / D_), var = (lane ? q1 : q0) * (1.0f / D_) - m * m;
      unsigned ms0 = cvtpk(m + (lane ? x0[1] : x0[0]), __builtin_amdgcn_sqrtf(rbf(rbf(__builtin_fmaxf(var, 0.f)) + 1e-5f)));
      asm volatile("s_waitcnt vmcnt(0)" : "+v"(gen), "+v"(sh[0]), "+v"(sh[1]), "+v"(ms0)::"memory");
      STAMP(15);
      elo = __builtin_amdgcn_readfirstlane(gen.x) + 1u;
      ehi = __builtin_amdgcn_readfirstlane(gen.y) + (elo == 0u ? 1u : 0u);
      *(volatile LAS u32x2*)(size_t)(L0 + L_A + wj * 8) = u32x2{elo, ehi};
      st8_dev(rW, vst0, u32x2{ms0, elo});
      f32x2 sk[4] = {{blo(sh[0].x), blo(sh[1].x)}, {bhi(sh[0].x), bhi(sh[1].x)}, {blo(sh[0].y), blo(sh[1].y)}, {bhi(sh[0].y), bhi(sh[1].y)}};
      PIN(sk[0]); PIN(sk[1]); PIN(sk[2]); PIN(sk[3]);
      for (unsigned n = 0; n < SPIN_MAX * 64; ++n) {
        const u32x2 f = *(volatile LAS u32x2*)(size_t)(L0 + L_A + pp * 8);
        if (__all(f.x == elo && f.y == ehi)) break;
      }
      asm volatile("" ::: "memory");
      STAMP(1);
      u32x2 t[2][5];
#pragma unroll
      for (int j = 0; j < 5; ++j) { t[0][j] = __builtin_bit_cast(u32x2, trd(ak[j])); t[1][j] = __builtin_bit_cast(u32x2, trd(ak[j] ^ 16)); }
      SB();
      f32x2 kk[20], r[8];
#pragma unroll
      for (int j = 0; j < 5; ++j) {
        kk[4 * j] = f32x2{blo(t[0][j].x), blo(t[1][j].x)}; kk[4 * j + 1] = f32x2{bhi(t[0][j].x), bhi(t[1][j].x)};
        kk[4 * j + 2] = f32x2{blo(t[0][j].y), blo(t[1][j].y)}; kk[4 * j + 3] = f32x2{bhi(t[0][j].y), bhi(t[1][j].y)};
      }
#pragma unroll
      for (int e = 0; e < 8; ++e) r[e] = kk[e + 1] * sk[3];
#pragma unroll
      for (int j = 2; j >= 0; --j)
#pragma unroll
        for (int e = 0; e < 8; ++e) r[e] = __builtin_elementwise_fma(kk[e + 4 - j], sk[j], r[e]);
#pragma unroll
      for (int e = 0; e < 8; ++e) r[e] *= kk[12 + e];
      u32x4 po[2];
#pragma unroll
      for (int i = 0; i < 2; ++i)
#pragma unroll
        for (int e = 0; e < 4; ++e) po[i][e] = (unsigned)__builtin_amdgcn_ds_bpermute(src4, (int)cvtpk(r[7 - 2 * e][i], r[6 - 2 * e][i]));
      st16_dev(rW, vkv, po[0]); st16_dev(rW, vkv + 32768, po[1]);
      __builtin_amdgcn_s_setprio(1);
    }
    STAMP(2);
    {
      bf16x8 xf[2][2]; s16x4 wf[2][3][2];
#define PINA(t, i) PIN(acc[t][i])
#define M1(p, t, i) do { acc[t][i] = MFMA32(cat(wf[p][i][0], wf[p][i][1]), xf[p][t], acc[t][i]); PINA(t, i); } while (0)
#define MS(p, i, on) do { if ((on) && !NOMS) { cs[i] = MFMA16(sel, cat(wf[p][i][0], wf[p][i][1]), cs[i]); PIN(cs[i]); } } while (0)
#define RW(s, i) do { wf[(s) & 1][i][0] = trd(aw[(s) >> 1][i] + ((s) & 1) * 3072); wf[(s) & 1][i][1] = trd(aw[(s) >> 1][i] + ((s) & 1) * 3072 + 768); } while (0)
#define RX(s, t) xf[(s) & 1][t] = lds_ld<bf16x8>(ax[s] + (t) * 4096)
#define KEEP(p) do { asm volatile("" ::"v"(wf[p][0][0]), "v"(wf[p][0][1]), "v"(wf[p][1][0]), "v"(wf[p][1][1]), "v"(wf[p][2][0]), "v"(wf[p][2][1]), "v"(xf[p][0]), "v"(xf[p][1])); } while (0)
#define ADV(a) do { a += dl; PIN(a); } while (0)
#define STEP(p, s, on) do { M1(p, 0, 0); RW(s, 0); SB(); M1(p, 1, 0); RX(s, 0); RX(s, 1); SB(); M1(p, 0, 1); RW(s, 1); SB();  \
        M1(p, 1, 1); RW(s, 2); SB(); M1(p, 0, 2);                                                                             \
        if ((s) == 3) { ADV(ax[0]); ADV(ax[1]); ADV(ax[2]); ADV(ax[3]); ADV(aw[0][0]); } SB(); M1(p, 1, 2);                     \
        if ((s) == 3) { ADV(aw[0][1]); ADV(aw[0][2]); ADV(aw[1][0]); ADV(aw[1][1]); ADV(aw[1][2]); }                            \
        MS(p, 0, on); MS(p, 1, on); MS(p, 2, on); KEEP(p); SB(); } while (0)
      LBAR();
      STAMP(16);
      RW(0, 0); RX(0, 0); RX(0, 1); RW(0, 1); RW(0, 2); SB();
      unsigned dl = SLOT;
#pragma unroll
      for (int i = 0; i < 3; ++i) {
        cs[i] = f32x4{0.f, 0.f, 0.f, 0.f};
#pragma unroll
        for (int r = 0; r < 16; ++r) { acc[0][i][r] = 0.f; acc[1][i][r] = 0.f; }
      }
      STEP(0, 1, true);
#pragma nounroll
      for (int c = 0; c < 7; ++c) {
        dl = (c == 3) ? 0u - 3u * SLOT : (unsigned)SLOT;
        STEP(1, 2, true);
        if (c == 2) WAITV(0);
        STEP(0, 3, false);
        LBAR();
        STEP(1, 0, false); STEP(0, 1, true);
      }
      STAMP(4);
      STEP(1, 2, true);
#pragma unroll
      for (int i = 0; i < 3; ++i) lds_st<float>(L0 + L_CS + (wj * 3 + i) * 256 + lane * 4, cs[i][0]);
      STEP(0, 3, false);
      LBAR();
      float co[3]; unsigned ms[2];
#pragma unroll
      for (int i = 0; i < 3; ++i) co[i] = lds_ld<float>(L0 + L_CS + ((wj ^ 1) * 3 + i) * 256 + lane * 4);
#pragma unroll
      for (int t = 0; t < 2; ++t) ms[t] = lds_ld<unsigned>(L0 + L_ST + (64 * tp + 32 * t + l31) * 4);
      SB();
      M1(1, 0, 0); M1(1, 1, 0); M1(1, 0, 1); M1(1, 1, 1); M1(1, 0, 2); M1(1, 1, 2); SB();
      float k0[2], k1[2], s2[2];
      u32x4 bm[2];
#pragma unroll
      for (int t = 0; t < 2; ++t) {
        const unsigned mn = (ms[t] & 0xffffu) ^ 0x8000u;
        bm[t] = u32x4{mn | (mn << 16), mn, 0u, 0u};
        const float sd = bhi(ms[t]), rs = __builtin_amdgcn_rcpf(sd);
        k0[t] = rs * -2.3022081983f; k1[t] = k0[t] * rs * rs * 0.044715f; s2[t] = sd * sd;
      }
#pragma unroll
      for (int i = 0; i < 3; ++i) {
        const float c = cs[i][0] + co[i], c1 = c - rbf(c), c2 = c1 - rbf(c1);
        const u32x4 am = {cvtpk(c, c1), cvtpk(c2, 0.f), 0u, 0u};
#pragma unroll
        for (int t = 0; t < 2; ++t) { acc[t][i] = MFMA32(__builtin_bit_cast(bf16x8, am), __builtin_bit_cast(bf16x8, bm[t]), acc[t][i]); PINA(t, i); }
      }
      STAMP(5);
      const unsigned vhs = WS_H + (unsigned)(((4 * rt + 2 * tp) * 86 + 6 * cg + 3 * ch) * 1024 + lane * 16);
#pragma unroll
      for (int i = 0; i < 3; ++i) {
        const unsigned vo = (vhs + i * 1024) | (6 * cg + 3 * ch + i < 86 ? 0u : OOB);
#pragma unroll
        for (int t = 0; t < 2; ++t) {
          f32x2 g[4], zz[4], gu[4];
#pragma unroll
          for (int r = 0; r < 4; ++r) {
            g[r] = f32x2{acc[t][i][2 * r], acc[t][i][2 * r + 1]};
            zz[r] = g[r] * __builtin_elementwise_fma(g[r] * g[r], f32x2{k1[t], k1[t]}, f32x2{k0[t], k0[t]});
          }
#pragma unroll
          for (int r = 0; r < 4; ++r) {
            zz[r] = f32x2{__builtin_amdgcn_exp2f(zz[r][0]), __builtin_amdgcn_exp2f(zz[r][1])};
            gu[r] = g[r] * f32x2{acc[t][i][8 + 2 * r], acc[t][i][9 + 2 * r]};
          }
          u32x4 hq;
#pragma unroll
          for (int r = 0; r < 4; ++r) {
            const f32x2 dd = __builtin_elementwise_fma(zz[r], f32x2{s2[t], s2[t]}, f32x2{s2[t], s2[t]});
            const f32x2 hv = gu[r] * f32x2{__builtin_amdgcn_rcpf(dd[0]), __builtin_amdgcn_rcpf(dd[1])};
            hq[r] = cvtpk(hv[0], hv[1]);
          }
          st16_dev(rW, vo + t * (86 * 1024), hq);
        }
      }
    }
    if (lane == 0) lds_inc(L0 + L_SY + 24);
    STAMP(6);
    const unsigned vd = VD();
    PARK();
    WAITV(0);
    if (lane == 0 && lds_inc(L0 + L_SY + 16) == 3u && cg < 15) { u32x2 e = {elo, ehi}; st8_dev(rW, WS_FH + (unsigned)((rt * 16 + cg) * 128), e); }
    STAMP(8);
    lds_wait_ge(L0 + L_SY + 64 + wj * 4, 1u);
#pragma unroll
    for (int j = 0; j < 4; ++j) WDU(14 + j, L_B1 + wj * 8192 + j * 2048);
#pragma unroll
    for (int j = 0; j < 4; ++j) WDU(18 + j, L_B2 + wj * 10240 + j * 2048);
    xo = ld16(rX, VXO()); st2 = ld8_dev(rW, VS2());
    {
      const int gf = ks0 / 6, gl = (ks0 + 20 + (wj < 2)) / 6, gi = min(gf + l7, gl);
      const unsigned vfh = WS_FH + (unsigned)(((r64 >> 1) * 16 + gi) * 128);
      u32x2 fh = ld8_dev(rW, vfh);
      asm volatile("s_waitcnt vmcnt(0)" : "+v"(fh), "+v"(xo), "+v"(st2)::"memory");
      if (!__all(fh.x == elo && fh.y == ehi)) fh = ld8_dev(rW, vfh);
      XN();
      asm volatile("s_waitcnt vmcnt(0)" : "+v"(fh)::"memory");
      spin_flags(rW, vfh, fh, elo, ehi);
    }
    LDS_SET(L0 + L_SY + 80 + wj * 4, 1u);
  }
  STAMP(10);
  {
    u32x4 hf[22];
    const unsigned vh = WS_H + (unsigned)(((2 * r64 + (w >> 2)) * 86 + ks0) * 1024 + lane * 16);
#define HL(j) hf[j] = ld16_dev(rW, vh, __builtin_amdgcn_readfirstlane(min(PH(j), klast) * 1024))
#pragma unroll
    for (int j = 0; j < HWIN; ++j) HL(j);
    const unsigned vz = WS_ZT + (unsigned)((64 * ct + 8 * w + l3) * 4096 + r64 * 128 + l7 * 16);
    u32x4 zr;
    STAMP(28);
    const unsigned pq = w < 4 ? L0 + L_P0 : L0 + L_B3, ps = w < 4 ? 8192 : 10240;
    unsigned pw[4], pr[4], zw, zt_, zsrc, vo;
#pragma unroll
    for (int i = 0; i < 4; ++i) {
      pw[i] = pq + wj * ps + l31 * 256 + (((hh ^ l7) ^ (2 * i)) << 4); PIN(pw[i]);
      pr[i] = pq + i * ps + (8 * wj + l3) * 256 + (((2 * l7) ^ l3) << 4); PIN(pr[i]);
    }
    { const int r = 8 * w + l3, r2 = 8 * (4 * (G >> 1) + pp) + qq;
      zw = L0 + L_ZT + r * 128 + ((l7 ^ fsw(r)) << 4); zt_ = L0 + L_ZT + r2 * 128 + ((w ^ fsw(r2)) << 4) + 8 * (G & 1);
      zsrc = 4 * (16 * (((l7 >> 2) << 1) | (l3 >> 2)) + 4 * (l7 & 3) + (l3 & 3)); vo = VXO(); PIN(zw); PIN(zt_); PIN(zsrc); PIN(vo); }
    lds_wait_ge(L0 + L_SY + wj * 4, 1u);
    const unsigned a1 = AD() + L_B1 + wj * 8192, a2 = AD() + L_B2 + wj * 10240;
    f32x16 fo[2];
#pragma unroll
    for (int r = 0; r < 16; ++r) { fo[0][r] = 0.f; fo[1][r] = 0.f; }
#define G2(j) do { if ((j) + 2 >= 8 && (j) + 2 < 22) { wpk[((j) + 2) & 7][0] = WDF(UA((j) + 2), 0); wpk[((j) + 2) & 7][1] = WDF(UA((j) + 2), 1); } \
      asm volatile("s_waitcnt vmcnt(%1)" : "+v"(hf[j]) : "i"((j) + HWIN <= 22 ? HWIN - 1 : (j) <= ZAT ? 21 - (j) : 22 - (j)) : "memory"); \
      fo[0] = MFMA32(wpk[(j) & 7][0], __builtin_bit_cast(bf16x8, hf[j]), fo[0]);                                      \
      fo[1] = MFMA32(wpk[(j) & 7][1], __builtin_bit_cast(bf16x8, hf[j]), fo[1]); PIN(fo[0]); PIN(fo[1]); SB();      \
      if ((j) + HWIN < 22) HL(((j) + HWIN) % 22); if ((j) == ZAT) zr = ld16_dev(rW, vz, 0); } while (0)
    G2(0); STAMP(29); G2(1); G2(2); G2(3); G2(4); G2(5); G2(6); G2(7); G2(8); G2(9); G2(10);
    G2(11); G2(12); G2(13);
    if (w < 4) LDS_SET(L0 + L_SY + 96 + wj * 4, 1u);
    G2(14); G2(15); G2(16); G2(17); G2(18);
    lds_spin_ge(L0 + L_SY + (w < 4 ? 20 : 96 + wj * 4), w < 4 ? 4u : 1u);
    G2(19); G2(20); G2(21);
    STAMP(11);
#pragma unroll
    for (int dt = 0; dt < 2; ++dt)
#pragma unroll
      for (int i = 0; i < 4; ++i)
        lds_st<f32x4>(pw[i] + dt * 128, f32x4{fo[dt][4 * i], fo[dt][4 * i + 1], fo[dt][4 * i + 2], fo[dt][4 * i + 3]});
    asm volatile("s_waitcnt vmcnt(0)" : "+v"(zr)::"memory");
    for (unsigned n = 0; __builtin_expect(__any(max(max(zr[0], zr[1]), max(zr[2], zr[3])) == POISON) && n < SPIN_MAX, 0); ++n) {
      zr = ld16_dev(rW, vz, 0);
      asm volatile("s_waitcnt vmcnt(0)" : "+v"(zr)::"memory");
    }
    STAMP(30);
    if (w >= 4) lds_spin_ge(L0 + L_SY + 20, 4u);
    lds_st<u32x4>(zw, zr);
    LBAR();
    STAMP(12);
    const u32x2 zt0 = __builtin_bit_cast(u32x2, trd(zt_)), zt1 = __builtin_bit_cast(u32x2, trd(zt_ + 512));
    f32x4 pa[4], pb[4];
#pragma unroll
    for (int k = 0; k < 4; ++k) { pa[k] = lds_ld<f32x4>(pr[k]); pb[k] = lds_ld<f32x4>(pr[k] ^ 16); }
    SB();
    const unsigned zq[4] = {(unsigned)__builtin_amdgcn_ds_bpermute(zsrc, (int)zt0.x), (unsigned)__builtin_amdgcn_ds_bpermute(zsrc, (int)zt0.y),
                            (unsigned)__builtin_amdgcn_ds_bpermute(zsrc, (int)zt1.x), (unsigned)__builtin_amdgcn_ds_bpermute(zsrc, (int)zt1.y)};
    const f32x4 p0 = (pa[0] + pa[1]) + (pa[2] + pa[3]), p1 = (pb[0] + pb[1]) + (pb[2] + pb[3]);
    u32x4 ov;
#pragma unroll
    for (int i = 0; i < 4; ++i) {
      const f32x2 F = i < 2 ? f32x2{p0[2 * i], p0[2 * i + 1]} : f32x2{p1[2 * i - 4], p1[2 * i - 3]};
      const f32x2 t = F * f32x2{-1.4426950408889634f, -1.4426950408889634f};
      const f32x2 d = f32x2{__builtin_amdgcn_exp2f(t[0]), __builtin_amdgcn_exp2f(t[1])} + f32x2{1.0f, 1.0f};
      const f32x2 g = F * f32x2{__builtin_amdgcn_rcpf(d[0]), __builtin_amdgcn_rcpf(d[1])};
      const f32x2 o = __builtin_elementwise_fma(xn[i] * f32x2{blo(zq[i]), bhi(zq[i])}, g, f32x2{blo(xo[i]), bhi(xo[i])});
      ov[i] = cvtpk(o[0], o[1]);
    }
    __builtin_amdgcn_raw_buffer_store_b128(ov, mk_rsrc(out, S_ * D_ * 2), vo, 0, OUTAUX);
  }
}

extern "C" void kernel_launch(void* const* d_in, const int* in_sizes, int n_in, void* d_out, int out_size, void* d_ws, size_t ws_size,
                              hipStream_t stream) {
  (void)in_sizes; (void)n_in; (void)out_size;
  (void)hipFuncSetAttribute((const void*)h3_geglu_fused, hipFuncAttributeMaxDynamicSharedMemorySize, LDS_BYTES);
  h3_geglu_fused<<<256, 512, LDS_BYTES, stream>>>((const u16*)d_in[0], (const u16*)d_in[1], (const u16*)d_in[2], (const u16*)d_in[3],
                                                  (const u16*)d_in[4], (const u16*)d_in[5], (const u16*)d_in[7], (const u16*)d_in[8],
                                                  (const u16*)d_in[9], (u16*)d_out, (char*)d_ws + WS_BASE(ws_size));
}
```
